```python
import torch

def _softmax(x, axis=-1):
    return torch.softmax(x, dim=axis)
def _gelu(x):
    return torch.nn.functional.gelu(x, approximate="tanh")
def _silu(x):
    return torch.nn.functional.silu(x)
def _sigmoid(x):
    return torch.sigmoid(x)
def _layernorm(x, axis=-1, eps=1e-5):
    m = x.mean(dim=axis, keepdim=True)
    v = x.var(dim=axis, keepdim=True, unbiased=False)
    return (x - m) / torch.sqrt(v + eps)
def _scatter_add(tgt, idx, src, axis=0):
    return tgt.clone().index_add_(axis, idx, src)
def _segment_sum(src, seg, n):
    out = torch.zeros((n,) + src.shape[1:], dtype=src.dtype)
    return out.index_add_(0, seg, src)
def _pad(x, pw):
    flat = tuple(v for pair in reversed(pw) for v in pair)
    return torch.nn.functional.pad(x, flat)

kernel_name = 'sequential_preLN__sliding_block__glu_gated_reglu__prompt'

def setup_inputs():

    _w = (lambda a: (lambda t: t.to(torch.bfloat16) if t.is_floating_point() else t)(torch.as_tensor(a)))
    def _A_setup():
        B, S, D = 1, 2048, 512
        return {
            "x": _w((lambda *s: torch.randn(*s, dtype=torch.bfloat16) * 0.05)(B, S, D)),
            "wqkv": _w((lambda *s: torch.randn(*s, dtype=torch.bfloat16) * 0.05)(D, 3 * D)),
            "wo": _w((lambda *s: torch.randn(*s, dtype=torch.bfloat16) * 0.05)(D, D)),
        }
    _A_in = _A_setup()

    def _B_setup():
        B, S, D, F = 1, 2048, 512, 1792
        return {
            "x": _w((lambda *s: torch.randn(*s, dtype=torch.bfloat16) * 0.05)(B, S, D)),
            "W_gate": _w((lambda *s: torch.randn(*s, dtype=torch.bfloat16) * 0.8)(D, F) * (2.0 / D) ** 0.5),
            "W_up": _w((lambda *s: torch.randn(*s, dtype=torch.bfloat16) * 0.8)(D, F) * (2.0 / D) ** 0.5),
            "W_down": _w((lambda *s: torch.randn(*s, dtype=torch.bfloat16) * 0.8)(F, D) * (1.0 / F) ** 0.5),
        }
    _B_in = _B_setup()

    return {
        "A_x": _A_in["x"],
        "A_wqkv": _A_in["wqkv"],
        "A_wo": _A_in["wo"],
        "B_x": _B_in["x"],
        "B_W_gate": _B_in["W_gate"],
        "B_W_up": _B_in["W_up"],
        "B_W_down": _B_in["W_down"],
    }

def reference(A_x, A_wqkv, A_wo, B_x, B_W_gate, B_W_up, B_W_down):
    def _A_ref(x, wqkv, wo):
        B, S, D = x.shape
        H = 32; Dh = D // H; W = 256
        qkv = (lambda x, *d: x.permute(*d))((x @ wqkv).reshape(B, S, 3, H, Dh), 2, 0, 3, 1, 4)
        q, k, v = qkv[0], qkv[1], qkv[2]
        scores = torch.einsum("bhqd,bhkd->bhqk", q, k) * (Dh ** -0.5)
        idx = torch.arange(S)
        mask = (idx[:, None] - idx[None, :] >= 0) & (idx[:, None] - idx[None, :] < W)
        scores = torch.where(mask[None, None], scores, -1e9)
        p = _softmax(scores, axis=-1)
        o = torch.einsum("bhqk,bhkd->bhqd", p, v)
        return (lambda x, *d: x.permute(*d))(o, 0, 2, 1, 3).reshape(B, S, D) @ wo

    def _B_ref(x, W_gate, W_up, W_down):
        gate = torch.einsum("bsd,df->bsf", x, W_gate)
        up = torch.einsum("bsd,df->bsf", x, W_up)
        gate = gate * (lambda x, dt: x.to(dt))(gate > 0, gate.dtype)
        h = gate * up
        return torch.einsum("bsf,fd->bsd", h, W_down)

    x = A_x
    h = x + _A_ref(_layernorm(x, axis=-1), A_wqkv, A_wo)
    h2 = h + _B_ref(_layernorm(h, axis=-1), B_W_gate, B_W_up, B_W_down)
    return h2
```

```cpp
#include <hip/hip_runtime.h>
#include <hip/hip_bf16.h>
#include <stdint.h>

typedef __attribute__((ext_vector_type(8))) __bf16 bf16x8;
typedef __attribute__((ext_vector_type(2))) __bf16 bf16x2;
typedef __attribute__((ext_vector_type(4))) short s16x4;
typedef __attribute__((ext_vector_type(8))) short s16x8;
typedef __attribute__((ext_vector_type(2))) float f32x2;
typedef __attribute__((ext_vector_type(4))) float f32x4;
typedef __attribute__((ext_vector_type(16))) float f32x16;
typedef __attribute__((ext_vector_type(2))) unsigned u32x2;
typedef __attribute__((ext_vector_type(4))) unsigned u32x4;
typedef __attribute__((address_space(3))) char* lds_ptr;
typedef const __attribute__((address_space(1))) void* gptr_t;

#define DEVI __device__ __forceinline__
#define WAIT_V(n) asm volatile("s_waitcnt vmcnt(" #n ")" ::: "memory")
#define WAIT_L0() asm volatile("s_waitcnt lgkmcnt(0)" ::: "memory")
#define BAR() __builtin_amdgcn_s_barrier()

#define STAMP(diag, kid, ph) do { } while (0)

constexpr int S_ = 2048, D_ = 512, F_ = 1792;
constexpr float EPS = 1e-5f;
constexpr float QSCALE = 0.25f * 1.44269504088896340736f;

constexpr size_t WS_QF = 0;
constexpr size_t WS_KF = WS_QF + (2u << 20);
constexpr size_t WS_VF = WS_KF + (2u << 20);
constexpr size_t WS_OF = WS_VF + (2u << 20);
constexpr size_t WS_HF = WS_OF + (2u << 20);
constexpr size_t WS_MF = WS_HF + (2u << 20);
constexpr size_t WS_WOT = WS_MF + (7u << 20);
constexpr size_t WS_WGU = WS_WOT + (512u << 10);
constexpr size_t WS_WDT = WS_WGU + (3584u << 10);
constexpr size_t WS_ST2 = WS_WDT + (1792u << 10);
constexpr size_t WS_CSP = WS_ST2 + (256u << 10);
constexpr size_t WS_DIAG = WS_CSP + (64u << 10);

DEVI void glds16(const void* g, char* smem_base, int lds_off) {
    __builtin_amdgcn_global_load_lds((gptr_t)g, (__attribute__((address_space(3))) void*)((lds_ptr)smem_base + lds_off), 16, 0, 0);
}
DEVI bf16x8 lds_read16(char* smem, int off) { return *(const bf16x8*)(smem + off); }
DEVI bf16x8 lds_read_tr(char* smem, int off0, int off1) {
    s16x4 a = __builtin_amdgcn_ds_read_tr16_b64_v4i16((__attribute__((address_space(3))) s16x4*)((lds_ptr)smem + off0));
    s16x4 b = __builtin_amdgcn_ds_read_tr16_b64_v4i16((__attribute__((address_space(3))) s16x4*)((lds_ptr)smem + off1));
    s16x8 c = __builtin_shufflevector(a, b, 0, 1, 2, 3, 4, 5, 6, 7);
    return __builtin_bit_cast(bf16x8, c);
}
DEVI unsigned pack2(float a, float b) { return __builtin_bit_cast(unsigned, __builtin_convertvector(f32x2{a, b}, bf16x2)); }
DEVI float bf_lo(unsigned u) { return __builtin_bit_cast(float, u << 16); }
DEVI float bf_hi(unsigned u) { return __builtin_bit_cast(float, u & 0xffff0000u); }
DEVI float swap32_add(float v) {
    unsigned a = __builtin_bit_cast(unsigned, v), b = a;
    asm volatile("" : "+v"(b));
    auto r = __builtin_amdgcn_permlane32_swap(a, b, false, false);
    const unsigned r0 = r[0], r1 = r[1];
    return __builtin_bit_cast(float, r0) + __builtin_bit_cast(float, r1);
}
DEVI float swap32_max(float v) {
    unsigned a = __builtin_bit_cast(unsigned, v), b = a;
    asm volatile("" : "+v"(b));
    auto r = __builtin_amdgcn_permlane32_swap(a, b, false, false);
    const unsigned r0 = r[0], r1 = r[1];
    return fmaxf(__builtin_bit_cast(float, r0), __builtin_bit_cast(float, r1));
}
DEVI f32x4 mfma16(bf16x8 a, bf16x8 b, f32x4 c) { return __builtin_amdgcn_mfma_f32_16x16x32_bf16(a, b, c, 0, 0, 0); }
DEVI f32x16 mfma32(bf16x8 a, bf16x8 b, f32x16 c) { return __builtin_amdgcn_mfma_f32_32x32x16_bf16(a, b, c, 0, 0, 0); }
DEVI bf16x8 ones8() {
    u32x4 o = {0x3F803F80u, 0x3F803F80u, 0x3F803F80u, 0x3F803F80u};
    return __builtin_bit_cast(bf16x8, o);
}

constexpr int K1_SLOT = 28672, K1_XB = 16384, K1_RING = 5;
constexpr int K1_STAT = K1_SLOT * K1_RING;
constexpr int K1_CSP = K1_STAT + 1024;
constexpr int K1_CS = K1_CSP + 1536;
constexpr int K1_LDS = K1_CS + 384;

template <int NV>
DEVI void qkv_body(const uint16_t* __restrict__ x, const uint16_t* __restrict__ wqkv, char* __restrict__ ws,
                   char* smem, int rb, int ct) {
    const int tid = threadIdx.x, lane = tid & 63, w = __builtin_amdgcn_readfirstlane(tid >> 6);
    const int m = lane & 15, g = lane >> 4;
    const char* xsrc[4];
#pragma unroll
    for (int i = 0; i < 4; ++i) {
        const int r = 32 * w + 8 * i + (lane >> 3);
        const int c = (lane & 7) ^ ((r >> 1) & 7);
        xsrc[i] = (const char*)x + (size_t)(rb * 128 + r) * 1024 + c * 16;
    }
    const char* wsrc[3];
#pragma unroll
    for (int i = 0; i < 3; ++i) {
        const int ci = 64 * (3 * w + i) + lane;
        const int r = ci / 12, cp = ci % 12;
        const int c = cp ^ (((r >> 3) & 1) << 1);
        wsrc[i] = (const char*)wqkv + (size_t)r * 3072 + ct * 192 + c * 16;
    }
    auto issue = [&](int c) {
        const int slot = (c % K1_RING) * K1_SLOT;
#pragma unroll
        for (int i = 0; i < 4; ++i) glds16(xsrc[i] + c * 128, smem, slot + (32 * w + 8 * i) * 128);
#pragma unroll
        for (int i = 0; i < 3; ++i) glds16(wsrc[i] + (size_t)c * 64 * 3072, smem, slot + K1_XB + (3 * w + i) * 1024);
    };
    int xoff[2][2];
#pragma unroll
    for (int tt = 0; tt < 2; ++tt)
#pragma unroll
        for (int s = 0; s < 2; ++s) {
            const int r = 32 * w + 16 * tt + m;
            xoff[tt][s] = r * 128 + (((4 * s + g) ^ ((r >> 1) & 7)) << 4);
        }
    const int wq = (lane >> 2) & 3, wp = lane & 3;
    const int woff_even = K1_XB + (8 * g + wq) * 192 + wp * 8;
    f32x4 acc[2][6], ssum[2], ssq[2], cs[6];
#pragma unroll
    for (int tt = 0; tt < 2; ++tt) {
        ssum[tt] = f32x4{0, 0, 0, 0};
        ssq[tt] = f32x4{0, 0, 0, 0};
#pragma unroll
        for (int nt = 0; nt < 6; ++nt) acc[tt][nt] = f32x4{0, 0, 0, 0};
    }
#pragma unroll
    for (int nt = 0; nt < 6; ++nt) cs[nt] = f32x4{0, 0, 0, 0};
    const bf16x8 ones = ones8();
    bf16x8 xf[2][2], wf[2][6];
    auto ldfrags = [&](int ks, int b) {
        const int slot = ((ks >> 1) % K1_RING) * K1_SLOT, s = ks & 1;
#pragma unroll
        for (int tt = 0; tt < 2; ++tt) xf[b][tt] = lds_read16(smem, slot + xoff[tt][s]);
#pragma unroll
        for (int nt = 0; nt < 6; ++nt) {
            const int o = slot + woff_even + s * 32 * 192 + ((nt ^ (g & 1)) << 5);
            wf[b][nt] = lds_read_tr(smem, o, o + 4 * 192);
        }
    };
#pragma unroll
    for (int c = 0; c < K1_RING; ++c) issue(c);
    WAIT_V(28);
    BAR();
    ldfrags(0, 0);
#pragma unroll
    for (int ks = 0; ks < 16; ++ks) {
        const int b = ks & 1;
        if (ks < 15) {
            if (ks & 1) {
                const int c = ks >> 1;
                WAIT_L0();
                if (c <= 3) WAIT_V(21);
                else if (c == 4) WAIT_V(14);
                else if (c == 5) WAIT_V(7);
                else WAIT_V(0);
                BAR();
                if (c + K1_RING < 8) issue(c + K1_RING);
            }
            ldfrags(ks + 1, b ^ 1);
        }
#pragma unroll
        for (int nt = 0; nt < 6; ++nt)
#pragma unroll
            for (int tt = 0; tt < 2; ++tt)
                acc[tt][nt] = (nt >= 6 - NV) ? mfma16(xf[b][tt], wf[b][nt], acc[tt][nt]) : mfma16(wf[b][nt], xf[b][tt], acc[tt][nt]);
#pragma unroll
        for (int tt = 0; tt < 2; ++tt) {
            ssum[tt] = mfma16(ones, xf[b][tt], ssum[tt]);
            ssq[tt] = mfma16(xf[b][tt], xf[b][tt], ssq[tt]);
        }
        if ((ks & 3) == w) {
#pragma unroll
            for (int nt = 0; nt < 6; ++nt) cs[nt] = mfma16(wf[b][nt], ones, cs[nt]);
        }
    }
    f32x2* stat = (f32x2*)(smem + K1_STAT);
    float* csp = (float*)(smem + K1_CSP);
    float* csf = (float*)(smem + K1_CS);
#pragma unroll
    for (int tt = 0; tt < 2; ++tt) {
        const int r = m & 3;
        const float sq = r == 0 ? ssq[tt][0] : r == 1 ? ssq[tt][1] : r == 2 ? ssq[tt][2] : ssq[tt][3];
        const float mean = ssum[tt][0] * (1.0f / D_);
        const float var = fmaxf(sq * (1.0f / D_) - mean * mean, 0.0f);
        if ((m >> 2) == g) stat[32 * w + 16 * tt + m] = f32x2{mean, __builtin_amdgcn_rsqf(var + EPS)};
    }
    if (m == 0) {
#pragma unroll
        for (int nt = 0; nt < 6; ++nt) *(f32x4*)(csp + w * 96 + nt * 16 + 4 * g) = cs[nt];
    }
    WAIT_L0();
    BAR();
    if (tid < 96) csf[tid] = (csp[tid] + csp[96 + tid]) + (csp[192 + tid] + csp[288 + tid]);
    WAIT_L0();
    BAR();
    const int tile = rb * 4 + w;
#pragma unroll
    for (int nt = 0; nt < 6; ++nt) {
        const int col = ct * 96 + nt * 16;
        if (nt < 6 - NV) {
            const int isk = col >= 512;
            const int head = (col & 511) >> 4;
            const float sc = isk ? 1.0f : QSCALE;
            const f32x4 c4 = *(const f32x4*)(csf + nt * 16 + 4 * g);
            char* dst = ws + (isk ? WS_KF : WS_QF) + (size_t)(tile * 32 + head) * 1024 + (g >> 1) * 512 + (g & 1) * 8;
#pragma unroll
            for (int tt = 0; tt < 2; ++tt) {
                const f32x2 st = stat[32 * w + 16 * tt + m];
                const float rs = st[1] * sc;
                float v[4];
#pragma unroll
                for (int r = 0; r < 4; ++r) v[r] = (acc[tt][nt][r] - st[0] * c4[r]) * rs;
                *(u32x2*)(dst + (16 * tt + m) * 16) = u32x2{pack2(v[0], v[1]), pack2(v[2], v[3])};
            }
        } else {
            const int head = (col - 1024) >> 4;
            const float c1 = csf[nt * 16 + m];
            char* dst = ws + WS_VF + (size_t)(tile * 16 + (head >> 1)) * 2048 + ((g & 1) * 32 + (head & 1) * 16 + m) * 16 + (g >> 1) * 8;
#pragma unroll
            for (int tt = 0; tt < 2; ++tt) {
                float v[4];
#pragma unroll
                for (int r = 0; r < 4; ++r) {
                    const f32x2 st = stat[32 * w + 16 * tt + 4 * g + r];
                    v[r] = (acc[tt][nt][r] - st[0] * c1) * st[1];
                }
                *(u32x2*)(dst + tt * 1024) = u32x2{pack2(v[0], v[1]), pack2(v[2], v[3])};
            }
        }
    }
}

__global__ __launch_bounds__(256, 1) void k1_qkv(const uint16_t* __restrict__ x, const uint16_t* __restrict__ wqkv, char* __restrict__ ws) {
    extern __shared__ __attribute__((aligned(16))) char smem[];
    STAMP((unsigned long long*)(ws + WS_DIAG), 0, 0);
    const int xcd = blockIdx.x & 7, idx = blockIdx.x >> 3;
    const int rb = 2 * xcd + (idx >> 4), ct = idx & 15;
    if (ct < 10) qkv_body<0>(x, wqkv, ws, smem, rb, ct);
    else if (ct == 10) qkv_body<2>(x, wqkv, ws, smem, rb, ct);
    else qkv_body<6>(x, wqkv, ws, smem, rb, ct);
    STAMP((unsigned long long*)(ws + WS_DIAG), 0, 3);
}

constexpr int K2_K = 0, K2_V = 24576, K2_ONES = 49152, K2_JOB = 49152 + 256, K2_RED = K2_JOB + 32768;
constexpr int K2_LDS = K2_RED + 8 * 128 * 4;

template <bool FULL>
DEVI void attn_task(char* smem, char* __restrict__ ws, bf16x8 q, int rb, int i, int e, int p, int lane) {
    const int a = lane & 31, h = lane >> 5;
    const int koff = K2_K + e * 1024 + lane * 16;
    const bool isdata = a < 16;
    const int voff = isdata ? K2_V + (h * 32 + e * 16 + a) * 16 : K2_ONES;
    const int vstride = isdata ? 1024 : 0;
    const int umin = FULL ? 0 : 8 - 4 * rb;
    const float NEG = -__builtin_inff();
    const f32x16 zero = {0};
    f32x16 mg;
    {
        const f32x16 sl = mfma32(lds_read16(smem, koff + (i + 8) * 2048), q, zero);
        f32x16 sf;
        if (FULL || i >= umin) sf = mfma32(lds_read16(smem, koff + i * 2048), q, zero);
        else {
#pragma unroll
            for (int r = 0; r < 16; ++r) sf[r] = NEG;
        }
#pragma unroll
        for (int r = 0; r < 16; ++r) mg[r] = ((r & 3) + 8 * (r >> 2) + 4 * h > a) ? sf[r] : sl[r];
    }
    float mx = fmaxf(mg[0], mg[1]);
#pragma unroll
    for (int r = 2; r < 16; r += 2) mx = fmaxf(fmaxf(mx, mg[r]), mg[r + 1]);
#pragma unroll
    for (int t = 1; t < 8; ++t) {
        if (FULL || i + t >= umin) {
            const f32x16 s = mfma32(lds_read16(smem, koff + (i + t) * 2048), q, zero);
#pragma unroll
            for (int r = 0; r < 16; r += 2) mx = fmaxf(fmaxf(mx, s[r]), s[r + 1]);
        }
    }
    mx = swap32_max(mx);
    f32x16 negm;
#pragma unroll
    for (int r = 0; r < 16; ++r) negm[r] = -mx;
    f32x16 o = zero;
    {
        unsigned pf[8], pl[8];
#pragma unroll
        for (int r = 0; r < 16; r += 2) {
            const float p0 = __builtin_amdgcn_exp2f(mg[r] - mx), p1 = __builtin_amdgcn_exp2f(mg[r + 1] - mx);
            const unsigned pk = pack2(p0, p1);
            const int row = (r & 3) + 8 * (r >> 2) + 4 * h;
            const unsigned mk = (row > a ? 0xffffu : 0u) | (row + 1 > a ? 0xffff0000u : 0u);
            pf[r >> 1] = pk & mk;
            pl[r >> 1] = pk & ~mk;
        }
#pragma unroll
        for (int s = 0; s < 2; ++s) {
            const bf16x8 bl = __builtin_bit_cast(bf16x8, u32x4{pl[4 * s], pl[4 * s + 1], pl[4 * s + 2], pl[4 * s + 3]});
            o = mfma32(lds_read16(smem, voff + ((i + 8) * 2 + s) * vstride), bl, o);
            if (FULL || i >= umin) {
                const bf16x8 bfi = __builtin_bit_cast(bf16x8, u32x4{pf[4 * s], pf[4 * s + 1], pf[4 * s + 2], pf[4 * s + 3]});
                o = mfma32(lds_read16(smem, voff + (i * 2 + s) * vstride), bfi, o);
            }
        }
    }
#pragma unroll
    for (int t = 1; t < 8; ++t) {
        if (FULL || i + t >= umin) {
            const f32x16 s = mfma32(lds_read16(smem, koff + (i + t) * 2048), q, negm);
            unsigned pk[8];
#pragma unroll
            for (int r = 0; r < 16; r += 2) pk[r >> 1] = pack2(__builtin_amdgcn_exp2f(s[r]), __builtin_amdgcn_exp2f(s[r + 1]));
#pragma unroll
            for (int s2 = 0; s2 < 2; ++s2) {
                const bf16x8 b = __builtin_bit_cast(bf16x8, u32x4{pk[4 * s2], pk[4 * s2 + 1], pk[4 * s2 + 2], pk[4 * s2 + 3]});
                o = mfma32(lds_read16(smem, voff + ((i + t) * 2 + s2) * vstride), b, o);
            }
        }
    }
    const float inv = __builtin_amdgcn_rcpf(o[8]);
    const u32x4 ov = {pack2(o[0] * inv, o[1] * inv), pack2(o[2] * inv, o[3] * inv), pack2(o[4] * inv, o[5] * inv), pack2(o[6] * inv, o[7] * inv)};
    *(u32x4*)(ws + WS_OF + (size_t)((rb * 4 + i) * 32 + 2 * p + e) * 1024 + lane * 16) = ov;
}

__global__ __launch_bounds__(512, 2) void k2_attn(const uint16_t* __restrict__ wo, const uint16_t* __restrict__ wg, const uint16_t* __restrict__ wu,
                                                  const uint16_t* __restrict__ wd, char* __restrict__ ws) {
    extern __shared__ __attribute__((aligned(16))) char smem[];
    STAMP((unsigned long long*)(ws + WS_DIAG), 1, 0);
    const int tid = threadIdx.x, lane = tid & 63, w = __builtin_amdgcn_readfirstlane(tid >> 6);
    const int xcd = blockIdx.x & 7, idx = blockIdx.x >> 3;
    const int rb = 2 * xcd + (idx >> 4), p = idx & 15;
    const int i = w & 3, e = w >> 2;
#pragma unroll
    for (int n = 0; n < 6; ++n) {
        const int pc = 6 * w + n;
        const int isv = pc >= 24, u = (pc % 24) >> 1, half = pc & 1;
        int kt = 4 * rb - 8 + u;
        kt = kt < 0 ? 0 : kt;
        const char* src = ws + (isv ? WS_VF : WS_KF) + (size_t)(kt * 16 + p) * 2048 + half * 1024 + lane * 16;
        glds16(src, smem, (isv ? K2_V : K2_K) + u * 2048 + half * 1024);
    }
    const bf16x8 q = *(const bf16x8*)(ws + WS_QF + (size_t)((rb * 4 + i) * 32 + 2 * p + e) * 1024 + lane * 16);
    if (tid < 4) *(unsigned*)(smem + K2_ONES + tid * 4) = 0x3F803F80u;
    WAIT_V(0);
    WAIT_L0();
    BAR();
    const int bid = blockIdx.x;
    const int job = K2_JOB + w * 4096;
    if (bid < 112) {
        const int cg = bid % 28, s = 8 * (bid / 28) + w;
#pragma unroll
        for (int n = 0; n < 4; ++n) {
            const uint16_t* src = (n >> 1) ? wu : wg;
            const int row = 16 * s + 8 * (n & 1) + (lane >> 3);
            glds16((const char*)src + (size_t)row * (F_ * 2) + cg * 128 + (lane & 7) * 16, smem, job + n * 1024);
        }
    } else {
        const int jj = (bid - 112) * 8 + w;
        const bool iso = jj < 256;
        const int j2 = iso ? jj : jj - 256;
        const uint16_t* src = iso ? wo : wd;
        const int fp = j2 & 7, s = j2 >> 3;
#pragma unroll
        for (int n = 0; n < 2; ++n) {
            const int row = 16 * s + 8 * n + (lane >> 3);
            glds16((const char*)src + (size_t)row * (D_ * 2) + fp * 128 + (lane & 7) * 16, smem, job + n * 1024);
        }
    }
    if (rb >= 2) attn_task<true>(smem, ws, q, rb, i, e, p, lane);
    else attn_task<false>(smem, ws, q, rb, i, e, p, lane);
    WAIT_V(1);
    const int G = lane >> 4, nh = G & 1, hh = G >> 1, rq = (lane >> 2) & 3, pp = lane & 3;
    if (bid < 112) {
        const int cg = bid % 28, kq = bid / 28, s = 8 * kq + w;
        float part[4];
#pragma unroll
        for (int t = 0; t < 4; ++t) {
            const int o = job + nh * 2048 + (4 * hh + rq) * 128 + t * 32 + pp * 8;
            const bf16x8 f = lds_read_tr(smem, o, o + 8 * 128);
            const int T = 4 * cg + t, j = T / 7, t7 = T % 7;
            *(bf16x8*)(ws + WS_WGU + (size_t)((j * 32 + s) * 7 + t7) * 1024 + lane * 16) = f;
            const u32x4 u = __builtin_bit_cast(u32x4, f);
            float sum = 0.f;
#pragma unroll
            for (int r = 0; r < 4; ++r) sum += bf_lo(u[r]) + bf_hi(u[r]);
            part[t] = swap32_add(sum);
        }
        float* red = (float*)(smem + K2_RED);
        if (lane < 32) {
#pragma unroll
            for (int t = 0; t < 4; ++t) red[w * 128 + nh * 64 + t * 16 + (lane & 15)] = part[t];
        }
        WAIT_L0();
        BAR();
        if (tid < 128) {
            float sum = 0.f;
#pragma unroll
            for (int ww = 0; ww < 8; ++ww) sum += red[ww * 128 + tid];
            ((float*)(ws + WS_CSP))[(kq * 2 + (tid >> 6)) * F_ + cg * 64 + (tid & 63)] = sum;
        }
    } else {
        const int jj = (bid - 112) * 8 + w;
        const bool iso = jj < 256;
        const int j2 = iso ? jj : jj - 256;
        const int fp = j2 & 7, s = j2 >> 3;
        char* dst = ws + (iso ? WS_WOT + (size_t)(fp * 32 + s) * 2048 : WS_WDT + (size_t)(fp * 112 + s) * 2048) + lane * 16;
#pragma unroll
        for (int t = 0; t < 2; ++t) {
            const int o = job + (4 * hh + rq) * 128 + t * 64 + nh * 32 + pp * 8;
            *(bf16x8*)(dst + t * 1024) = lds_read_tr(smem, o, o + 8 * 128);
        }
    }
    STAMP((unsigned long long*)(ws + WS_DIAG), 1, 3);
}

constexpr int K3_A = 0, K3_B = 65536, K3_X = 131072, K3_LDS = K3_X + 8192;

__global__ __launch_bounds__(256, 1) void k3_oproj(const uint16_t* __restrict__ x, char* __restrict__ ws) {
    extern __shared__ __attribute__((aligned(16))) char smem[];
    STAMP((unsigned long long*)(ws + WS_DIAG), 2, 0);
    const int tid = threadIdx.x, lane = tid & 63, w = __builtin_amdgcn_readfirstlane(tid >> 6);
    const int xcd = blockIdx.x & 7, idx = blockIdx.x >> 3;
    const int tb = 4 * xcd + (idx >> 3), fp = idx & 7;
    const int tt = w & 1, ft = w >> 1;
    const char* asrc = ws + WS_WOT + (size_t)fp * 65536 + lane * 16;
    const char* bsrc = ws + WS_OF + (size_t)tb * 65536 + lane * 16;
#pragma unroll
    for (int st = 0; st < 4; ++st) {
#pragma unroll
        for (int n = 0; n < 4; ++n) {
            const int pa = st * 16 + w * 4 + n;
            glds16(asrc + pa * 1024, smem, K3_A + pa * 1024);
        }
#pragma unroll
        for (int n = 0; n < 4; ++n) {
            const int pb = (w >> 1) * 32 + st * 8 + (w & 1) * 4 + n;
            glds16(bsrc + pb * 1024, smem, K3_B + pb * 1024);
        }
    }
#pragma unroll
    for (int n = 0; n < 2; ++n) {
        const int r = (w * 2 + n) * 8 + (lane >> 3);
        const int c = (lane & 7) ^ ((r >> 1) & 7);
        glds16((const char*)x + (size_t)(tb * 64 + r) * 1024 + fp * 128 + c * 16, smem, K3_X + (w * 2 + n) * 1024);
    }
    f32x16 acc = {0};
#pragma unroll
    for (int st = 0; st < 4; ++st) {
        if (st == 0) WAIT_V(26);
        else if (st == 1) WAIT_V(18);
        else if (st == 2) WAIT_V(10);
        else WAIT_V(2);
        BAR();
#pragma unroll
        for (int n = 0; n < 8; ++n) {
            const int s = st * 8 + n;
            acc = mfma32(lds_read16(smem, K3_A + (s * 2 + ft) * 1024 + lane * 16), lds_read16(smem, K3_B + (tt * 32 + s) * 1024 + lane * 16), acc);
        }
    }
    WAIT_V(0);
    BAR();
    const int tok = lane & 31, h = lane >> 5;
    const int r = tt * 32 + tok;
    unsigned pk[8];
    float sum = 0.f, sq = 0.f;
#pragma unroll
    for (int qd = 0; qd < 4; ++qd) {
        const u32x2 xv = *(const u32x2*)(smem + K3_X + r * 128 + (((ft * 4 + qd) ^ ((r >> 1) & 7)) << 4) + h * 8);
        pk[2 * qd] = pack2(acc[4 * qd] + bf_lo(xv[0]), acc[4 * qd + 1] + bf_hi(xv[0]));
        pk[2 * qd + 1] = pack2(acc[4 * qd + 2] + bf_lo(xv[1]), acc[4 * qd + 3] + bf_hi(xv[1]));
#pragma unroll
        for (int k = 0; k < 2; ++k) {
            const float lo = bf_lo(pk[2 * qd + k]), hi = bf_hi(pk[2 * qd + k]);
            sum += lo + hi;
            sq += lo * lo + hi * hi;
        }
    }
    char* hdst = ws + WS_HF + (size_t)((tb * 2 + tt) * 32 + (fp * 2 + ft) * 2) * 1024 + lane * 16;
    *(u32x4*)hdst = u32x4{pk[0], pk[1], pk[2], pk[3]};
    *(u32x4*)(hdst + 1024) = u32x4{pk[4], pk[5], pk[6], pk[7]};
    sum = swap32_add(sum);
    sq = swap32_add(sq);
    if (h == 0) ((f32x2*)(ws + WS_ST2))[(fp * 2 + ft) * S_ + tb * 64 + r] = f32x2{sum, sq};
    STAMP((unsigned long long*)(ws + WS_DIAG), 2, 3);
}

constexpr int K4_A = 28672, K4_SLOT = 45056, K4_RING = 3, K4_CS = K4_SLOT * K4_RING, K4_LDS = K4_CS + 224 * 4;

__global__ __launch_bounds__(256, 1) void k4_gateup(char* __restrict__ ws) {
    extern __shared__ __attribute__((aligned(16))) char smem[];
    STAMP((unsigned long long*)(ws + WS_DIAG), 3, 0);
    const int tid = threadIdx.x, lane = tid & 63, w = __builtin_amdgcn_readfirstlane(tid >> 6);
    const int xcd = blockIdx.x & 7, idx = blockIdx.x >> 3;
    const int rb = 2 * xcd + (idx >> 4), j = idx & 15;
    const int tok = lane & 31, h = lane >> 5;
    const int tile = rb * 4 + w;
    f32x2 st[8];
#pragma unroll
    for (int n = 0; n < 8; ++n) st[n] = ((const f32x2*)(ws + WS_ST2))[(h * 8 + n) * S_ + tile * 32 + tok];
    float csq[4];
    {
        const int t = tid < 224 ? tid : 0;
        const int gu = t >= 112, col = j * 112 + (gu ? t - 112 : t);
#pragma unroll
        for (int n = 0; n < 4; ++n) csq[n] = ((const float*)(ws + WS_CSP))[(n * 2 + gu) * F_ + col];
    }
    const char* asrc = ws + WS_WGU + (size_t)j * (32 * 7168) + w * 7168 + lane * 16;
    const char* bsrc = ws + WS_HF + (size_t)tile * 32768 + lane * 16;
    auto issue = [&](int c) {
        const int slot = (c % K4_RING) * K4_SLOT;
#pragma unroll
        for (int n = 0; n < 7; ++n) glds16(asrc + c * 28672 + n * 1024, smem, slot + w * 7168 + n * 1024);
#pragma unroll
        for (int n = 0; n < 4; ++n) glds16(bsrc + (c * 4 + n) * 1024, smem, slot + K4_A + (w * 4 + n) * 1024);
    };
    f32x16 acc[7];
#pragma unroll
    for (int t = 0; t < 7; ++t) acc[t] = f32x16{0};
    bf16x8 af[2][7], bfr[2];
    auto ldfrags = [&](int s, int b) {
        const int slot = ((s >> 2) % K4_RING) * K4_SLOT, n = s & 3;
        bfr[b] = lds_read16(smem, slot + K4_A + (w * 4 + n) * 1024 + lane * 16);
#pragma unroll
        for (int t = 0; t < 7; ++t) af[b][t] = lds_read16(smem, slot + (n * 7 + t) * 1024 + lane * 16);
    };
#pragma unroll
    for (int c = 0; c < K4_RING; ++c) issue(c);
    WAIT_V(22);
    BAR();
    ldfrags(0, 0);
#pragma unroll
    for (int s = 0; s < 32; ++s) {
        const int b = s & 1;
        if (s < 31) {
            if ((s & 3) == 3) {
                const int c = s >> 2;
                WAIT_L0();
                if (c <= 5) WAIT_V(11);
                else WAIT_V(0);
                BAR();
                if (c + K4_RING < 8) issue(c + K4_RING);
            }
            ldfrags(s + 1, b ^ 1);
        }
#pragma unroll
        for (int t = 0; t < 7; ++t) acc[t] = mfma32(af[b][t], bfr[b], acc[t]);
    }
    float* cs = (float*)(smem + K4_CS);
    if (tid < 224) cs[tid] = (csq[0] + csq[1]) + (csq[2] + csq[3]);
    float sum = 0.f, sq = 0.f;
#pragma unroll
    for (int n = 0; n < 8; ++n) {
        sum += st[n][0];
        sq += st[n][1];
    }
    sum = swap32_add(sum);
    sq = swap32_add(sq);
    const float mean = sum * (1.0f / D_);
    const float rstd = __builtin_amdgcn_rsqf(fmaxf(sq * (1.0f / D_) - mean * mean, 0.0f) + EPS);
    WAIT_L0();
    BAR();
    char* dst = ws + WS_MF + (size_t)(tile * 112 + j * 7) * 1024 + lane * 16;
#pragma unroll
    for (int t = 0; t < 7; ++t) {
        float v[8];
#pragma unroll
        for (int qd = 0; qd < 2; ++qd) {
            const f32x4 cg = *(const f32x4*)(cs + t * 16 + 8 * qd + 4 * h);
            const f32x4 cu = *(const f32x4*)(cs + 112 + t * 16 + 8 * qd + 4 * h);
#pragma unroll
            for (int r = 0; r < 4; ++r) {
                const float gt = (acc[t][4 * qd + r] - mean * cg[r]) * rstd;
                const float up = (acc[t][8 + 4 * qd + r] - mean * cu[r]) * rstd;
                v[4 * qd + r] = fmaxf(gt, 0.0f) * up;
            }
        }
        *(u32x4*)(dst + t * 1024) = u32x4{pack2(v[0], v[1]), pack2(v[2], v[3]), pack2(v[4], v[5]), pack2(v[6], v[7])};
    }
    STAMP((unsigned long long*)(ws + WS_DIAG), 3, 3);
}

constexpr int K5_SLOT = 32768, K5_RING = 4, K5_B = 16384, K5_OUT = K5_SLOT * K5_RING, K5_LDS = K5_OUT + 8192;

__global__ __launch_bounds__(256, 1) void k5_down(char* __restrict__ ws, uint16_t* __restrict__ out) {
    extern __shared__ __attribute__((aligned(16))) char smem[];
    STAMP((unsigned long long*)(ws + WS_DIAG), 4, 0);
    const int tid = threadIdx.x, lane = tid & 63, w = __builtin_amdgcn_readfirstlane(tid >> 6);
    const int xcd = blockIdx.x & 7, idx = blockIdx.x >> 3;
    const int tb = 4 * xcd + (idx >> 3), fp = idx & 7;
    const int tt = w & 1, ft = w >> 1;
    const char* hsrc = ws + WS_HF + (size_t)((tb * 2 + tt) * 32 + (fp * 2 + ft) * 2) * 1024 + lane * 16;
    const u32x4 h0 = *(const u32x4*)hsrc, h1 = *(const u32x4*)(hsrc + 1024);
    const char* asrc = ws + WS_WDT + (size_t)fp * (112 * 2048) + w * 4096 + lane * 16;
    const char* bsrc = ws + WS_MF + (size_t)(tb * 2 + (w >> 1)) * (112 * 1024) + (w & 1) * 4096 + lane * 16;
    auto issue = [&](int c) {
        const int slot = (c % K5_RING) * K5_SLOT;
#pragma unroll
        for (int n = 0; n < 4; ++n) glds16(asrc + c * 16384 + n * 1024, smem, slot + w * 4096 + n * 1024);
#pragma unroll
        for (int n = 0; n < 4; ++n) glds16(bsrc + c * 8192 + n * 1024, smem, slot + K5_B + (w >> 1) * 8192 + (w & 1) * 4096 + n * 1024);
    };
    f32x16 acc = {0};
    bf16x8 af[2][4], bfr[2][4];
    auto ldfrags = [&](int hs, int b) {
        const int slot = ((hs >> 1) % K5_RING) * K5_SLOT, n0 = (hs & 1) * 4;
#pragma unroll
        for (int n = 0; n < 4; ++n) {
            af[b][n] = lds_read16(smem, slot + ((n0 + n) * 2 + ft) * 1024 + lane * 16);
            bfr[b][n] = lds_read16(smem, slot + K5_B + tt * 8192 + (n0 + n) * 1024 + lane * 16);
        }
    };
#pragma unroll
    for (int c = 0; c < K5_RING; ++c) issue(c);
    WAIT_V(24);
    BAR();
    ldfrags(0, 0);
#pragma unroll
    for (int hs = 0; hs < 28; ++hs) {
        const int b = hs & 1;
        if (hs < 27) {
            if (hs & 1) {
                const int c = hs >> 1;
                WAIT_L0();
                if (c <= 10) WAIT_V(16);
                else if (c == 11) WAIT_V(8);
                else WAIT_V(0);
                BAR();
                if (c + K5_RING < 14) issue(c + K5_RING);
            }
            ldfrags(hs + 1, b ^ 1);
        }
#pragma unroll
        for (int n = 0; n < 4; ++n) acc = mfma32(af[b][n], bfr[b][n], acc);
    }
    const int tok = lane & 31, h = lane >> 5;
    const int r = tt * 32 + tok;
    const unsigned hv[8] = {h0[0], h0[1], h0[2], h0[3], h1[0], h1[1], h1[2], h1[3]};
#pragma unroll
    for (int qd = 0; qd < 4; ++qd) {
        const u32x2 v = {pack2(acc[4 * qd] + bf_lo(hv[2 * qd]), acc[4 * qd + 1] + bf_hi(hv[2 * qd])),
                         pack2(acc[4 * qd + 2] + bf_lo(hv[2 * qd + 1]), acc[4 * qd + 3] + bf_hi(hv[2 * qd + 1]))};
        *(u32x2*)(smem + K5_OUT + r * 128 + (((ft * 4 + qd) ^ ((r >> 1) & 7)) << 4) + h * 8) = v;
    }
    WAIT_L0();
    BAR();
#pragma unroll
    for (int n = 0; n < 2; ++n) {
        const int rr = (w * 2 + n) * 8 + (lane >> 3), cp = lane & 7;
        const u32x4 v = *(const u32x4*)(smem + K5_OUT + rr * 128 + cp * 16);
        *(u32x4*)((char*)out + (size_t)(tb * 64 + rr) * 1024 + fp * 128 + ((cp ^ ((rr >> 1) & 7)) << 4)) = v;
    }
    STAMP((unsigned long long*)(ws + WS_DIAG), 4, 3);
}

extern "C" void kernel_launch(void* const* d_in, const int* in_sizes, int n_in, void* d_out, int out_size, void* d_ws,
                              size_t ws_size, hipStream_t stream) {
    const uint16_t* x = (const uint16_t*)d_in[0];
    const uint16_t* wqkv = (const uint16_t*)d_in[1];
    const uint16_t* wo = (const uint16_t*)d_in[2];
    const uint16_t* wg = (const uint16_t*)d_in[4];
    const uint16_t* wu = (const uint16_t*)d_in[5];
    const uint16_t* wd = (const uint16_t*)d_in[6];
    char* ws = (char*)d_ws;
    (void)hipFuncSetAttribute((const void*)k1_qkv, hipFuncAttributeMaxDynamicSharedMemorySize, K1_LDS);
    (void)hipFuncSetAttribute((const void*)k2_attn, hipFuncAttributeMaxDynamicSharedMemorySize, K2_LDS);
    (void)hipFuncSetAttribute((const void*)k3_oproj, hipFuncAttributeMaxDynamicSharedMemorySize, K3_LDS);
    (void)hipFuncSetAttribute((const void*)k4_gateup, hipFuncAttributeMaxDynamicSharedMemorySize, K4_LDS);
    (void)hipFuncSetAttribute((const void*)k5_down, hipFuncAttributeMaxDynamicSharedMemorySize, K5_LDS);
    k1_qkv<<<256, 256, K1_LDS, stream>>>(x, wqkv, ws);
    k2_attn<<<256, 512, K2_LDS, stream>>>(wo, wg, wu, wd, ws);
    k3_oproj<<<256, 256, K3_LDS, stream>>>(x, ws);
    k4_gateup<<<256, 256, K4_LDS, stream>>>(ws);
    k5_down<<<256, 256, K5_LDS, stream>>>(ws, (uint16_t*)d_out);
}
```

```cpp
#include <hip/hip_runtime.h>
#include <hip/hip_bf16.h>
#include <stdint.h>

typedef __attribute__((ext_vector_type(8))) __bf16 bf16x8;
typedef __attribute__((ext_vector_type(2))) __bf16 bf16x2;
typedef __attribute__((ext_vector_type(4))) short s16x4;
typedef __attribute__((ext_vector_type(8))) short s16x8;
typedef __attribute__((ext_vector_type(2))) float f32x2;
typedef __attribute__((ext_vector_type(4))) float f32x4;
typedef __attribute__((ext_vector_type(16))) float f32x16;
typedef __attribute__((ext_vector_type(2))) unsigned u32x2;
typedef __attribute__((ext_vector_type(4))) unsigned u32x4;
typedef __attribute__((address_space(3))) char* lds_ptr;
typedef const __attribute__((address_space(1))) void* gptr_t;

#define DEVI __device__ __forceinline__
#define WAIT_V(n) asm volatile("s_waitcnt vmcnt(" #n ")" ::: "memory")
#define WAIT_L0() asm volatile("s_waitcnt lgkmcnt(0)" ::: "memory")
#define BAR() __builtin_amdgcn_s_barrier()
#define SB() __builtin_amdgcn_sched_barrier(0)

#define DIAG_DECL do { } while (0)
#define STAMP(ph) do { } while (0)
#define DIAG_END(ws, kid) do { } while (0)
#define DIAG_ARG
#define DIAG_PASS

constexpr int S_ = 2048, D_ = 512, F_ = 1792;
constexpr float EPS = 1e-5f;
constexpr float QSCALE = 0.25f * 1.44269504088896340736f;

constexpr size_t WS_QF = 0;
constexpr size_t WS_KF = WS_QF + (2u << 20);
constexpr size_t WS_VF = WS_KF + (2u << 20);
constexpr size_t WS_OF = WS_VF + (2u << 20);
constexpr size_t WS_HF = WS_OF + (2u << 20);
constexpr size_t WS_MF = WS_HF + (2u << 20);
constexpr size_t WS_WOT = WS_MF + (7u << 20);
constexpr size_t WS_WGU = WS_WOT + (512u << 10);
constexpr size_t WS_WDT = WS_WGU + (3584u << 10);
constexpr size_t WS_ST2 = WS_WDT + (1792u << 10);
constexpr size_t WS_CSP = WS_ST2 + (256u << 10);
constexpr size_t WS_DIAG = WS_CSP + (64u << 10);

DEVI void glds16(const void* g, char* smem_base, int lds_off) {
    __builtin_amdgcn_global_load_lds((gptr_t)g, (__attribute__((address_space(3))) void*)((lds_ptr)smem_base + lds_off), 16, 0, 0);
}
DEVI bf16x8 lds_read16(char* smem, int off) { return *(const bf16x8*)(smem + off); }
DEVI bf16x8 lds_read_tr(char* smem, int off0, int off1) {
    s16x4 a = __builtin_amdgcn_ds_read_tr16_b64_v4i16((__attribute__((address_space(3))) s16x4*)((lds_ptr)smem + off0));
    s16x4 b = __builtin_amdgcn_ds_read_tr16_b64_v4i16((__attribute__((address_space(3))) s16x4*)((lds_ptr)smem + off1));
    s16x8 c = __builtin_shufflevector(a, b, 0, 1, 2, 3, 4, 5, 6, 7);
    return __builtin_bit_cast(bf16x8, c);
}
#define K4_XA 2
#define K4_RB(xcd, idx) (((xcd) % K4_XA) * (16 / K4_XA) + ((idx) % (16 / K4_XA)))
#define K4_J(xcd, idx) (((xcd) / K4_XA) * (2 * K4_XA) + ((idx) / (16 / K4_XA)))
#define K1_XA 4
#define K1_RB(xcd, idx) (((xcd) % K1_XA) * (16 / K1_XA) + ((idx) % (16 / K1_XA)))
#define K1_CT(xcd, idx) (((xcd) / K1_XA) * (2 * K1_XA) + ((idx) / (16 / K1_XA)))
#define K5_DEPTH 6
DEVI void st16(void* p, u32x4 v) {
    asm volatile("global_store_dwordx4 %0, %1, off sc1\n\ts_nop 1" ::"v"(p), "v"(v) : "memory");
}
DEVI unsigned pack2(float a, float b) { return __builtin_bit_cast(unsigned, __builtin_convertvector(f32x2{a, b}, bf16x2)); }
DEVI float bf_lo(unsigned u) { return __builtin_bit_cast(float, u << 16); }
DEVI float bf_hi(unsigned u) { return __builtin_bit_cast(float, u & 0xffff0000u); }
DEVI float swap32_add(float v) {
    unsigned a = __builtin_bit_cast(unsigned, v), b = a;
    asm volatile("" : "+v"(b));
    auto r = __builtin_amdgcn_permlane32_swap(a, b, false, false);
    const unsigned r0 = r[0], r1 = r[1];
    return __builtin_bit_cast(float, r0) + __builtin_bit_cast(float, r1);
}
DEVI void swap32(unsigned& a, unsigned& b) {
    auto r = __builtin_amdgcn_permlane32_swap(a, b, false, false);
    const unsigned r0 = r[0], r1 = r[1];
    a = r0;
    b = r1;
}
DEVI float swap32_max(float v) {
    unsigned a = __builtin_bit_cast(unsigned, v), b = a;
    asm volatile("" : "+v"(b));
    auto r = __builtin_amdgcn_permlane32_swap(a, b, false, false);
    const unsigned r0 = r[0], r1 = r[1];
    return fmaxf(__builtin_bit_cast(float, r0), __builtin_bit_cast(float, r1));
}
DEVI f32x4 mfma16(bf16x8 a, bf16x8 b, f32x4 c) { return __builtin_amdgcn_mfma_f32_16x16x32_bf16(a, b, c, 0, 0, 0); }
DEVI f32x16 mfma32(bf16x8 a, bf16x8 b, f32x16 c) { return __builtin_amdgcn_mfma_f32_32x32x16_bf16(a, b, c, 0, 0, 0); }
DEVI bf16x8 ones8() {
    u32x4 o = {0x3F803F80u, 0x3F803F80u, 0x3F803F80u, 0x3F803F80u};
    return __builtin_bit_cast(bf16x8, o);
}

constexpr int K1_SLOT = 28672, K1_XB = 16384, K1_RING = 5;
constexpr int K1_STAT = K1_SLOT * K1_RING;
constexpr int K1_CSP = K1_STAT + 1024;
constexpr int K1_CS = K1_CSP + 1536;
constexpr int K1_LDS = K1_CS + 384;

DEVI void qkv_loader(const uint16_t* __restrict__ x, const uint16_t* __restrict__ wqkv, char* smem, int rb, int ct, int w, int lane) {
    const char* xsrc[4];
#pragma unroll
    for (int i = 0; i < 4; ++i) {
        const int r = 32 * w + 8 * i + (lane >> 3);
        const int c = (lane & 7) ^ ((r >> 1) & 7);
        xsrc[i] = (const char*)x + (size_t)(rb * 128 + r) * 1024 + c * 16;
    }
    const char* wsrc[3];
#pragma unroll
    for (int i = 0; i < 3; ++i) {
        const int ci = 64 * (3 * w + i) + lane;
        const int r = ci / 12, cp = ci % 12;
        const int c = cp ^ (((r >> 3) & 1) << 1);
        wsrc[i] = (const char*)wqkv + (size_t)r * 3072 + ct * 192 + c * 16;
    }
    auto issue = [&](int c) {
        const int slot = (c % K1_RING) * K1_SLOT;
#pragma unroll
        for (int i = 0; i < 4; ++i) glds16(xsrc[i] + c * 128, smem, slot + (32 * w + 8 * i) * 128);
#pragma unroll
        for (int i = 0; i < 3; ++i) glds16(wsrc[i] + (size_t)c * 64 * 3072, smem, slot + K1_XB + (3 * w + i) * 1024);
    };
    const int m = lane & 15, g = lane >> 4, wq = (lane >> 2) & 3, wp = lane & 3;
    int woff[3];
#pragma unroll
    for (int nt = 0; nt < 3; ++nt)
        woff[nt] = K1_XB + ((w >> 1) * 32 + 8 * g + wq) * 192 + wp * 8 + (((3 * (w & 1) + nt) ^ (g & 1)) << 5);
    const bf16x8 ones = ones8();
    f32x4 cs[3];
#pragma unroll
    for (int nt = 0; nt < 3; ++nt) cs[nt] = f32x4{0, 0, 0, 0};
#pragma unroll
    for (int c = 0; c < K1_RING; ++c) issue(c);
#pragma unroll
    for (int c = 0; c < 8; ++c) {
        if (c == 0) WAIT_V(28);
        else if (c <= 4) WAIT_V(21);
        else if (c == 5) WAIT_V(14);
        else if (c == 6) WAIT_V(7);
        else WAIT_V(0);
        WAIT_L0();
        BAR();
        const int slot = (c % K1_RING) * K1_SLOT;
        bf16x8 wf[3];
#pragma unroll
        for (int nt = 0; nt < 3; ++nt) wf[nt] = lds_read_tr(smem, slot + woff[nt], slot + woff[nt] + 4 * 192);
        SB();
        if (c >= 1 && c - 1 + K1_RING < 8) issue(c - 1 + K1_RING);
        SB();
#pragma unroll
        for (int nt = 0; nt < 3; ++nt) cs[nt] = mfma16(wf[nt], ones, cs[nt]);
        SB();
    }
    if (m == 0) {
#pragma unroll
        for (int nt = 0; nt < 3; ++nt) *(f32x4*)(smem + K1_CSP + ((w >> 1) * 96 + (3 * (w & 1) + nt) * 16 + 4 * g) * 4) = cs[nt];
    }
    WAIT_L0();
    BAR();
    if (w < 2) {
        const int t = w * 64 + lane;
        const float* csp = (const float*)(smem + K1_CSP);
        if (t < 96) ((float*)(smem + K1_CS))[t] = csp[t] + csp[96 + t];
    }
    WAIT_L0();
    BAR();
}

template <int NV>
DEVI void qkv_body(char* __restrict__ ws, char* smem, int rb, int ct, int w, int lane DIAG_ARG) {
    const int m = lane & 15, g = lane >> 4;
    const int wr = w & 1, wc = w >> 1;
    int xoff[4][2];
#pragma unroll
    for (int tt = 0; tt < 4; ++tt)
#pragma unroll
        for (int s = 0; s < 2; ++s) {
            const int r = 64 * wr + 16 * (tt ^ (2 * wc)) + m;
            xoff[tt][s] = r * 128 + (((4 * s + g) ^ ((r >> 1) & 7)) << 4);
        }
    const int wq = (lane >> 2) & 3, wp = lane & 3;
    int woff[3];
#pragma unroll
    for (int nt = 0; nt < 3; ++nt) woff[nt] = K1_XB + (8 * g + wq) * 192 + wp * 8 + (((3 * wc + nt) ^ (g & 1)) << 5);
    f32x4 acc[4][3], ssum[2], ssq[2];
#pragma unroll
    for (int tt = 0; tt < 4; ++tt)
#pragma unroll
        for (int nt = 0; nt < 3; ++nt) acc[tt][nt] = f32x4{0, 0, 0, 0};
#pragma unroll
    for (int tt = 0; tt < 2; ++tt) {
        ssum[tt] = f32x4{0, 0, 0, 0};
        ssq[tt] = f32x4{0, 0, 0, 0};
    }
    const bf16x8 ones = ones8();
    bf16x8 xf[2][4], wf[2][3];
    auto rd = [&](int ks, int n) {
        const int slot = ((ks >> 1) % K1_RING) * K1_SLOT;
        if (n == 0 || (n >= 2 && n <= 4)) {
            const int tt = n == 0 ? 0 : n - 1;
            xf[ks & 1][tt] = lds_read16(smem, slot + xoff[tt][ks & 1]);
        } else {
            const int nt = n == 1 ? 0 : n - 4;
            const int o = slot + woff[nt] + (ks & 1) * 32 * 192;
            wf[ks & 1][nt] = lds_read_tr(smem, o, o + 4 * 192);
        }
    };
    BAR();
    STAMP(1);
#pragma unroll
    for (int n = 0; n < 7; ++n) rd(0, n);
    SB();
#pragma unroll
    for (int ks = 0; ks < 16; ++ks) {
        const int b = ks & 1;
        const int first = b ? 6 : 0;
#pragma unroll
        for (int n = 0; n < 16; ++n) {
            if (b && ks < 15 && n == first) {
                WAIT_L0();
                BAR();
                SB();
            }
            if (n < 12) {
                const int nt = n >> 2, tt = n & 3;
                acc[tt][nt] = (nt >= 3 - NV) ? mfma16(xf[b][tt], wf[b][nt], acc[tt][nt]) : mfma16(wf[b][nt], xf[b][tt], acc[tt][nt]);
            } else if (n < 14) {
                ssum[n - 12] = mfma16(ones, xf[b][n - 12], ssum[n - 12]);
            } else {
                ssq[n - 14] = mfma16(xf[b][n - 14], xf[b][n - 14], ssq[n - 14]);
            }
            if (ks < 15 && n >= first && n < first + 7) rd(ks + 1, n - first);
            SB();
        }
    }
    STAMP(2);
    f32x2* stat = (f32x2*)(smem + K1_STAT);
    const float* csf = (const float*)(smem + K1_CS);
#pragma unroll
    for (int tt = 0; tt < 2; ++tt) {
        const int r = m & 3;
        const float sq = r == 0 ? ssq[tt][0] : r == 1 ? ssq[tt][1] : r == 2 ? ssq[tt][2] : ssq[tt][3];
        const float mean = ssum[tt][0] * (1.0f / D_);
        const float var = fmaxf(sq * (1.0f / D_) - mean * mean, 0.0f);
        if ((m >> 2) == g) stat[64 * wr + 32 * wc + 16 * tt + m] = f32x2{mean, __builtin_amdgcn_rsqf(var + EPS)};
    }
    WAIT_L0();
    BAR();
    BAR();
    STAMP(3);
#pragma unroll
    for (int nt = 0; nt < 3; ++nt) {
        const int ntl = 3 * wc + nt;
        const int col = ct * 96 + ntl * 16;
        const int head = (col & 511) >> 4;
        const bool isv = nt >= 3 - NV;
        const int isk = col >= 512;
        const float sc = (!isv && !isk) ? QSCALE : 1.0f;
        const f32x4 c4 = *(const f32x4*)(csf + ntl * 16 + 4 * g);
        const float c1 = csf[ntl * 16 + m];
        char* base = ws + (isv ? WS_VF : isk ? WS_KF : WS_QF) + head * 1024 +
                     (isv ? lane * 16 : ((lane & 15) + 16 * (lane >> 5) + 32 * ((lane >> 4) & 1)) * 16);
#pragma unroll
        for (int pr = 0; pr < 2; ++pr) {
            unsigned pk[2][2];
#pragma unroll
            for (int u = 0; u < 2; ++u) {
                const int tt = 2 * pr + u;
                const int tok0 = 64 * wr + 16 * (tt ^ (2 * wc));
                float v[4];
                if (!isv) {
                    const f32x2 st = stat[tok0 + m];
                    const float rs = st[1] * sc;
#pragma unroll
                    for (int r = 0; r < 4; ++r) v[r] = (acc[tt][nt][r] - st[0] * c4[r]) * rs;
                } else {
#pragma unroll
                    for (int r = 0; r < 4; ++r) {
                        const f32x2 st = stat[tok0 + 4 * g + r];
                        v[r] = (acc[tt][nt][r] - st[0] * c1) * st[1];
                    }
                }
                pk[u][0] = pack2(v[0], v[1]);
                pk[u][1] = pack2(v[2], v[3]);
            }
            swap32(pk[0][0], pk[1][0]);
            swap32(pk[0][1], pk[1][1]);
            const int tile = rb * 4 + 2 * wr + (pr ^ wc);
            st16(base + (size_t)tile * 32768, u32x4{pk[0][0], pk[0][1], pk[1][0], pk[1][1]});
        }
    }
}

__global__ __launch_bounds__(512, 2) void k1_qkv(const uint16_t* __restrict__ x, const uint16_t* __restrict__ wqkv, char* __restrict__ ws) {
    extern __shared__ __attribute__((aligned(16))) char smem[];
    DIAG_DECL;
    const int lane = threadIdx.x & 63, wv = __builtin_amdgcn_readfirstlane(threadIdx.x >> 6), w = wv & 3;
    const int xcd = blockIdx.x & 7, idx = blockIdx.x >> 3;
    const int rb = K1_RB(xcd, idx), ct = K1_CT(xcd, idx);
    if (wv >= 4) {
        qkv_loader(x, wqkv, smem, rb, ct, w, lane);
        return;
    }
    if (ct >= 11) qkv_body<3>(ws, smem, rb, ct, w, lane DIAG_PASS);
    else if (ct == 10 && w >= 2) qkv_body<2>(ws, smem, rb, ct, w, lane DIAG_PASS);
    else qkv_body<0>(ws, smem, rb, ct, w, lane DIAG_PASS);
    DIAG_END(ws, 0);
}

constexpr int K2_VS = 2064;
constexpr int K2_K = 0, K2_V = 24576, K2_JOB = K2_V + 12 * K2_VS + 192, K2_RED = K2_JOB + 32768;
constexpr int K2_LDS = K2_RED + 8 * 128 * 4;

DEVI f32x16 splat16(float v) {
    f32x16 r;
#pragma unroll
    for (int i = 0; i < 16; ++i) r[i] = v;
    return r;
}
constexpr unsigned long long row_mask(int r) {
    unsigned long long m = 0;
    for (int l = 0; l < 64; ++l)
        if ((r & 3) + 8 * (r >> 2) + 4 * (l >> 5) > (l & 31)) m |= 1ull << l;
    return m;
}
template <int R>
DEVI unsigned sel_row(unsigned no, unsigned yes) { return __builtin_amdgcn_inverse_ballot_w64(row_mask(R)) ? yes : no; }
template <int R>
DEVI void merge_rows(const f32x16& sf, const f32x16& sl, f32x16& mg) {
    if constexpr (R < 16) {
        const float no = sl[R], yes = sf[R];
        mg[R] = __builtin_amdgcn_inverse_ballot_w64(row_mask(R)) ? yes : no;
        merge_rows<R + 1>(sf, sl, mg);
    }
}
DEVI f32x16 attn_merge(const f32x16& sf, const f32x16& sl) {
    f32x16 mg;
    merge_rows<0>(sf, sl, mg);
    return mg;
}
template <int R>
DEVI void split_rows(const f32x16& mg, float mx, unsigned (&pf)[8], unsigned (&pl)[8]) {
    if constexpr (R < 16) {
        const unsigned pk = pack2(__builtin_amdgcn_exp2f(mg[R] - mx), __builtin_amdgcn_exp2f(mg[R + 1] - mx));
        const unsigned mk = sel_row<R>(sel_row<R + 1>(0u, 0xffff0000u), 0xffffffffu);
        pf[R >> 1] = pk & mk;
        pl[R >> 1] = pk & ~mk;
        split_rows<R + 2>(mg, mx, pf, pl);
    }
}
DEVI void attn_split(const f32x16& mg, float mx, unsigned (&pf)[8], unsigned (&pl)[8]) { split_rows<0>(mg, mx, pf, pl); }
DEVI bf16x8 as_frag(const unsigned* p) { return __builtin_bit_cast(bf16x8, u32x4{p[0], p[1], p[2], p[3]}); }

template <bool FULL>
DEVI f32x16 attn_exact(char* smem, bf16x8 q, int koff, int vb0, int vb1, int i, int umin) {
    const float NEG = -__builtin_inff();
    const f32x16 zero = {0};
    const f32x16 sl = mfma32(lds_read16(smem, koff + 8 * 2048), q, zero);
    f32x16 sf = splat16(NEG);
    if (FULL || i >= umin) sf = mfma32(lds_read16(smem, koff), q, zero);
    const f32x16 mg = attn_merge(sf, sl);
    float mx = fmaxf(mg[0], mg[1]);
#pragma unroll
    for (int r = 2; r < 16; r += 2) mx = fmaxf(fmaxf(mx, mg[r]), mg[r + 1]);
#pragma unroll 1
    for (int t = 1; t < 8; ++t) {
        if (FULL || i + t >= umin) {
            const f32x16 s = mfma32(lds_read16(smem, koff + t * 2048), q, zero);
#pragma unroll
            for (int r = 0; r < 16; r += 2) mx = fmaxf(fmaxf(mx, s[r]), s[r + 1]);
        }
    }
    mx = swap32_max(mx);
    const f32x16 negm = splat16(-mx);
    f32x16 o = zero;
    unsigned pf[8], pl[8];
    attn_split(mg, mx, pf, pl);
#pragma unroll
    for (int s = 0; s < 2; ++s) {
        o = mfma32(lds_read16(smem, (s ? vb1 : vb0) + 8 * K2_VS), as_frag(pl + 4 * s), o);
        if (FULL || i >= umin) o = mfma32(lds_read16(smem, s ? vb1 : vb0), as_frag(pf + 4 * s), o);
    }
#pragma unroll 1
    for (int t = 1; t < 8; ++t) {
        if (FULL || i + t >= umin) {
            const f32x16 s = mfma32(lds_read16(smem, koff + t * 2048), q, negm);
            unsigned pk[8];
#pragma unroll
            for (int r = 0; r < 16; r += 2) pk[r >> 1] = pack2(__builtin_amdgcn_exp2f(s[r]), __builtin_amdgcn_exp2f(s[r + 1]));
            o = mfma32(lds_read16(smem, vb0 + t * K2_VS), as_frag(pk), o);
            o = mfma32(lds_read16(smem, vb1 + t * K2_VS), as_frag(pk + 4), o);
        }
    }
    return o;
}

template <bool FULL, class Mid>
DEVI f32x16 attn_fast(char* smem, bf16x8 q, int koff, int vb0, int vb1, int i, int umin, Mid mid DIAG_ARG) {
    const float NEG = -__builtin_inff();
    const f32x16 zero = {0};
    auto cinit = [&](int t) { return FULL ? zero : splat16(i + t >= umin ? 0.0f : NEG); };
    bf16x8 kf[2], vf[2][2];
    const bf16x8 kd = lds_read16(smem, koff + 8 * 2048), k0 = lds_read16(smem, koff);
    kf[1] = lds_read16(smem, koff + 2048);
    const bf16x8 vd0 = lds_read16(smem, vb0 + 8 * K2_VS), vz0 = lds_read16(smem, vb0);
    const bf16x8 vd1 = lds_read16(smem, vb1 + 8 * K2_VS), vz1 = lds_read16(smem, vb1);
    const f32x16 sl = mfma32(kd, q, zero);
    const f32x16 sf = mfma32(k0, q, cinit(0));
    f32x16 sc[2];
    sc[1] = mfma32(kf[1], q, cinit(1));
    kf[0] = lds_read16(smem, koff + 2 * 2048);
    vf[1][0] = lds_read16(smem, vb0 + K2_VS);
    vf[1][1] = lds_read16(smem, vb1 + K2_VS);
    SB();
    f32x16 o = zero;
    {
        unsigned pf[8], pl[8];
        attn_split(attn_merge(sf, sl), 0.0f, pf, pl);
        o = mfma32(vd0, as_frag(pl), o);
        o = mfma32(vz0, as_frag(pf), o);
        o = mfma32(vd1, as_frag(pl + 4), o);
        o = mfma32(vz1, as_frag(pf + 4), o);
    }
    SB();
    STAMP(2);
#pragma unroll
    for (int t = 1; t < 8; ++t) {
        const int b = t & 1;
        if (t < 7) {
            sc[b ^ 1] = mfma32(kf[b ^ 1], q, cinit(t + 1));
            if (t < 6) kf[b] = lds_read16(smem, koff + (t + 2) * 2048);
            vf[b ^ 1][0] = lds_read16(smem, vb0 + (t + 1) * K2_VS);
            vf[b ^ 1][1] = lds_read16(smem, vb1 + (t + 1) * K2_VS);
        }
        SB();
        unsigned pk[8];
#pragma unroll
        for (int r = 0; r < 8; r += 2) pk[r >> 1] = pack2(__builtin_amdgcn_exp2f(sc[b][r]), __builtin_amdgcn_exp2f(sc[b][r + 1]));
        const float e8 = __builtin_amdgcn_exp2f(sc[b][8]), e9 = __builtin_amdgcn_exp2f(sc[b][9]);
        SB();
        o = mfma32(vf[b][0], as_frag(pk), o);
        SB();
        pk[4] = pack2(e8, e9);
#pragma unroll
        for (int r = 10; r < 16; r += 2) pk[r >> 1] = pack2(__builtin_amdgcn_exp2f(sc[b][r]), __builtin_amdgcn_exp2f(sc[b][r + 1]));
        SB();
        o = mfma32(vf[b][1], as_frag(pk + 4), o);
        SB();
        if (t == 5) {
            STAMP(3);
            mid();
            STAMP(4);
        }
    }
    return o;
}

template <bool FULL, class Mid>
DEVI void attn_task(char* smem, char* __restrict__ ws, bf16x8 q, int rb, int i, int e, int p, int lane, Mid mid DIAG_ARG) {
    const int a = lane & 31, h = lane >> 5;
    const int koff = K2_K + e * 1024 + i * 2048 + lane * 16;
    const bool isdata = a < 16;
    const int vb0 = K2_V + i * K2_VS + (isdata ? e * 1024 + h * 256 + a * 16 : 2048);
    const int vb1 = vb0 + (isdata ? 512 : 0);
    const int umin = FULL ? 0 : 8 - 4 * rb;
    f32x16 o = attn_fast<FULL>(smem, q, koff, vb0, vb1, i, umin, mid DIAG_PASS);
    float mag = 0.f;
#pragma unroll
    for (int r = 0; r < 8; ++r) mag += __builtin_fabsf(o[r]);
    const bool safe = o[8] >= 0x1p-60f && o[8] <= 0x1p60f && mag <= 0x1p120f;
    if (__builtin_amdgcn_ballot_w64(!safe) != 0) {
        o = attn_exact<FULL>(smem, q, koff, vb0, vb1, i, umin);
    }
    const float inv = __builtin_amdgcn_rcpf(o[8]);
    const u32x4 ov = {pack2(o[0] * inv, o[1] * inv), pack2(o[2] * inv, o[3] * inv), pack2(o[4] * inv, o[5] * inv), pack2(o[6] * inv, o[7] * inv)};
    st16(ws + WS_OF + (size_t)((rb * 4 + i) * 32 + 2 * p + e) * 1024 + lane * 16, ov);
}

__global__ __launch_bounds__(512, 2) void k2_attn(const uint16_t* __restrict__ wo, const uint16_t* __restrict__ wg, const uint16_t* __restrict__ wu,
                                                  const uint16_t* __restrict__ wd, char* __restrict__ ws) {
    extern __shared__ __attribute__((aligned(16))) char smem[];
    DIAG_DECL;
    const int tid = threadIdx.x, lane = tid & 63, w = __builtin_amdgcn_readfirstlane(tid >> 6);
    const int xcd = blockIdx.x & 7, idx = blockIdx.x >> 3;
    const int rb = 2 * xcd + (idx >> 4), p = idx & 15;
    const int i = w & 3, e = w >> 2;
#pragma unroll
    for (int n = 0; n < 6; ++n) {
        const int pc = 6 * w + n;
        const int isv = pc >= 24, u = (pc % 24) >> 1, half = pc & 1;
        int kt = 4 * rb - 8 + u;
        kt = kt < 0 ? 0 : kt;
        const char* src = ws + (isv ? WS_VF : WS_KF) + (size_t)(kt * 16 + p) * 2048 + half * 1024 + lane * 16;
        glds16(src, smem, (isv ? K2_V + u * K2_VS : K2_K + u * 2048) + half * 1024);
    }
    const bf16x8 q = *(const bf16x8*)(ws + WS_QF + (size_t)((rb * 4 + i) * 32 + 2 * p + e) * 1024 + lane * 16);
    if (tid < 48) *(unsigned*)(smem + K2_V + (tid >> 2) * K2_VS + 2048 + (tid & 3) * 4) = 0x3F803F80u;
    asm volatile("" ::"v"(q));
    WAIT_V(0);
    WAIT_L0();
    BAR();
    STAMP(1);
    const int bid = blockIdx.x;
    const int job = K2_JOB + w * 4096;
    if (bid < 112) {
        const int cg = bid % 28, s = 8 * (bid / 28) + w;
#pragma unroll
        for (int n = 0; n < 4; ++n) {
            const uint16_t* src = (n >> 1) ? wu : wg;
            const int row = 16 * s + 8 * (n & 1) + (lane >> 3);
            glds16((const char*)src + (size_t)row * (F_ * 2) + cg * 128 + (lane & 7) * 16, smem, job + n * 1024);
        }
    } else {
        const int jj = (bid - 112) * 8 + w;
        const bool iso = jj < 256;
        const int j2 = iso ? jj : jj - 256;
        const uint16_t* src = iso ? wo : wd;
        const int fp = j2 & 7, s = j2 >> 3;
#pragma unroll
        for (int n = 0; n < 2; ++n) {
            const int row = 16 * s + 8 * n + (lane >> 3);
            glds16((const char*)src + (size_t)row * (D_ * 2) + fp * 128 + (lane & 7) * 16, smem, job + n * 1024);
        }
    }
    auto retile = [&]() {
        WAIT_V(0);
        const int G = lane >> 4, nh = G & 1, hh = G >> 1, rq = (lane >> 2) & 3, pp = lane & 3;
        if (bid < 112) {
            const int cg = bid % 28, kq = bid / 28, s = 8 * kq + w;
            float* red = (float*)(smem + K2_RED);
#pragma unroll
            for (int t = 0; t < 4; ++t) {
                const int o = job + nh * 2048 + (4 * hh + rq) * 128 + t * 32 + pp * 8;
                const bf16x8 f = lds_read_tr(smem, o, o + 8 * 128);
                const int T = 4 * cg + t, j = T / 7, t7 = T % 7;
                const u32x4 u = __builtin_bit_cast(u32x4, f);
                st16(ws + WS_WGU + (size_t)((j * 32 + s) * 7 + t7) * 1024 + lane * 16, u);
                float sum = 0.f;
#pragma unroll
                for (int r = 0; r < 4; ++r) sum += bf_lo(u[r]) + bf_hi(u[r]);
                sum = swap32_add(sum);
                if (lane < 32) red[w * 128 + nh * 64 + t * 16 + (lane & 15)] = sum;
            }
        } else {
            const int jj = (bid - 112) * 8 + w;
            const bool iso = jj < 256;
            const int j2 = iso ? jj : jj - 256;
            const int fp = j2 & 7, s = j2 >> 3;
            char* dst = ws + (iso ? WS_WOT + (size_t)(fp * 32 + s) * 2048 : WS_WDT + (size_t)(fp * 112 + s) * 2048) + lane * 16;
#pragma unroll
            for (int t = 0; t < 2; ++t) {
                const int o = job + (4 * hh + rq) * 128 + t * 64 + nh * 32 + pp * 8;
                st16(dst + t * 1024, __builtin_bit_cast(u32x4, lds_read_tr(smem, o, o + 8 * 128)));
            }
        }
        SB();
    };
    if (rb >= 2) attn_task<true>(smem, ws, q, rb, i, e, p, lane, retile DIAG_PASS);
    else attn_task<false>(smem, ws, q, rb, i, e, p, lane, retile DIAG_PASS);
    STAMP(5);
    if (bid < 112) {
        const int cg = bid % 28, kq = bid / 28;
        const float* red = (const float*)(smem + K2_RED);
        WAIT_L0();
        BAR();
        if (tid < 128) {
            float sum = 0.f;
#pragma unroll
            for (int ww = 0; ww < 8; ++ww) sum += red[ww * 128 + tid];
            ((float*)(ws + WS_CSP))[(kq * 2 + (tid >> 6)) * F_ + cg * 64 + (tid & 63)] = sum;
        }
    }
    DIAG_END(ws, 1);
}

constexpr int G64_RED = 0, G64_T = 49152, G64_LDS = G64_T + 8192;

template <int NS, int DEPTH, class Pre>
DEVI f32x16 gemm64(char* smem, const char* wt, const char* act, int act_tile_stride, int w, int lane, Pre pre DIAG_ARG) {
    const char* pa[2];
    const char* pb[2];
#pragma unroll
    for (int q = 0; q < 2; ++q) {
        pa[q] = wt + (size_t)(w * NS) * 2048 + (q ^ (w >> 1)) * 1024 + lane * 16;
        pb[q] = act + (size_t)(q ^ (w & 1)) * act_tile_stride + (size_t)(w * NS) * 1024 + lane * 16;
    }
    bf16x8 fa[NS][2], fb[NS][2];
    auto load = [&](int s) {
#pragma unroll
        for (int q = 0; q < 2; ++q) {
            fa[s][q] = *(const bf16x8*)(pa[q] + s * 2048);
            fb[s][q] = *(const bf16x8*)(pb[q] + s * 1024);
        }
    };
#pragma unroll
    for (int s = 0; s < DEPTH; ++s) load(s);
    SB();
    f32x16 acc[4];
#pragma unroll
    for (int q = 0; q < 4; ++q) acc[q] = f32x16{0};
#pragma unroll
    for (int s = 0; s < NS; ++s) {
        if (s + DEPTH < NS) load(s + DEPTH);
        SB();
#pragma unroll
        for (int q = 0; q < 4; ++q) acc[q] = mfma32(fa[s][q >> 1], fb[s][q & 1], acc[q]);
        SB();
        if (s == 0) STAMP(1);
    }
    STAMP(2);
    pre();
#pragma unroll
    for (int q = 1; q < 4; ++q) {
        char* dst = smem + G64_RED + ((w ^ q) * 3 + q - 1) * 4096 + lane * 16;
#pragma unroll
        for (int r = 0; r < 4; ++r)
            *(f32x4*)(dst + r * 1024) = f32x4{acc[q][4 * r], acc[q][4 * r + 1], acc[q][4 * r + 2], acc[q][4 * r + 3]};
    }
    WAIT_L0();
    BAR();
    f32x16 o = acc[0];
#pragma unroll
    for (int q = 0; q < 3; ++q) {
        const char* src = smem + G64_RED + (w * 3 + q) * 4096 + lane * 16;
#pragma unroll
        for (int r = 0; r < 4; ++r) {
            const f32x4 v = *(const f32x4*)(src + r * 1024);
#pragma unroll
            for (int k = 0; k < 4; ++k) o[4 * r + k] += v[k];
        }
    }
    STAMP(3);
    return o;
}

__global__ __launch_bounds__(256, 1) void k3_oproj(const uint16_t* __restrict__ x, char* __restrict__ ws) {
    extern __shared__ __attribute__((aligned(16))) char smem[];
    DIAG_DECL;
    const int tid = threadIdx.x, lane = tid & 63, w = __builtin_amdgcn_readfirstlane(tid >> 6);
    const int xcd = blockIdx.x & 7, idx = blockIdx.x >> 3;
    const int tb = 4 * xcd + (idx >> 3), fp = idx & 7;
    const int tt = w & 1, ft = w >> 1;
    u32x4 xr[2];
#pragma unroll
    for (int n = 0; n < 2; ++n) {
        const int r = (w * 2 + n) * 8 + (lane >> 3);
        xr[n] = *(const u32x4*)((const char*)x + (size_t)(tb * 64 + r) * 1024 + fp * 128 + (lane & 7) * 16);
    }
    const char* wt = ws + WS_WOT + (size_t)fp * 65536;
    const char* act = ws + WS_OF + (size_t)tb * 65536;
    auto stage_x = [&]() {
#pragma unroll
        for (int n = 0; n < 2; ++n) {
            const int r = (w * 2 + n) * 8 + (lane >> 3);
            *(u32x4*)(smem + G64_T + r * 128 + (((lane & 7) ^ ((r >> 1) & 7)) << 4)) = xr[n];
        }
    };
    const f32x16 acc = gemm64<8, 8>(smem, wt, act, 32768, w, lane, stage_x DIAG_PASS);
    const int tok = lane & 31, h = lane >> 5;
    const int r = tt * 32 + tok;
    unsigned pk[8];
    float sum = 0.f, sq = 0.f;
#pragma unroll
    for (int qd = 0; qd < 4; ++qd) {
        const u32x2 xv = *(const u32x2*)(smem + G64_T + r * 128 + (((ft * 4 + qd) ^ ((r >> 1) & 7)) << 4) + h * 8);
        pk[2 * qd] = pack2(acc[4 * qd] + bf_lo(xv[0]), acc[4 * qd + 1] + bf_hi(xv[0]));
        pk[2 * qd + 1] = pack2(acc[4 * qd + 2] + bf_lo(xv[1]), acc[4 * qd + 3] + bf_hi(xv[1]));
#pragma unroll
        for (int k = 0; k < 2; ++k) {
            const float lo = bf_lo(pk[2 * qd + k]), hi = bf_hi(pk[2 * qd + k]);
            sum += lo + hi;
            sq += lo * lo + hi * hi;
        }
    }
    char* hdst = ws + WS_HF + (size_t)((tb * 2 + tt) * 32 + (fp * 2 + ft) * 2) * 1024 + lane * 16;
    st16(hdst, u32x4{pk[0], pk[1], pk[2], pk[3]});
    st16(hdst + 1024, u32x4{pk[4], pk[5], pk[6], pk[7]});
    sum = swap32_add(sum);
    sq = swap32_add(sq);
    if (h == 0) ((f32x2*)(ws + WS_ST2))[(fp * 2 + ft) * S_ + tb * 64 + r] = f32x2{sum, sq};
    DIAG_END(ws, 2);
}

constexpr int K4_A = 28672, K4_SLOT = 45056, K4_RING = 3, K4_CS = K4_SLOT * K4_RING, K4_LDS = K4_CS + 224 * 4;

__global__ __launch_bounds__(512, 2) void k4_gateup(char* __restrict__ ws) {
    extern __shared__ __attribute__((aligned(16))) char smem[];
    DIAG_DECL;
    const int tid = threadIdx.x, lane = tid & 63, wv = __builtin_amdgcn_readfirstlane(tid >> 6), w = wv & 3;
    const int xcd = blockIdx.x & 7, idx = blockIdx.x >> 3;
    const int rb = K4_RB(xcd, idx), j = K4_J(xcd, idx);
    const int tile = rb * 4 + w;
    if (wv >= 4) {
        const char* asrc = ws + WS_WGU + (size_t)j * (32 * 7168) + w * 7168 + lane * 16;
        const char* bsrc = ws + WS_HF + (size_t)tile * 32768 + lane * 16;
        auto issue = [&](int c) {
            const int slot = (c % K4_RING) * K4_SLOT;
#pragma unroll
            for (int n = 0; n < 4; ++n) glds16(bsrc + (c * 4 + n) * 1024, smem, slot + K4_A + (w * 4 + n) * 1024);
#pragma unroll
            for (int n = 0; n < 7; ++n) glds16(asrc + c * 28672 + n * 1024, smem, slot + w * 7168 + n * 1024);
        };
#pragma unroll
        for (int c = 0; c < K4_RING; ++c) issue(c);
#pragma unroll
        for (int c = 0; c < 8; ++c) {
            if (c == 0) WAIT_V(22);
            else if (c < 7) WAIT_V(11);
            else WAIT_V(0);
            BAR();
            if (c >= 1 && c - 1 + K4_RING < 8) issue(c - 1 + K4_RING);
        }
        return;
    }
    const int tok = lane & 31, h = lane >> 5;
    f32x2 st[8];
#pragma unroll
    for (int n = 0; n < 8; ++n) st[n] = ((const f32x2*)(ws + WS_ST2))[(h * 8 + n) * S_ + tile * 32 + tok];
    {
        const int t = tid < 224 ? tid : 0;
        const int gu = t >= 112, col = j * 112 + (gu ? t - 112 : t);
        float csq[4];
#pragma unroll
        for (int n = 0; n < 4; ++n) csq[n] = ((const float*)(ws + WS_CSP))[(n * 2 + gu) * F_ + col];
        if (tid < 224) ((float*)(smem + K4_CS))[tid] = (csq[0] + csq[1]) + (csq[2] + csq[3]);
    }
    float sum = 0.f, sq = 0.f;
#pragma unroll
    for (int n = 0; n < 8; ++n) {
        sum += st[n][0];
        sq += st[n][1];
    }
    sum = swap32_add(sum);
    sq = swap32_add(sq);
    const float mean = sum * (1.0f / D_);
    const float rstd = __builtin_amdgcn_rsqf(fmaxf(sq * (1.0f / D_) - mean * mean, 0.0f) + EPS);
    f32x16 acc[7];
#pragma unroll
    for (int t = 0; t < 7; ++t) acc[t] = f32x16{0};
    bf16x8 af[2][7], bfr[2];
    auto rdB = [&](int s) {
        bfr[s & 1] = lds_read16(smem, ((s >> 2) % K4_RING) * K4_SLOT + K4_A + (w * 4 + (s & 3)) * 1024 + lane * 16);
    };
    auto rdA = [&](int s, int t) {
        af[s & 1][t] = lds_read16(smem, ((s >> 2) % K4_RING) * K4_SLOT + ((s & 3) * 7 + t) * 1024 + lane * 16);
    };
    WAIT_L0();
    BAR();
    STAMP(1);
    rdB(0);
#pragma unroll
    for (int t = 0; t < 7; ++t) rdA(0, t);
    SB();
#pragma unroll
    for (int s = 0; s < 32; ++s) {
        const int b = s & 1;
        const bool edge = (s & 3) == 3;
#pragma unroll
        for (int t = 0; t < 7; ++t) {
            if (edge && s < 31 && t == 4) {
                WAIT_L0();
                BAR();
                SB();
            }
            acc[t] = mfma32(af[b][t], bfr[b], acc[t]);
            if (s < 31) {
                if (!edge) {
                    if (t == 0) rdB(s + 1);
                    rdA(s + 1, t);
                } else if (t == 4) {
                    rdB(s + 1);
                    rdA(s + 1, 0);
                    rdA(s + 1, 1);
                } else if (t == 5) {
                    rdA(s + 1, 2);
                    rdA(s + 1, 3);
                    rdA(s + 1, 4);
                } else if (t == 6) {
                    rdA(s + 1, 5);
                    rdA(s + 1, 6);
                }
            }
            SB();
        }
    }
    STAMP(2);
    const float* cs = (const float*)(smem + K4_CS);
    char* dst = ws + WS_MF + (size_t)(tile * 112 + j * 7) * 1024 + lane * 16;
#pragma unroll
    for (int t = 0; t < 7; ++t) {
        float v[8];
#pragma unroll
        for (int qd = 0; qd < 2; ++qd) {
            const f32x4 cg = *(const f32x4*)(cs + t * 16 + 8 * qd + 4 * h);
            const f32x4 cu = *(const f32x4*)(cs + 112 + t * 16 + 8 * qd + 4 * h);
#pragma unroll
            for (int r = 0; r < 4; ++r) {
                const float gt = (acc[t][4 * qd + r] - mean * cg[r]) * rstd;
                const float up = (acc[t][8 + 4 * qd + r] - mean * cu[r]) * rstd;
                v[4 * qd + r] = fmaxf(gt, 0.0f) * up;
            }
        }
        st16(dst + t * 1024, u32x4{pack2(v[0], v[1]), pack2(v[2], v[3]), pack2(v[4], v[5]), pack2(v[6], v[7])});
    }
    DIAG_END(ws, 3);
}

__global__ __launch_bounds__(256, 1) void k5_down(char* __restrict__ ws, uint16_t* __restrict__ out) {
    extern __shared__ __attribute__((aligned(16))) char smem[];
    DIAG_DECL;
    const int tid = threadIdx.x, lane = tid & 63, w = __builtin_amdgcn_readfirstlane(tid >> 6);
    const int xcd = blockIdx.x & 7, idx = blockIdx.x >> 3;
    const int tb = 4 * xcd + (idx >> 3), fp = idx & 7;
    const int tt = w & 1, ft = w >> 1;
    const char* hsrc = ws + WS_HF + (size_t)((tb * 2 + tt) * 32 + (fp * 2 + ft) * 2) * 1024 + lane * 16;
    const u32x4 h0 = *(const u32x4*)hsrc, h1 = *(const u32x4*)(hsrc + 1024);
    const char* wt = ws + WS_WDT + (size_t)fp * (112 * 2048);
    const char* act = ws + WS_MF + (size_t)(tb * 2) * (112 * 1024);
    const f32x16 acc = gemm64<28, K5_DEPTH>(smem, wt, act, 112 * 1024, w, lane, [] {} DIAG_PASS);
    const int tok = lane & 31, h = lane >> 5;
    const int r = tt * 32 + tok;
    const unsigned hv[8] = {h0[0], h0[1], h0[2], h0[3], h1[0], h1[1], h1[2], h1[3]};
#pragma unroll
    for (int qd = 0; qd < 4; ++qd) {
        const u32x2 v = {pack2(acc[4 * qd] + bf_lo(hv[2 * qd]), acc[4 * qd + 1] + bf_hi(hv[2 * qd])),
                         pack2(acc[4 * qd + 2] + bf_lo(hv[2 * qd + 1]), acc[4 * qd + 3] + bf_hi(hv[2 * qd + 1]))};
        *(u32x2*)(smem + G64_T + r * 128 + (((ft * 4 + qd) ^ ((r >> 1) & 7)) << 4) + h * 8) = v;
    }
    WAIT_L0();
    BAR();
#pragma unroll
    for (int n = 0; n < 2; ++n) {
        const int rr = (w * 2 + n) * 8 + (lane >> 3), cp = lane & 7;
        const u32x4 v = *(const u32x4*)(smem + G64_T + rr * 128 + cp * 16);
        *(u32x4*)((char*)out + (size_t)(tb * 64 + rr) * 1024 + fp * 128 + ((cp ^ ((rr >> 1) & 7)) << 4)) = v;
    }
    DIAG_END(ws, 4);
}

extern "C" void kernel_launch(void* const* d_in, const int* in_sizes, int n_in, void* d_out, int out_size, void* d_ws,
                              size_t ws_size, hipStream_t stream) {
    const uint16_t* x = (const uint16_t*)d_in[0];
    const uint16_t* wqkv = (const uint16_t*)d_in[1];
    const uint16_t* wo = (const uint16_t*)d_in[2];
    const uint16_t* wg = (const uint16_t*)d_in[4];
    const uint16_t* wu = (const uint16_t*)d_in[5];
    const uint16_t* wd = (const uint16_t*)d_in[6];
    char* ws = (char*)d_ws;
    (void)hipFuncSetAttribute((const void*)k1_qkv, hipFuncAttributeMaxDynamicSharedMemorySize, K1_LDS);
    (void)hipFuncSetAttribute((const void*)k2_attn, hipFuncAttributeMaxDynamicSharedMemorySize, K2_LDS);
    (void)hipFuncSetAttribute((const void*)k3_oproj, hipFuncAttributeMaxDynamicSharedMemorySize, G64_LDS);
    (void)hipFuncSetAttribute((const void*)k4_gateup, hipFuncAttributeMaxDynamicSharedMemorySize, K4_LDS);
    (void)hipFuncSetAttribute((const void*)k5_down, hipFuncAttributeMaxDynamicSharedMemorySize, G64_LDS);
    k1_qkv<<<256, 512, K1_LDS, stream>>>(x, wqkv, ws);
    k2_attn<<<256, 512, K2_LDS, stream>>>(wo, wg, wu, wd, ws);
    k3_oproj<<<256, 256, G64_LDS, stream>>>(x, ws);
    k4_gateup<<<256, 512, K4_LDS, stream>>>(ws);
    k5_down<<<256, 256, G64_LDS, stream>>>(ws, (uint16_t*)d_out);
}
```

```cpp
#include <hip/hip_runtime.h>
#include <hip/hip_bf16.h>
#include <stdint.h>

typedef __attribute__((ext_vector_type(8))) __bf16 bf16x8;
typedef __attribute__((ext_vector_type(2))) __bf16 bf16x2;
typedef __attribute__((ext_vector_type(4))) short s16x4;
typedef __attribute__((ext_vector_type(8))) short s16x8;
typedef __attribute__((ext_vector_type(2))) float f32x2;
typedef __attribute__((ext_vector_type(4))) float f32x4;
typedef __attribute__((ext_vector_type(16))) float f32x16;
typedef __attribute__((ext_vector_type(2))) unsigned u32x2;
typedef __attribute__((ext_vector_type(4))) unsigned u32x4;
typedef __attribute__((address_space(3))) char* lds_ptr;
typedef const __attribute__((address_space(1))) void* gptr_t;

#define DEVI __device__ __forceinline__
#define WAIT_V(n) asm volatile("s_waitcnt vmcnt(" #n ")" ::: "memory")
#define WAIT_L0() asm volatile("s_waitcnt lgkmcnt(0)" ::: "memory")
#define BAR() __builtin_amdgcn_s_barrier()
#define SB() __builtin_amdgcn_sched_barrier(0)

#define DIAG_DECL do { } while (0)
#define STAMP(ph) do { } while (0)
#define DIAG_END(ws, kid) do { } while (0)
#define DIAG_ARG
#define DIAG_PASS

constexpr int S_ = 2048, D_ = 512, F_ = 1792;
constexpr float EPS = 1e-5f;
constexpr float QSCALE = 0.25f * 1.44269504088896340736f;

constexpr size_t WS_QF = 0;
constexpr size_t WS_KF = WS_QF + (2u << 20);
constexpr size_t WS_VF = WS_KF + (2u << 20);
constexpr size_t WS_OF = WS_VF + (2u << 20);
constexpr size_t WS_HF = WS_OF + (2u << 20);
constexpr size_t WS_MF = WS_HF + (2u << 20);
constexpr size_t WS_WOT = WS_MF + (7u << 20);
constexpr size_t WS_WGU = WS_WOT + (512u << 10);
constexpr size_t WS_WDT = WS_WGU + (3584u << 10);
constexpr size_t WS_ST2 = WS_WDT + (1792u << 10);
constexpr size_t WS_CSP = WS_ST2 + (256u << 10);
constexpr size_t WS_DIAG = WS_CSP + (64u << 10);

DEVI void glds16(const void* g, char* smem_base, int lds_off) {
    __builtin_amdgcn_global_load_lds((gptr_t)g, (__attribute__((address_space(3))) void*)((lds_ptr)smem_base + lds_off), 16, 0, 0);
}
DEVI bf16x8 lds_read16(char* smem, int off) { return *(const bf16x8*)(smem + off); }
DEVI bf16x8 lds_read_tr(char* smem, int off0, int off1) {
    s16x4 a = __builtin_amdgcn_ds_read_tr16_b64_v4i16((__attribute__((address_space(3))) s16x4*)((lds_ptr)smem + off0));
    s16x4 b = __builtin_amdgcn_ds_read_tr16_b64_v4i16((__attribute__((address_space(3))) s16x4*)((lds_ptr)smem + off1));
    s16x8 c = __builtin_shufflevector(a, b, 0, 1, 2, 3, 4, 5, 6, 7);
    return __builtin_bit_cast(bf16x8, c);
}
#define K4_XA 2
#define K4_RB(xcd, idx) (((xcd) % K4_XA) * (16 / K4_XA) + ((idx) % (16 / K4_XA)))
#define K4_J(xcd, idx) (((xcd) / K4_XA) * (2 * K4_XA) + ((idx) / (16 / K4_XA)))
#define K1_XA 4
#define K1_RB(xcd, idx) (((xcd) % K1_XA) * (16 / K1_XA) + ((idx) % (16 / K1_XA)))
#define K1_CT(xcd, idx) (((xcd) / K1_XA) * (2 * K1_XA) + ((idx) / (16 / K1_XA)))
#define K1_WARM_AT 3
#define K5_DEPTH 6
DEVI void st16(void* p, u32x4 v) {
    asm volatile("global_store_dwordx4 %0, %1, off sc1\n\ts_nop 1" ::"v"(p), "v"(v) : "memory");
}
DEVI void touch_lines(const char* base, int nbytes, int lane, int scratch) {
    for (int off = 0; off < nbytes; off += 8192) {
        const int o = min(off + lane * 128, nbytes - 4);
        int keep;
        asm volatile("s_mov_b32 %0, m0\n\ts_mov_b32 m0, %2\n\ts_nop 0\n\tglobal_load_lds_dword %1, off\n\ts_mov_b32 m0, %0"
                     : "=&s"(keep) : "v"(base + o), "s"(scratch) : "memory");
    }
}
#define CODE_MARK(name) asm volatile(#name ":")
#define TOUCH_CODE(from, to, lane, scratch) do { \
    unsigned long long pc_; int d0_, d1_; \
    asm volatile("s_getpc_b64 %0\n.Lpc%=:\n\ts_mov_b32 %1, " #from "-.Lpc%=\n\ts_mov_b32 %2, " #to "-.Lpc%=" : "=&s"(pc_), "=&s"(d0_), "=&s"(d1_)); \
    const int n_ = d1_ - d0_; \
    if (n_ > 0 && n_ <= 65536) touch_lines((const char*)pc_ + d0_, n_ & ~3, lane, scratch); \
} while (0)
DEVI unsigned pack2(float a, float b) { return __builtin_bit_cast(unsigned, __builtin_convertvector(f32x2{a, b}, bf16x2)); }
DEVI float bf_lo(unsigned u) { return __builtin_bit_cast(float, u << 16); }
DEVI float bf_hi(unsigned u) { return __builtin_bit_cast(float, u & 0xffff0000u); }
DEVI float swap32_add(float v) {
    unsigned a = __builtin_bit_cast(unsigned, v), b = a;
    asm volatile("" : "+v"(b));
    auto r = __builtin_amdgcn_permlane32_swap(a, b, false, false);
    const unsigned r0 = r[0], r1 = r[1];
    return __builtin_bit_cast(float, r0) + __builtin_bit_cast(float, r1);
}
DEVI void swap32(unsigned& a, unsigned& b) {
    auto r = __builtin_amdgcn_permlane32_swap(a, b, false, false);
    const unsigned r0 = r[0], r1 = r[1];
    a = r0;
    b = r1;
}
DEVI float swap32_max(float v) {
    unsigned a = __builtin_bit_cast(unsigned, v), b = a;
    asm volatile("" : "+v"(b));
    auto r = __builtin_amdgcn_permlane32_swap(a, b, false, false);
    const unsigned r0 = r[0], r1 = r[1];
    return fmaxf(__builtin_bit_cast(float, r0), __builtin_bit_cast(float, r1));
}
DEVI f32x4 mfma16(bf16x8 a, bf16x8 b, f32x4 c) { return __builtin_amdgcn_mfma_f32_16x16x32_bf16(a, b, c, 0, 0, 0); }
DEVI f32x16 mfma32(bf16x8 a, bf16x8 b, f32x16 c) { return __builtin_amdgcn_mfma_f32_32x32x16_bf16(a, b, c, 0, 0, 0); }
DEVI bf16x8 ones8() {
    u32x4 o = {0x3F803F80u, 0x3F803F80u, 0x3F803F80u, 0x3F803F80u};
    return __builtin_bit_cast(bf16x8, o);
}

constexpr int K1_SLOT = 28672, K1_XB = 16384, K1_RING = 5;
constexpr int K1_STAT = K1_SLOT * K1_RING;
constexpr int K1_CSP = K1_STAT + 1024;
constexpr int K1_CS = K1_CSP + 1536;
constexpr int K1_SCR = K1_CS + 384, K1_LDS = K1_SCR + 256;

DEVI void qkv_loader(const uint16_t* __restrict__ x, const uint16_t* __restrict__ wqkv, char* smem, int rb, int ct, int w, int lane) {
    const char* xsrc[4];
#pragma unroll
    for (int i = 0; i < 4; ++i) {
        const int r = 32 * w + 8 * i + (lane >> 3);
        const int c = (lane & 7) ^ ((r >> 1) & 7);
        xsrc[i] = (const char*)x + (size_t)(rb * 128 + r) * 1024 + c * 16;
    }
    const char* wsrc[3];
#pragma unroll
    for (int i = 0; i < 3; ++i) {
        const int ci = 64 * (3 * w + i) + lane;
        const int r = ci / 12, cp = ci % 12;
        const int c = cp ^ (((r >> 3) & 1) << 1);
        wsrc[i] = (const char*)wqkv + (size_t)r * 3072 + ct * 192 + c * 16;
    }
    auto issue = [&](int c) {
        const int slot = (c % K1_RING) * K1_SLOT;
#pragma unroll
        for (int i = 0; i < 4; ++i) glds16(xsrc[i] + c * 128, smem, slot + (32 * w + 8 * i) * 128);
#pragma unroll
        for (int i = 0; i < 3; ++i) glds16(wsrc[i] + (size_t)c * 64 * 3072, smem, slot + K1_XB + (3 * w + i) * 1024);
    };
    const int m = lane & 15, g = lane >> 4, wq = (lane >> 2) & 3, wp = lane & 3;
    int woff[3];
#pragma unroll
    for (int nt = 0; nt < 3; ++nt)
        woff[nt] = K1_XB + ((w >> 1) * 32 + 8 * g + wq) * 192 + wp * 8 + (((3 * (w & 1) + nt) ^ (g & 1)) << 5);
    const bf16x8 ones = ones8();
    f32x4 cs[3];
#pragma unroll
    for (int nt = 0; nt < 3; ++nt) cs[nt] = f32x4{0, 0, 0, 0};
#pragma unroll
    for (int c = 0; c < K1_RING; ++c) issue(c);
#pragma unroll
    for (int c = 0; c < 8; ++c) {
        if (c == 0) WAIT_V(28);
        else if (c <= 4) WAIT_V(21);
        else if (c == 5) WAIT_V(14);
        else if (c == 6) WAIT_V(7);
        else WAIT_V(0);
        WAIT_L0();
        BAR();
        const int slot = (c % K1_RING) * K1_SLOT;
        bf16x8 wf[3];
#pragma unroll
        for (int nt = 0; nt < 3; ++nt) wf[nt] = lds_read_tr(smem, slot + woff[nt], slot + woff[nt] + 4 * 192);
        SB();
        if (c >= 1 && c - 1 + K1_RING < 8) issue(c - 1 + K1_RING);
        SB();
#pragma unroll
        for (int nt = 0; nt < 3; ++nt) cs[nt] = mfma16(wf[nt], ones, cs[nt]);
        SB();
    }
    if (m == 0) {
#pragma unroll
        for (int nt = 0; nt < 3; ++nt) *(f32x4*)(smem + K1_CSP + ((w >> 1) * 96 + (3 * (w & 1) + nt) * 16 + 4 * g) * 4) = cs[nt];
    }
    WAIT_L0();
    BAR();
    if (w < 2) {
        const int t = w * 64 + lane;
        const float* csp = (const float*)(smem + K1_CSP);
        if (t < 96) ((float*)(smem + K1_CS))[t] = csp[t] + csp[96 + t];
    }
    WAIT_L0();
    BAR();
}

template <int NV, class Warm>
DEVI void qkv_body(char* __restrict__ ws, char* smem, int rb, int ct, int w, int lane, Warm warm DIAG_ARG) {
    const int m = lane & 15, g = lane >> 4;
    const int wr = w & 1, wc = w >> 1;
    int xoff[4][2];
#pragma unroll
    for (int tt = 0; tt < 4; ++tt)
#pragma unroll
        for (int s = 0; s < 2; ++s) {
            const int r = 64 * wr + 16 * (tt ^ (2 * wc)) + m;
            xoff[tt][s] = r * 128 + (((4 * s + g) ^ ((r >> 1) & 7)) << 4);
        }
    const int wq = (lane >> 2) & 3, wp = lane & 3;
    int woff[3];
#pragma unroll
    for (int nt = 0; nt < 3; ++nt) woff[nt] = K1_XB + (8 * g + wq) * 192 + wp * 8 + (((3 * wc + nt) ^ (g & 1)) << 5);
    f32x4 acc[4][3], ssum[2], ssq[2];
#pragma unroll
    for (int tt = 0; tt < 4; ++tt)
#pragma unroll
        for (int nt = 0; nt < 3; ++nt) acc[tt][nt] = f32x4{0, 0, 0, 0};
#pragma unroll
    for (int tt = 0; tt < 2; ++tt) {
        ssum[tt] = f32x4{0, 0, 0, 0};
        ssq[tt] = f32x4{0, 0, 0, 0};
    }
    const bf16x8 ones = ones8();
    bf16x8 xf[2][4], wf[2][3];
    auto rd = [&](int ks, int n) {
        const int slot = ((ks >> 1) % K1_RING) * K1_SLOT;
        if (n == 0 || (n >= 2 && n <= 4)) {
            const int tt = n == 0 ? 0 : n - 1;
            xf[ks & 1][tt] = lds_read16(smem, slot + xoff[tt][ks & 1]);
        } else {
            const int nt = n == 1 ? 0 : n - 4;
            const int o = slot + woff[nt] + (ks & 1) * 32 * 192;
            wf[ks & 1][nt] = lds_read_tr(smem, o, o + 4 * 192);
        }
    };
    BAR();
    STAMP(1);
#pragma unroll
    for (int n = 0; n < 7; ++n) rd(0, n);
    SB();
#pragma unroll
    for (int ks = 0; ks < 16; ++ks) {
        const int b = ks & 1;
        const int first = b ? 6 : 0;
#pragma unroll
        for (int n = 0; n < 16; ++n) {
            if (b && ks < 15 && n == first) {
                WAIT_L0();
                BAR();
                if (ks == K1_WARM_AT) warm();
                SB();
            }
            if (n < 12) {
                const int nt = n >> 2, tt = n & 3;
                acc[tt][nt] = (nt >= 3 - NV) ? mfma16(xf[b][tt], wf[b][nt], acc[tt][nt]) : mfma16(wf[b][nt], xf[b][tt], acc[tt][nt]);
            } else if (n < 14) {
                ssum[n - 12] = mfma16(ones, xf[b][n - 12], ssum[n - 12]);
            } else {
                ssq[n - 14] = mfma16(xf[b][n - 14], xf[b][n - 14], ssq[n - 14]);
            }
            if (ks < 15 && n >= first && n < first + 7) rd(ks + 1, n - first);
            SB();
        }
    }
    STAMP(2);
    f32x2* stat = (f32x2*)(smem + K1_STAT);
    const float* csf = (const float*)(smem + K1_CS);
#pragma unroll
    for (int tt = 0; tt < 2; ++tt) {
        const int r = m & 3;
        const float sq = r == 0 ? ssq[tt][0] : r == 1 ? ssq[tt][1] : r == 2 ? ssq[tt][2] : ssq[tt][3];
        const float mean = ssum[tt][0] * (1.0f / D_);
        const float var = fmaxf(sq * (1.0f / D_) - mean * mean, 0.0f);
        if ((m >> 2) == g) stat[64 * wr + 32 * wc + 16 * tt + m] = f32x2{mean, __builtin_amdgcn_rsqf(var + EPS)};
    }
    WAIT_L0();
    BAR();
    BAR();
    STAMP(3);
#pragma unroll
    for (int nt = 0; nt < 3; ++nt) {
        const int ntl = 3 * wc + nt;
        const int col = ct * 96 + ntl * 16;
        const int head = (col & 511) >> 4;
        const bool isv = nt >= 3 - NV;
        const int isk = col >= 512;
        const float sc = (!isv && !isk) ? QSCALE : 1.0f;
        const f32x4 c4 = *(const f32x4*)(csf + ntl * 16 + 4 * g);
        const float c1 = csf[ntl * 16 + m];
        char* base = ws + (isv ? WS_VF : isk ? WS_KF : WS_QF) + head * 1024 +
                     (isv ? lane * 16 : ((lane & 15) + 16 * (lane >> 5) + 32 * ((lane >> 4) & 1)) * 16);
#pragma unroll
        for (int pr = 0; pr < 2; ++pr) {
            unsigned pk[2][2];
#pragma unroll
            for (int u = 0; u < 2; ++u) {
                const int tt = 2 * pr + u;
                const int tok0 = 64 * wr + 16 * (tt ^ (2 * wc));
                float v[4];
                if (!isv) {
                    const f32x2 st = stat[tok0 + m];
                    const float rs = st[1] * sc;
#pragma unroll
                    for (int r = 0; r < 4; ++r) v[r] = (acc[tt][nt][r] - st[0] * c4[r]) * rs;
                } else {
#pragma unroll
                    for (int r = 0; r < 4; ++r) {
                        const f32x2 st = stat[tok0 + 4 * g + r];
                        v[r] = (acc[tt][nt][r] - st[0] * c1) * st[1];
                    }
                }
                pk[u][0] = pack2(v[0], v[1]);
                pk[u][1] = pack2(v[2], v[3]);
            }
            swap32(pk[0][0], pk[1][0]);
            swap32(pk[0][1], pk[1][1]);
            const int tile = rb * 4 + 2 * wr + (pr ^ wc);
            st16(base + (size_t)tile * 32768, u32x4{pk[0][0], pk[0][1], pk[1][0], pk[1][1]});
        }
    }
}

__global__ __launch_bounds__(512, 2) void k1_qkv(const uint16_t* __restrict__ x, const uint16_t* __restrict__ wqkv, const uint16_t* __restrict__ wo,
                                                 const uint16_t* __restrict__ wg, const uint16_t* __restrict__ wu, const uint16_t* __restrict__ wd,
                                                 char* __restrict__ ws) {
    extern __shared__ __attribute__((aligned(16))) char smem[];
    CODE_MARK(sq_code_k1);
    DIAG_DECL;
    const int lane = threadIdx.x & 63, wv = __builtin_amdgcn_readfirstlane(threadIdx.x >> 6), w = wv & 3;
    const int xcd = blockIdx.x & 7, idx = blockIdx.x >> 3;
    const int rb = K1_RB(xcd, idx), ct = K1_CT(xcd, idx);
    if (wv >= 4) {
        qkv_loader(x, wqkv, smem, rb, ct, w, lane);
        return;
    }
    if (w == 0 && (idx & 3) == 0) TOUCH_CODE(sq_code_k1, sq_code_k2, lane, K1_SCR);
    auto warm = [&]() {
        const int gw = blockIdx.x * 4 + w;
        touch_lines((const char*)wg + (size_t)gw * 1792, 1792, lane < 14 ? lane : 13, K1_SCR);
        touch_lines((const char*)wu + (size_t)gw * 1792, 1792, lane < 14 ? lane : 13, K1_SCR);
        touch_lines((const char*)wd + (size_t)gw * 1792, 1792, lane < 14 ? lane : 13, K1_SCR);
        touch_lines((const char*)wo + (size_t)gw * 512, 512, lane & 3, K1_SCR);
        if (blockIdx.x < 8 && w == 1) TOUCH_CODE(sq_code_k2, sq_code_end, lane, K1_SCR);
    };
    if (ct >= 11) qkv_body<3>(ws, smem, rb, ct, w, lane, warm DIAG_PASS);
    else if (ct == 10 && w >= 2) qkv_body<2>(ws, smem, rb, ct, w, lane, warm DIAG_PASS);
    else qkv_body<0>(ws, smem, rb, ct, w, lane, warm DIAG_PASS);
    WAIT_V(0);
    DIAG_END(ws, 0);
}

constexpr int K2_VS = 2064;
constexpr int K2_K = 0, K2_V = 24576, K2_Q = K2_V + 12 * K2_VS + 192, K2_JOB = K2_Q + 8192, K2_RED = K2_JOB + 32768;
constexpr int K2_SCR = K2_RED + 8 * 128 * 4, K2_LDS = K2_SCR + 256;

DEVI f32x16 splat16(float v) {
    f32x16 r;
#pragma unroll
    for (int i = 0; i < 16; ++i) r[i] = v;
    return r;
}
constexpr unsigned long long row_mask(int r) {
    unsigned long long m = 0;
    for (int l = 0; l < 64; ++l)
        if ((r & 3) + 8 * (r >> 2) + 4 * (l >> 5) > (l & 31)) m |= 1ull << l;
    return m;
}
template <int R>
DEVI unsigned sel_row(unsigned no, unsigned yes) { return __builtin_amdgcn_inverse_ballot_w64(row_mask(R)) ? yes : no; }
template <int R>
DEVI void merge_rows(const f32x16& sf, const f32x16& sl, f32x16& mg) {
    if constexpr (R < 16) {
        const float no = sl[R], yes = sf[R];
        mg[R] = __builtin_amdgcn_inverse_ballot_w64(row_mask(R)) ? yes : no;
        merge_rows<R + 1>(sf, sl, mg);
    }
}
DEVI f32x16 attn_merge(const f32x16& sf, const f32x16& sl) {
    f32x16 mg;
    merge_rows<0>(sf, sl, mg);
    return mg;
}
template <int R>
DEVI void split_rows(const f32x16& mg, float mx, unsigned (&pf)[8], unsigned (&pl)[8]) {
    if constexpr (R < 16) {
        const unsigned pk = pack2(__builtin_amdgcn_exp2f(mg[R] - mx), __builtin_amdgcn_exp2f(mg[R + 1] - mx));
        const unsigned mk = sel_row<R>(sel_row<R + 1>(0u, 0xffff0000u), 0xffffffffu);
        pf[R >> 1] = pk & mk;
        pl[R >> 1] = pk & ~mk;
        split_rows<R + 2>(mg, mx, pf, pl);
    }
}
DEVI void attn_split(const f32x16& mg, float mx, unsigned (&pf)[8], unsigned (&pl)[8]) { split_rows<0>(mg, mx, pf, pl); }
DEVI bf16x8 as_frag(const unsigned* p) { return __builtin_bit_cast(bf16x8, u32x4{p[0], p[1], p[2], p[3]}); }

template <bool FULL>
DEVI f32x16 attn_exact(char* smem, bf16x8 q, int koff, int vb0, int vb1, int i, int umin) {
    const float NEG = -__builtin_inff();
    const f32x16 zero = {0};
    const f32x16 sl = mfma32(lds_read16(smem, koff + 8 * 2048), q, zero);
    f32x16 sf = splat16(NEG);
    if (FULL || i >= umin) sf = mfma32(lds_read16(smem, koff), q, zero);
    const f32x16 mg = attn_merge(sf, sl);
    float mx = fmaxf(mg[0], mg[1]);
#pragma unroll
    for (int r = 2; r < 16; r += 2) mx = fmaxf(fmaxf(mx, mg[r]), mg[r + 1]);
#pragma unroll 1
    for (int t = 1; t < 8; ++t) {
        if (FULL || i + t >= umin) {
            const f32x16 s = mfma32(lds_read16(smem, koff + t * 2048), q, zero);
#pragma unroll
            for (int r = 0; r < 16; r += 2) mx = fmaxf(fmaxf(mx, s[r]), s[r + 1]);
        }
    }
    mx = swap32_max(mx);
    const f32x16 negm = splat16(-mx);
    f32x16 o = zero;
    unsigned pf[8], pl[8];
    attn_split(mg, mx, pf, pl);
#pragma unroll
    for (int s = 0; s < 2; ++s) {
        o = mfma32(lds_read16(smem, (s ? vb1 : vb0) + 8 * K2_VS), as_frag(pl + 4 * s), o);
        if (FULL || i >= umin) o = mfma32(lds_read16(smem, s ? vb1 : vb0), as_frag(pf + 4 * s), o);
    }
#pragma unroll 1
    for (int t = 1; t < 8; ++t) {
        if (FULL || i + t >= umin) {
            const f32x16 s = mfma32(lds_read16(smem, koff + t * 2048), q, negm);
            unsigned pk[8];
#pragma unroll
            for (int r = 0; r < 16; r += 2) pk[r >> 1] = pack2(__builtin_amdgcn_exp2f(s[r]), __builtin_amdgcn_exp2f(s[r + 1]));
            o = mfma32(lds_read16(smem, vb0 + t * K2_VS), as_frag(pk), o);
            o = mfma32(lds_read16(smem, vb1 + t * K2_VS), as_frag(pk + 4), o);
        }
    }
    return o;
}

template <bool FULL, class Mid>
DEVI f32x16 attn_fast(char* smem, bf16x8 q, int koff, int vb0, int vb1, int i, int umin, Mid mid DIAG_ARG) {
    const float NEG = -__builtin_inff();
    const f32x16 zero = {0};
    auto cinit = [&](int t) { return FULL ? zero : splat16(i + t >= umin ? 0.0f : NEG); };
    bf16x8 kf[2], vf[2][2];
    const bf16x8 kd = lds_read16(smem, koff + 8 * 2048), k0 = lds_read16(smem, koff);
    kf[1] = lds_read16(smem, koff + 2048);
    const bf16x8 vd0 = lds_read16(smem, vb0 + 8 * K2_VS), vz0 = lds_read16(smem, vb0);
    const bf16x8 vd1 = lds_read16(smem, vb1 + 8 * K2_VS), vz1 = lds_read16(smem, vb1);
    const f32x16 sl = mfma32(kd, q, zero);
    const f32x16 sf = mfma32(k0, q, cinit(0));
    f32x16 sc[2];
    sc[1] = mfma32(kf[1], q, cinit(1));
    kf[0] = lds_read16(smem, koff + 2 * 2048);
    vf[1][0] = lds_read16(smem, vb0 + K2_VS);
    vf[1][1] = lds_read16(smem, vb1 + K2_VS);
    SB();
    f32x16 o = zero;
    {
        unsigned pf[8], pl[8];
        attn_split(attn_merge(sf, sl), 0.0f, pf, pl);
        o = mfma32(vd0, as_frag(pl), o);
        o = mfma32(vz0, as_frag(pf), o);
        o = mfma32(vd1, as_frag(pl + 4), o);
        o = mfma32(vz1, as_frag(pf + 4), o);
    }
    SB();
    STAMP(2);
#pragma unroll
    for (int t = 1; t < 8; ++t) {
        const int b = t & 1;
        if (t < 7) {
            sc[b ^ 1] = mfma32(kf[b ^ 1], q, cinit(t + 1));
            if (t < 6) kf[b] = lds_read16(smem, koff + (t + 2) * 2048);
            vf[b ^ 1][0] = lds_read16(smem, vb0 + (t + 1) * K2_VS);
            vf[b ^ 1][1] = lds_read16(smem, vb1 + (t + 1) * K2_VS);
        }
        SB();
        unsigned pk[8];
#pragma unroll
        for (int r = 0; r < 8; r += 2) pk[r >> 1] = pack2(__builtin_amdgcn_exp2f(sc[b][r]), __builtin_amdgcn_exp2f(sc[b][r + 1]));
        const float e8 = __builtin_amdgcn_exp2f(sc[b][8]), e9 = __builtin_amdgcn_exp2f(sc[b][9]);
        SB();
        o = mfma32(vf[b][0], as_frag(pk), o);
        SB();
        pk[4] = pack2(e8, e9);
#pragma unroll
        for (int r = 10; r < 16; r += 2) pk[r >> 1] = pack2(__builtin_amdgcn_exp2f(sc[b][r]), __builtin_amdgcn_exp2f(sc[b][r + 1]));
        SB();
        o = mfma32(vf[b][1], as_frag(pk + 4), o);
        SB();
        if (t == 5) {
            STAMP(3);
            mid();
            STAMP(4);
        }
    }
    return o;
}

template <bool FULL, class Mid>
DEVI void attn_task(char* smem, char* __restrict__ ws, bf16x8 q, int rb, int i, int e, int p, int lane, Mid mid DIAG_ARG) {
    const int a = lane & 31, h = lane >> 5;
    const int koff = K2_K + e * 1024 + i * 2048 + lane * 16;
    const bool isdata = a < 16;
    const int vb0 = K2_V + i * K2_VS + (isdata ? e * 1024 + h * 256 + a * 16 : 2048);
    const int vb1 = vb0 + (isdata ? 512 : 0);
    const int umin = FULL ? 0 : 8 - 4 * rb;
    f32x16 o = attn_fast<FULL>(smem, q, koff, vb0, vb1, i, umin, mid DIAG_PASS);
    float mag = 0.f;
#pragma unroll
    for (int r = 0; r < 8; ++r) mag += __builtin_fabsf(o[r]);
    const bool safe = o[8] >= 0x1p-60f && o[8] <= 0x1p60f && mag <= 0x1p120f;
    if (__builtin_amdgcn_ballot_w64(!safe) != 0) {
        o = attn_exact<FULL>(smem, q, koff, vb0, vb1, i, umin);
    }
    const float inv = __builtin_amdgcn_rcpf(o[8]);
    const u32x4 ov = {pack2(o[0] * inv, o[1] * inv), pack2(o[2] * inv, o[3] * inv), pack2(o[4] * inv, o[5] * inv), pack2(o[6] * inv, o[7] * inv)};
    st16(ws + WS_OF + (size_t)((rb * 4 + i) * 32 + 2 * p + e) * 1024 + lane * 16, ov);
}

__global__ __launch_bounds__(512, 2) void k2_attn(const uint16_t* __restrict__ wo, const uint16_t* __restrict__ wg, const uint16_t* __restrict__ wu,
                                                  const uint16_t* __restrict__ wd, char* __restrict__ ws) {
    extern __shared__ __attribute__((aligned(16))) char smem[];
    CODE_MARK(sq_code_k2);
    DIAG_DECL;
    const int tid = threadIdx.x, lane = tid & 63, w = __builtin_amdgcn_readfirstlane(tid >> 6);
    const int xcd = blockIdx.x & 7, idx = blockIdx.x >> 3;
    const int rb = 2 * xcd + (idx >> 4), p = idx & 15;
    const int i = w & 3, e = w >> 2;
    if (w == 7 && (idx & 3) == 0) TOUCH_CODE(sq_code_k2, sq_code_k3, lane, K2_SCR);
#pragma unroll
    for (int n = 0; n < 6; ++n) {
        const int pc = 6 * w + n;
        const int isv = pc >= 24, u = (pc % 24) >> 1, half = pc & 1;
        int kt = 4 * rb - 8 + u;
        kt = kt < 0 ? 0 : kt;
        const char* src = ws + (isv ? WS_VF : WS_KF) + (size_t)(kt * 16 + p) * 2048 + half * 1024 + lane * 16;
        glds16(src, smem, (isv ? K2_V + u * K2_VS : K2_K + u * 2048) + half * 1024);
    }
    glds16(ws + WS_QF + (size_t)((rb * 4 + i) * 32 + 2 * p + e) * 1024 + lane * 16, smem, K2_Q + w * 1024);
    if (tid < 48) *(unsigned*)(smem + K2_V + (tid >> 2) * K2_VS + 2048 + (tid & 3) * 4) = 0x3F803F80u;
    const int bid = blockIdx.x;
    const int job = K2_JOB + w * 4096;
    if (bid < 112) {
        const int cg = bid % 28, s = 8 * (bid / 28) + w;
#pragma unroll
        for (int n = 0; n < 4; ++n) {
            const uint16_t* src = (n >> 1) ? wu : wg;
            const int row = 16 * s + 8 * (n & 1) + (lane >> 3);
            glds16((const char*)src + (size_t)row * (F_ * 2) + cg * 128 + (lane & 7) * 16, smem, job + n * 1024);
        }
    } else {
        const int jj = (bid - 112) * 8 + w;
        const bool iso = jj < 256;
        const int j2 = iso ? jj : jj - 256;
        const uint16_t* src = iso ? wo : wd;
        const int fp = j2 & 7, s = j2 >> 3;
#pragma unroll
        for (int n = 0; n < 2; ++n) {
            const int row = 16 * s + 8 * n + (lane >> 3);
            glds16((const char*)src + (size_t)row * (D_ * 2) + fp * 128 + (lane & 7) * 16, smem, job + n * 1024);
        }
    }
    if (bid < 112) WAIT_V(4);
    else WAIT_V(2);
    WAIT_L0();
    BAR();
    STAMP(1);
    const bf16x8 q = lds_read16(smem, K2_Q + w * 1024 + lane * 16);
    auto retile = [&]() {
        WAIT_V(0);
        const int G = lane >> 4, nh = G & 1, hh = G >> 1, rq = (lane >> 2) & 3, pp = lane & 3;
        if (bid < 112) {
            const int cg = bid % 28, kq = bid / 28, s = 8 * kq + w;
            float* red = (float*)(smem + K2_RED);
#pragma unroll
            for (int t = 0; t < 4; ++t) {
                const int o = job + nh * 2048 + (4 * hh + rq) * 128 + t * 32 + pp * 8;
                const bf16x8 f = lds_read_tr(smem, o, o + 8 * 128);
                const int T = 4 * cg + t, j = T / 7, t7 = T % 7;
                const u32x4 u = __builtin_bit_cast(u32x4, f);
                st16(ws + WS_WGU + (size_t)((j * 32 + s) * 7 + t7) * 1024 + lane * 16, u);
                float sum = 0.f;
#pragma unroll
                for (int r = 0; r < 4; ++r) sum += bf_lo(u[r]) + bf_hi(u[r]);
                sum = swap32_add(sum);
                if (lane < 32) red[w * 128 + nh * 64 + t * 16 + (lane & 15)] = sum;
            }
        } else {
            const int jj = (bid - 112) * 8 + w;
            const bool iso = jj < 256;
            const int j2 = iso ? jj : jj - 256;
            const int fp = j2 & 7, s = j2 >> 3;
            char* dst = ws + (iso ? WS_WOT + (size_t)(fp * 32 + s) * 2048 : WS_WDT + (size_t)(fp * 112 + s) * 2048) + lane * 16;
#pragma unroll
            for (int t = 0; t < 2; ++t) {
                const int o = job + (4 * hh + rq) * 128 + t * 64 + nh * 32 + pp * 8;
                st16(dst + t * 1024, __builtin_bit_cast(u32x4, lds_read_tr(smem, o, o + 8 * 128)));
            }
        }
        SB();
    };
    if (rb >= 2) attn_task<true>(smem, ws, q, rb, i, e, p, lane, retile DIAG_PASS);
    else attn_task<false>(smem, ws, q, rb, i, e, p, lane, retile DIAG_PASS);
    STAMP(5);
    if (bid < 112) {
        const int cg = bid % 28, kq = bid / 28;
        const float* red = (const float*)(smem + K2_RED);
        WAIT_L0();
        BAR();
        if (tid < 128) {
            float sum = 0.f;
#pragma unroll
            for (int ww = 0; ww < 8; ++ww) sum += red[ww * 128 + tid];
            ((float*)(ws + WS_CSP))[(kq * 2 + (tid >> 6)) * F_ + cg * 64 + (tid & 63)] = sum;
        }
    }
    WAIT_V(0);
    DIAG_END(ws, 1);
}

constexpr int G64_RED = 0, G64_T = 49152, G64_SCR = G64_T + 8192, G64_LDS = G64_SCR + 256;

template <int NS, int DEPTH, class Pre>
DEVI f32x16 gemm64(char* smem, const char* wt, const char* act, int act_tile_stride, int w, int lane, Pre pre DIAG_ARG) {
    const char* pa[2];
    const char* pb[2];
#pragma unroll
    for (int q = 0; q < 2; ++q) {
        pa[q] = wt + (size_t)(w * NS) * 2048 + (q ^ (w >> 1)) * 1024 + lane * 16;
        pb[q] = act + (size_t)(q ^ (w & 1)) * act_tile_stride + (size_t)(w * NS) * 1024 + lane * 16;
    }
    bf16x8 fa[NS][2], fb[NS][2];
    auto load = [&](int s) {
#pragma unroll
        for (int q = 0; q < 2; ++q) {
            fa[s][q] = *(const bf16x8*)(pa[q] + s * 2048);
            fb[s][q] = *(const bf16x8*)(pb[q] + s * 1024);
        }
    };
#pragma unroll
    for (int s = 0; s < DEPTH; ++s) load(s);
    SB();
    f32x16 acc[4];
#pragma unroll
    for (int q = 0; q < 4; ++q) acc[q] = f32x16{0};
#pragma unroll
    for (int s = 0; s < NS; ++s) {
        if (s + DEPTH < NS) load(s + DEPTH);
        SB();
#pragma unroll
        for (int q = 0; q < 4; ++q) acc[q] = mfma32(fa[s][q >> 1], fb[s][q & 1], acc[q]);
        SB();
        if (s == 0) STAMP(1);
    }
    STAMP(2);
    pre();
#pragma unroll
    for (int q = 1; q < 4; ++q) {
        char* dst = smem + G64_RED + ((w ^ q) * 3 + q - 1) * 4096 + lane * 16;
#pragma unroll
        for (int r = 0; r < 4; ++r)
            *(f32x4*)(dst + r * 1024) = f32x4{acc[q][4 * r], acc[q][4 * r + 1], acc[q][4 * r + 2], acc[q][4 * r + 3]};
    }
    WAIT_L0();
    BAR();
    f32x16 o = acc[0];
#pragma unroll
    for (int q = 0; q < 3; ++q) {
        const char* src = smem + G64_RED + (w * 3 + q) * 4096 + lane * 16;
#pragma unroll
        for (int r = 0; r < 4; ++r) {
            const f32x4 v = *(const f32x4*)(src + r * 1024);
#pragma unroll
            for (int k = 0; k < 4; ++k) o[4 * r + k] += v[k];
        }
    }
    STAMP(3);
    return o;
}

__global__ __launch_bounds__(256, 1) void k3_oproj(const uint16_t* __restrict__ x, char* __restrict__ ws) {
    extern __shared__ __attribute__((aligned(16))) char smem[];
    CODE_MARK(sq_code_k3);
    DIAG_DECL;
    const int tid = threadIdx.x, lane = tid & 63, w = __builtin_amdgcn_readfirstlane(tid >> 6);
    const int xcd = blockIdx.x & 7, idx = blockIdx.x >> 3;
    const int tb = 4 * xcd + (idx >> 3), fp = idx & 7;
    const int tt = w & 1, ft = w >> 1;
    if (w == 0 && (idx & 3) == 0) TOUCH_CODE(sq_code_k3, sq_code_k4, lane, G64_SCR);
    u32x4 xr[2];
#pragma unroll
    for (int n = 0; n < 2; ++n) {
        const int r = (w * 2 + n) * 8 + (lane >> 3);
        xr[n] = *(const u32x4*)((const char*)x + (size_t)(tb * 64 + r) * 1024 + fp * 128 + (lane & 7) * 16);
    }
    const char* wt = ws + WS_WOT + (size_t)fp * 65536;
    const char* act = ws + WS_OF + (size_t)tb * 65536;
    auto stage_x = [&]() {
    #pragma unroll
        for (int n = 0; n < 2; ++n) {
            const int r = (w * 2 + n) * 8 + (lane >> 3);
            *(u32x4*)(smem + G64_T + r * 128 + (((lane & 7) ^ ((r >> 1) & 7)) << 4)) = xr[n];
        }
    };
    const f32x16 acc = gemm64<8, 8>(smem, wt, act, 32768, w, lane, stage_x DIAG_PASS);
    const int tok = lane & 31, h = lane >> 5;
    const int r = tt * 32 + tok;
    unsigned pk[8];
    float sum = 0.f, sq = 0.f;
#pragma unroll
    for (int qd = 0; qd < 4; ++qd) {
        const u32x2 xv = *(const u32x2*)(smem + G64_T + r * 128 + (((ft * 4 + qd) ^ ((r >> 1) & 7)) << 4) + h * 8);
        pk[2 * qd] = pack2(acc[4 * qd] + bf_lo(xv[0]), acc[4 * qd + 1] + bf_hi(xv[0]));
        pk[2 * qd + 1] = pack2(acc[4 * qd + 2] + bf_lo(xv[1]), acc[4 * qd + 3] + bf_hi(xv[1]));
#pragma unroll
        for (int k = 0; k < 2; ++k) {
            const float lo = bf_lo(pk[2 * qd + k]), hi = bf_hi(pk[2 * qd + k]);
            sum += lo + hi;
            sq += lo * lo + hi * hi;
        }
    }
    char* hdst = ws + WS_HF + (size_t)((tb * 2 + tt) * 32 + (fp * 2 + ft) * 2) * 1024 + lane * 16;
    st16(hdst, u32x4{pk[0], pk[1], pk[2], pk[3]});
    st16(hdst + 1024, u32x4{pk[4], pk[5], pk[6], pk[7]});
    sum = swap32_add(sum);
    sq = swap32_add(sq);
    if (h == 0) ((f32x2*)(ws + WS_ST2))[(fp * 2 + ft) * S_ + tb * 64 + r] = f32x2{sum, sq};
    WAIT_V(0);
    DIAG_END(ws, 2);
}

constexpr int K4_A = 28672, K4_SLOT = 45056, K4_RING = 3, K4_CS = K4_SLOT * K4_RING, K4_SCR = K4_CS + 224 * 4, K4_LDS = K4_SCR + 256;

__global__ __launch_bounds__(512, 2) void k4_gateup(char* __restrict__ ws) {
    extern __shared__ __attribute__((aligned(16))) char smem[];
    CODE_MARK(sq_code_k4);
    DIAG_DECL;
    const int tid = threadIdx.x, lane = tid & 63, wv = __builtin_amdgcn_readfirstlane(tid >> 6), w = wv & 3;
    const int xcd = blockIdx.x & 7, idx = blockIdx.x >> 3;
    const int rb = K4_RB(xcd, idx), j = K4_J(xcd, idx);
    const int tile = rb * 4 + w;
    if (wv >= 4) {
        const char* asrc = ws + WS_WGU + (size_t)j * (32 * 7168) + w * 7168 + lane * 16;
        const char* bsrc = ws + WS_HF + (size_t)tile * 32768 + lane * 16;
        auto issue = [&](int c) {
            const int slot = (c % K4_RING) * K4_SLOT;
#pragma unroll
            for (int n = 0; n < 4; ++n) glds16(bsrc + (c * 4 + n) * 1024, smem, slot + K4_A + (w * 4 + n) * 1024);
#pragma unroll
            for (int n = 0; n < 7; ++n) glds16(asrc + c * 28672 + n * 1024, smem, slot + w * 7168 + n * 1024);
        };
#pragma unroll
        for (int c = 0; c < K4_RING; ++c) issue(c);
#pragma unroll
        for (int c = 0; c < 8; ++c) {
            if (c == 0) WAIT_V(22);
            else if (c < 7) WAIT_V(11);
            else WAIT_V(0);
            BAR();
            if (c >= 1 && c - 1 + K4_RING < 8) issue(c - 1 + K4_RING);
        }
        return;
    }
    if (w == 0 && (idx & 3) == 0) TOUCH_CODE(sq_code_k4, sq_code_k5, lane, K4_SCR);
    const int tok = lane & 31, h = lane >> 5;
    f32x2 st[8];
#pragma unroll
    for (int n = 0; n < 8; ++n) st[n] = ((const f32x2*)(ws + WS_ST2))[(h * 8 + n) * S_ + tile * 32 + tok];
    {
        const int t = tid < 224 ? tid : 0;
        const int gu = t >= 112, col = j * 112 + (gu ? t - 112 : t);
        float csq[4];
#pragma unroll
        for (int n = 0; n < 4; ++n) csq[n] = ((const float*)(ws + WS_CSP))[(n * 2 + gu) * F_ + col];
        if (tid < 224) ((float*)(smem + K4_CS))[tid] = (csq[0] + csq[1]) + (csq[2] + csq[3]);
    }
    float sum = 0.f, sq = 0.f;
#pragma unroll
    for (int n = 0; n < 8; ++n) {
        sum += st[n][0];
        sq += st[n][1];
    }
    sum = swap32_add(sum);
    sq = swap32_add(sq);
    const float mean = sum * (1.0f / D_);
    const float rstd = __builtin_amdgcn_rsqf(fmaxf(sq * (1.0f / D_) - mean * mean, 0.0f) + EPS);
    f32x16 acc[7];
#pragma unroll
    for (int t = 0; t < 7; ++t) acc[t] = f32x16{0};
    bf16x8 af[2][7], bfr[2];
    auto rdB = [&](int s) {
        bfr[s & 1] = lds_read16(smem, ((s >> 2) % K4_RING) * K4_SLOT + K4_A + (w * 4 + (s & 3)) * 1024 + lane * 16);
    };
    auto rdA = [&](int s, int t) {
        af[s & 1][t] = lds_read16(smem, ((s >> 2) % K4_RING) * K4_SLOT + ((s & 3) * 7 + t) * 1024 + lane * 16);
    };
    WAIT_L0();
    BAR();
    STAMP(1);
    rdB(0);
#pragma unroll
    for (int t = 0; t < 7; ++t) rdA(0, t);
    SB();
#pragma unroll
    for (int s = 0; s < 32; ++s) {
        const int b = s & 1;
        const bool edge = (s & 3) == 3;
#pragma unroll
        for (int t = 0; t < 7; ++t) {
            if (edge && s < 31 && t == 4) {
                WAIT_L0();
                BAR();
                SB();
            }
            acc[t] = mfma32(af[b][t], bfr[b], acc[t]);
            if (s < 31) {
                if (!edge) {
                    if (t == 0) rdB(s + 1);
                    rdA(s + 1, t);
                } else if (t == 4) {
                    rdB(s + 1);
                    rdA(s + 1, 0);
                    rdA(s + 1, 1);
                } else if (t == 5) {
                    rdA(s + 1, 2);
                    rdA(s + 1, 3);
                    rdA(s + 1, 4);
                } else if (t == 6) {
                    rdA(s + 1, 5);
                    rdA(s + 1, 6);
                }
            }
            SB();
        }
    }
    STAMP(2);
    const float* cs = (const float*)(smem + K4_CS);
    char* dst = ws + WS_MF + (size_t)(tile * 112 + j * 7) * 1024 + lane * 16;
#pragma unroll
    for (int t = 0; t < 7; ++t) {
        float v[8];
#pragma unroll
        for (int qd = 0; qd < 2; ++qd) {
            const f32x4 cg = *(const f32x4*)(cs + t * 16 + 8 * qd + 4 * h);
            const f32x4 cu = *(const f32x4*)(cs + 112 + t * 16 + 8 * qd + 4 * h);
#pragma unroll
            for (int r = 0; r < 4; ++r) {
                const float gt = (acc[t][4 * qd + r] - mean * cg[r]) * rstd;
                const float up = (acc[t][8 + 4 * qd + r] - mean * cu[r]) * rstd;
                v[4 * qd + r] = fmaxf(gt, 0.0f) * up;
            }
        }
        st16(dst + t * 1024, u32x4{pack2(v[0], v[1]), pack2(v[2], v[3]), pack2(v[4], v[5]), pack2(v[6], v[7])});
    }
    WAIT_V(0);
    DIAG_END(ws, 3);
}

__global__ __launch_bounds__(256, 1) void k5_down(char* __restrict__ ws, uint16_t* __restrict__ out) {
    extern __shared__ __attribute__((aligned(16))) char smem[];
    CODE_MARK(sq_code_k5);
    DIAG_DECL;
    const int tid = threadIdx.x, lane = tid & 63, w = __builtin_amdgcn_readfirstlane(tid >> 6);
    const int xcd = blockIdx.x & 7, idx = blockIdx.x >> 3;
    const int tb = 4 * xcd + (idx >> 3), fp = idx & 7;
    const int tt = w & 1, ft = w >> 1;
    if (w == 0 && (idx & 3) == 0) TOUCH_CODE(sq_code_k5, sq_code_end, lane, G64_SCR);
    const char* hsrc = ws + WS_HF + (size_t)((tb * 2 + tt) * 32 + (fp * 2 + ft) * 2) * 1024 + lane * 16;
    const u32x4 h0 = *(const u32x4*)hsrc, h1 = *(const u32x4*)(hsrc + 1024);
    const char* wt = ws + WS_WDT + (size_t)fp * (112 * 2048);
    const char* act = ws + WS_MF + (size_t)(tb * 2) * (112 * 1024);
    const f32x16 acc = gemm64<28, K5_DEPTH>(smem, wt, act, 112 * 1024, w, lane, [] {} DIAG_PASS);
    const int tok = lane & 31, h = lane >> 5;
    const int r = tt * 32 + tok;
    const unsigned hv[8] = {h0[0], h0[1], h0[2], h0[3], h1[0], h1[1], h1[2], h1[3]};
#pragma unroll
    for (int qd = 0; qd < 4; ++qd) {
        const u32x2 v = {pack2(acc[4 * qd] + bf_lo(hv[2 * qd]), acc[4 * qd + 1] + bf_hi(hv[2 * qd])),
                         pack2(acc[4 * qd + 2] + bf_lo(hv[2 * qd + 1]), acc[4 * qd + 3] + bf_hi(hv[2 * qd + 1]))};
        *(u32x2*)(smem + G64_T + r * 128 + (((ft * 4 + qd) ^ ((r >> 1) & 7)) << 4) + h * 8) = v;
    }
    WAIT_L0();
    BAR();
#pragma unroll
    for (int n = 0; n < 2; ++n) {
        const int rr = (w * 2 + n) * 8 + (lane >> 3), cp = lane & 7;
        const u32x4 v = *(const u32x4*)(smem + G64_T + rr * 128 + cp * 16);
        *(u32x4*)((char*)out + (size_t)(tb * 64 + rr) * 1024 + fp * 128 + ((cp ^ ((rr >> 1) & 7)) << 4)) = v;
    }
    WAIT_V(0);
    CODE_MARK(sq_code_end);
    DIAG_END(ws, 4);
}

extern "C" void kernel_launch(void* const* d_in, const int* in_sizes, int n_in, void* d_out, int out_size, void* d_ws,
                              size_t ws_size, hipStream_t stream) {
    const uint16_t* x = (const uint16_t*)d_in[0];
    const uint16_t* wqkv = (const uint16_t*)d_in[1];
    const uint16_t* wo = (const uint16_t*)d_in[2];
    const uint16_t* wg = (const uint16_t*)d_in[4];
    const uint16_t* wu = (const uint16_t*)d_in[5];
    const uint16_t* wd = (const uint16_t*)d_in[6];
    char* ws = (char*)d_ws;
    (void)hipFuncSetAttribute((const void*)k1_qkv, hipFuncAttributeMaxDynamicSharedMemorySize, K1_LDS);
    (void)hipFuncSetAttribute((const void*)k2_attn, hipFuncAttributeMaxDynamicSharedMemorySize, K2_LDS);
    (void)hipFuncSetAttribute((const void*)k3_oproj, hipFuncAttributeMaxDynamicSharedMemorySize, G64_LDS);
    (void)hipFuncSetAttribute((const void*)k4_gateup, hipFuncAttributeMaxDynamicSharedMemorySize, K4_LDS);
    (void)hipFuncSetAttribute((const void*)k5_down, hipFuncAttributeMaxDynamicSharedMemorySize, G64_LDS);
    k1_qkv<<<256, 512, K1_LDS, stream>>>(x, wqkv, wo, wg, wu, wd, ws);
    k2_attn<<<256, 512, K2_LDS, stream>>>(wo, wg, wu, wd, ws);
    k3_oproj<<<256, 256, G64_LDS, stream>>>(x, ws);
    k4_gateup<<<256, 512, K4_LDS, stream>>>(ws);
    k5_down<<<256, 256, G64_LDS, stream>>>(ws, (uint16_t*)d_out);
}
```

```cpp
#include <hip/hip_runtime.h>
#include <hip/hip_bf16.h>
#include <stdint.h>
#include <type_traits>

typedef __attribute__((ext_vector_type(8))) __bf16 bf16x8;
typedef __attribute__((ext_vector_type(2))) __bf16 bf16x2;
typedef __attribute__((ext_vector_type(4))) short s16x4;
typedef __attribute__((ext_vector_type(8))) short s16x8;
typedef __attribute__((ext_vector_type(2))) float f32x2;
typedef __attribute__((ext_vector_type(4))) float f32x4;
typedef __attribute__((ext_vector_type(16))) float f32x16;
typedef __attribute__((ext_vector_type(2))) unsigned u32x2;
typedef __attribute__((ext_vector_type(4))) unsigned u32x4;
typedef __attribute__((address_space(3))) char* lds_ptr;
typedef const __attribute__((address_space(1))) void* gptr_t;

#define DEVI __device__ __forceinline__
#define WAIT_V(n) asm volatile("s_waitcnt vmcnt(" #n ")" ::: "memory")
template <int N>
__device__ __forceinline__ void wait_vm() { asm volatile("s_waitcnt vmcnt(%0)" ::"n"(N) : "memory"); }
template <int I, int N, class F>
__device__ __forceinline__ void static_for(F f) {
    if constexpr (I < N) {
        f(std::integral_constant<int, I>{});
        static_for<I + 1, N>(f);
    }
}
#define WAIT_V0() __builtin_amdgcn_s_waitcnt(0x0070)
#define WAIT_L0() asm volatile("s_waitcnt lgkmcnt(0)" ::: "memory")
#define BAR() __builtin_amdgcn_s_barrier()
#define SB() __builtin_amdgcn_sched_barrier(0)

#define DIAG_DECL do { } while (0)
#define STAMP(ph) do { } while (0)
#define DIAG_END(ws, kid) do { } while (0)
#define DIAG_ARG
#define DIAG_PASS

constexpr int S_ = 2048, D_ = 512, F_ = 1792;
constexpr float EPS = 1e-5f;
constexpr float QSCALE = 0.25f * 1.44269504088896340736f;

constexpr size_t WS_QF = 0;
constexpr size_t WS_KF = WS_QF + (2u << 20);
constexpr size_t WS_VF = WS_KF + (2u << 20);
constexpr size_t WS_OF = WS_VF + (2u << 20);
constexpr size_t WS_HF = WS_OF + (2u << 20);
constexpr size_t WS_MF = WS_HF + (2u << 20);
constexpr size_t WS_WOT = WS_MF + (7u << 20);
constexpr size_t WS_WGU = WS_WOT + (512u << 10);
constexpr size_t WS_WDT = WS_WGU + (3584u << 10);
constexpr size_t WS_ST2 = WS_WDT + (1792u << 10);
constexpr size_t WS_CSP = WS_ST2 + (256u << 10);
constexpr size_t WS_DIAG = WS_CSP + (64u << 10);
constexpr size_t WS_CNT = WS_DIAG + (1u << 20);

DEVI void glds16(const void* g, char* smem_base, int lds_off) {
    __builtin_amdgcn_global_load_lds((gptr_t)g, (__attribute__((address_space(3))) void*)((lds_ptr)smem_base + lds_off), 16, 0, 0);
}
DEVI bf16x8 lds_read16(char* smem, int off) { return *(const bf16x8*)(smem + off); }
DEVI bf16x8 lds_read_tr(char* smem, int off0, int off1) {
    s16x4 a = __builtin_amdgcn_ds_read_tr16_b64_v4i16((__attribute__((address_space(3))) s16x4*)((lds_ptr)smem + off0));
    s16x4 b = __builtin_amdgcn_ds_read_tr16_b64_v4i16((__attribute__((address_space(3))) s16x4*)((lds_ptr)smem + off1));
    s16x8 c = __builtin_shufflevector(a, b, 0, 1, 2, 3, 4, 5, 6, 7);
    return __builtin_bit_cast(bf16x8, c);
}
#define K4_XA 2
#define K4_RB(xcd, idx) (((xcd) % K4_XA) * (16 / K4_XA) + ((idx) % (16 / K4_XA)))
#define K4_J(xcd, idx) (((xcd) / K4_XA) * (2 * K4_XA) + ((idx) / (16 / K4_XA)))
#define K1_XA 4
#define K1_RB(xcd, idx) (((xcd) % K1_XA) * (16 / K1_XA) + ((idx) % (16 / K1_XA)))
#define K1_CT(xcd, idx) (((xcd) / K1_XA) * (2 * K1_XA) + ((idx) / (16 / K1_XA)))
#define K1_WARM_AT 3
#define K5_DEPTH 6
DEVI void st16(void* p, u32x4 v) {
    asm volatile("global_store_dwordx4 %0, %1, off sc1\n\ts_nop 1" ::"v"(p), "v"(v) : "memory");
}
typedef __attribute__((address_space(1))) unsigned* gcnt_t;
DEVI gcnt_t counter(char* ws, int edge, int rb) { return (gcnt_t)(ws + WS_CNT + (size_t)(edge * 16 + rb) * 256); }
DEVI void arrive(gcnt_t c) { __hip_atomic_fetch_add(c, 1u, __ATOMIC_RELAXED, __HIP_MEMORY_SCOPE_AGENT); }
DEVI void await(gcnt_t c, unsigned n) {
    for (int spin = 0; spin < (1 << 22); ++spin) {
        if (__hip_atomic_load(c, __ATOMIC_RELAXED, __HIP_MEMORY_SCOPE_AGENT) >= n) break;
        __builtin_amdgcn_s_sleep(1);
    }
}
#define LD_AUX 16
DEVI bf16x8 load_dev(__amdgpu_buffer_rsrc_t rs, int voff, int soff) {
    return __builtin_bit_cast(bf16x8, __builtin_amdgcn_raw_buffer_load_b128(rs, voff, soff, LD_AUX));
}
DEVI void touch_lines(const char* base, int nbytes, int lane, int scratch) {
    for (int off = 0; off < nbytes; off += 8192) {
        const int o = min(off + lane * 128, nbytes - 4);
        int keep;
        asm volatile("s_mov_b32 %0, m0\n\ts_mov_b32 m0, %2\n\ts_nop 0\n\tglobal_load_lds_dword %1, off\n\ts_mov_b32 m0, %0"
                     : "=&s"(keep) : "v"(base + o), "s"(scratch) : "memory");
    }
}
#define CODE_MARK(name) asm volatile(#name ":")
#define TOUCH_CODE(from, to, lane, scratch) do { \
    unsigned long long pc_; int d0_, d1_; \
    asm volatile("s_getpc_b64 %0\n.Lpc%=:\n\ts_mov_b32 %1, " #from "-.Lpc%=\n\ts_mov_b32 %2, " #to "-.Lpc%=" : "=&s"(pc_), "=&s"(d0_), "=&s"(d1_)); \
    const int n_ = d1_ - d0_; \
    if (n_ > 0 && n_ <= 65536) touch_lines((const char*)pc_ + d0_, n_ & ~3, lane, scratch); \
} while (0)
DEVI unsigned pack2(float a, float b) { return __builtin_bit_cast(unsigned, __builtin_convertvector(f32x2{a, b}, bf16x2)); }
DEVI float bf_lo(unsigned u) { return __builtin_bit_cast(float, u << 16); }
DEVI float bf_hi(unsigned u) { return __builtin_bit_cast(float, u & 0xffff0000u); }
DEVI float swap32_add(float v) {
    unsigned a = __builtin_bit_cast(unsigned, v), b = a;
    asm volatile("" : "+v"(b));
    auto r = __builtin_amdgcn_permlane32_swap(a, b, false, false);
    const unsigned r0 = r[0], r1 = r[1];
    return __builtin_bit_cast(float, r0) + __builtin_bit_cast(float, r1);
}
DEVI void swap32(unsigned& a, unsigned& b) {
    auto r = __builtin_amdgcn_permlane32_swap(a, b, false, false);
    const unsigned r0 = r[0], r1 = r[1];
    a = r0;
    b = r1;
}
DEVI float swap32_max(float v) {
    unsigned a = __builtin_bit_cast(unsigned, v), b = a;
    asm volatile("" : "+v"(b));
    auto r = __builtin_amdgcn_permlane32_swap(a, b, false, false);
    const unsigned r0 = r[0], r1 = r[1];
    return fmaxf(__builtin_bit_cast(float, r0), __builtin_bit_cast(float, r1));
}
DEVI f32x4 mfma16(bf16x8 a, bf16x8 b, f32x4 c) { return __builtin_amdgcn_mfma_f32_16x16x32_bf16(a, b, c, 0, 0, 0); }
DEVI f32x16 mfma32(bf16x8 a, bf16x8 b, f32x16 c) { return __builtin_amdgcn_mfma_f32_32x32x16_bf16(a, b, c, 0, 0, 0); }
DEVI bf16x8 ones8() {
    u32x4 o = {0x3F803F80u, 0x3F803F80u, 0x3F803F80u, 0x3F803F80u};
    return __builtin_bit_cast(bf16x8, o);
}

constexpr int K1_SLOT = 28672, K1_XB = 16384, K1_RING = 5;
constexpr int K1_STAT = K1_SLOT * K1_RING;
constexpr int K1_CSP = K1_STAT + 1024;
constexpr int K1_CS = K1_CSP + 1536;
constexpr int K1_SCR = K1_CS + 384, K1_JOB = K1_SCR + 256, K1_LDS = K1_JOB + 2048;

DEVI void qkv_loader(const uint16_t* __restrict__ x, const uint16_t* __restrict__ wqkv, const uint16_t* __restrict__ wo, char* __restrict__ ws,
                     char* smem, int rb, int ct, int w, int lane) {
    const char* xsrc[4];
#pragma unroll
    for (int i = 0; i < 4; ++i) {
        const int r = 32 * w + 8 * i + (lane >> 3);
        const int c = (lane & 7) ^ ((r >> 1) & 7);
        xsrc[i] = (const char*)x + (size_t)(rb * 128 + r) * 1024 + c * 16;
    }
    const char* wsrc[3];
#pragma unroll
    for (int i = 0; i < 3; ++i) {
        const int ci = 64 * (3 * w + i) + lane;
        const int r = ci / 12, cp = ci % 12;
        const int c = cp ^ (((r >> 3) & 1) << 1);
        wsrc[i] = (const char*)wqkv + (size_t)r * 3072 + ct * 192 + c * 16;
    }
    auto issue = [&](int c) {
        const int slot = (c % K1_RING) * K1_SLOT;
#pragma unroll
        for (int i = 0; i < 4; ++i) glds16(xsrc[i] + c * 128, smem, slot + (32 * w + 8 * i) * 128);
#pragma unroll
        for (int i = 0; i < 3; ++i) glds16(wsrc[i] + (size_t)c * 64 * 3072, smem, slot + K1_XB + (3 * w + i) * 1024);
    };
    const int m = lane & 15, g = lane >> 4, wq = (lane >> 2) & 3, wp = lane & 3;
    int woff[3];
#pragma unroll
    for (int nt = 0; nt < 3; ++nt)
        woff[nt] = K1_XB + ((w >> 1) * 32 + 8 * g + wq) * 192 + wp * 8 + (((3 * (w & 1) + nt) ^ (g & 1)) << 5);
    const bf16x8 ones = ones8();
    f32x4 cs[3];
#pragma unroll
    for (int nt = 0; nt < 3; ++nt) cs[nt] = f32x4{0, 0, 0, 0};
    const int jfp = blockIdx.x & 7, js = blockIdx.x >> 3;
    if (w == 0) {
#pragma unroll
        for (int n = 0; n < 2; ++n) glds16((const char*)wo + (size_t)(16 * js + 8 * n + (lane >> 3)) * (D_ * 2) + jfp * 128 + (lane & 7) * 16, smem, K1_JOB + n * 1024);
    }
#pragma unroll
    for (int c = 0; c < K1_RING; ++c) issue(c);
#pragma unroll
    for (int c = 0; c < 8; ++c) {
        if (c == 0) WAIT_V(28);
        else if (c <= 4) WAIT_V(21);
        else if (c == 5) WAIT_V(14);
        else if (c == 6) WAIT_V(7);
        else WAIT_V(0);
        WAIT_L0();
        BAR();
        const int slot = (c % K1_RING) * K1_SLOT;
        bf16x8 wf[3];
#pragma unroll
        for (int nt = 0; nt < 3; ++nt) wf[nt] = lds_read_tr(smem, slot + woff[nt], slot + woff[nt] + 4 * 192);
        SB();
        if (c >= 1 && c - 1 + K1_RING < 8) issue(c - 1 + K1_RING);
        SB();
#pragma unroll
        for (int nt = 0; nt < 3; ++nt) cs[nt] = mfma16(wf[nt], ones, cs[nt]);
        SB();
    }
    if (m == 0) {
#pragma unroll
        for (int nt = 0; nt < 3; ++nt) *(f32x4*)(smem + K1_CSP + ((w >> 1) * 96 + (3 * (w & 1) + nt) * 16 + 4 * g) * 4) = cs[nt];
    }
    WAIT_L0();
    BAR();
    if (w < 2) {
        const int t = w * 64 + lane;
        const float* csp = (const float*)(smem + K1_CSP);
        if (t < 96) ((float*)(smem + K1_CS))[t] = csp[t] + csp[96 + t];
    }
    WAIT_L0();
    BAR();
    if (w == 0) {
        const int G = lane >> 4, nh = G & 1, hh = G >> 1, rq = (lane >> 2) & 3, pp = lane & 3;
#pragma unroll
        for (int t = 0; t < 2; ++t) {
            const int o = K1_JOB + (4 * hh + rq) * 128 + t * 64 + nh * 32 + pp * 8;
            st16(ws + WS_WOT + (size_t)(jfp * 32 + js) * 2048 + t * 1024 + lane * 16, __builtin_bit_cast(u32x4, lds_read_tr(smem, o, o + 8 * 128)));
        }
    }
}

template <int NV, class Warm>
DEVI void qkv_body(char* __restrict__ ws, char* smem, int rb, int ct, int w, int lane, Warm warm DIAG_ARG) {
    const int m = lane & 15, g = lane >> 4;
    const int wr = w & 1, wc = w >> 1;
    int xoff[4][2];
#pragma unroll
    for (int tt = 0; tt < 4; ++tt)
#pragma unroll
        for (int s = 0; s < 2; ++s) {
            const int r = 64 * wr + 16 * (tt ^ (2 * wc)) + m;
            xoff[tt][s] = r * 128 + (((4 * s + g) ^ ((r >> 1) & 7)) << 4);
        }
    const int wq = (lane >> 2) & 3, wp = lane & 3;
    int woff[3];
#pragma unroll
    for (int nt = 0; nt < 3; ++nt) woff[nt] = K1_XB + (8 * g + wq) * 192 + wp * 8 + (((3 * wc + nt) ^ (g & 1)) << 5);
    f32x4 acc[4][3], ssum[2], ssq[2];
#pragma unroll
    for (int tt = 0; tt < 4; ++tt)
#pragma unroll
        for (int nt = 0; nt < 3; ++nt) acc[tt][nt] = f32x4{0, 0, 0, 0};
#pragma unroll
    for (int tt = 0; tt < 2; ++tt) {
        ssum[tt] = f32x4{0, 0, 0, 0};
        ssq[tt] = f32x4{0, 0, 0, 0};
    }
    const bf16x8 ones = ones8();
    bf16x8 xf[2][4], wf[2][3];
    auto rd = [&](int ks, int n) {
        const int slot = ((ks >> 1) % K1_RING) * K1_SLOT;
        if (n == 0 || (n >= 2 && n <= 4)) {
            const int tt = n == 0 ? 0 : n - 1;
            xf[ks & 1][tt] = lds_read16(smem, slot + xoff[tt][ks & 1]);
        } else {
            const int nt = n == 1 ? 0 : n - 4;
            const int o = slot + woff[nt] + (ks & 1) * 32 * 192;
            wf[ks & 1][nt] = lds_read_tr(smem, o, o + 4 * 192);
        }
    };
    BAR();
    STAMP(1);
#pragma unroll
    for (int n = 0; n < 7; ++n) rd(0, n);
    SB();
#pragma unroll
    for (int ks = 0; ks < 16; ++ks) {
        const int b = ks & 1;
        const int first = b ? 6 : 0;
#pragma unroll
        for (int n = 0; n < 16; ++n) {
            if (b && ks < 15 && n == first) {
                WAIT_L0();
                BAR();
                if (ks == K1_WARM_AT) warm();
                SB();
            }
            if (n < 12) {
                const int nt = n >> 2, tt = n & 3;
                acc[tt][nt] = (nt >= 3 - NV) ? mfma16(xf[b][tt], wf[b][nt], acc[tt][nt]) : mfma16(wf[b][nt], xf[b][tt], acc[tt][nt]);
            } else if (n < 14) {
                ssum[n - 12] = mfma16(ones, xf[b][n - 12], ssum[n - 12]);
            } else {
                ssq[n - 14] = mfma16(xf[b][n - 14], xf[b][n - 14], ssq[n - 14]);
            }
            if (ks < 15 && n >= first && n < first + 7) rd(ks + 1, n - first);
            SB();
        }
    }
    STAMP(2);
    f32x2* stat = (f32x2*)(smem + K1_STAT);
    const float* csf = (const float*)(smem + K1_CS);
#pragma unroll
    for (int tt = 0; tt < 2; ++tt) {
        const int r = m & 3;
        const float sq = r == 0 ? ssq[tt][0] : r == 1 ? ssq[tt][1] : r == 2 ? ssq[tt][2] : ssq[tt][3];
        const float mean = ssum[tt][0] * (1.0f / D_);
        const float var = fmaxf(sq * (1.0f / D_) - mean * mean, 0.0f);
        if ((m >> 2) == g) stat[64 * wr + 32 * wc + 16 * tt + m] = f32x2{mean, __builtin_amdgcn_rsqf(var + EPS)};
    }
    WAIT_L0();
    BAR();
    BAR();
    STAMP(3);
#pragma unroll
    for (int nt = 0; nt < 3; ++nt) {
        const int ntl = 3 * wc + nt;
        const int col = ct * 96 + ntl * 16;
        const int head = (col & 511) >> 4;
        const bool isv = nt >= 3 - NV;
        const int isk = col >= 512;
        const float sc = (!isv && !isk) ? QSCALE : 1.0f;
        const f32x4 c4 = *(const f32x4*)(csf + ntl * 16 + 4 * g);
        const float c1 = csf[ntl * 16 + m];
        char* base = ws + (isv ? WS_VF : isk ? WS_KF : WS_QF) + head * 1024 +
                     (isv ? lane * 16 : ((lane & 15) + 16 * (lane >> 5) + 32 * ((lane >> 4) & 1)) * 16);
#pragma unroll
        for (int pr = 0; pr < 2; ++pr) {
            unsigned pk[2][2];
#pragma unroll
            for (int u = 0; u < 2; ++u) {
                const int tt = 2 * pr + u;
                const int tok0 = 64 * wr + 16 * (tt ^ (2 * wc));
                float v[4];
                if (!isv) {
                    const f32x2 st = stat[tok0 + m];
                    const float rs = st[1] * sc;
#pragma unroll
                    for (int r = 0; r < 4; ++r) v[r] = (acc[tt][nt][r] - st[0] * c4[r]) * rs;
                } else {
#pragma unroll
                    for (int r = 0; r < 4; ++r) {
                        const f32x2 st = stat[tok0 + 4 * g + r];
                        v[r] = (acc[tt][nt][r] - st[0] * c1) * st[1];
                    }
                }
                pk[u][0] = pack2(v[0], v[1]);
                pk[u][1] = pack2(v[2], v[3]);
            }
            swap32(pk[0][0], pk[1][0]);
            swap32(pk[0][1], pk[1][1]);
            const int tile = rb * 4 + 2 * wr + (pr ^ wc);
            st16(base + (size_t)tile * 32768, u32x4{pk[0][0], pk[0][1], pk[1][0], pk[1][1]});
        }
    }
}

__global__ __launch_bounds__(512, 2) void k1_qkv(const uint16_t* __restrict__ x, const uint16_t* __restrict__ wqkv, const uint16_t* __restrict__ wo,
                                                 const uint16_t* __restrict__ wg, const uint16_t* __restrict__ wu, const uint16_t* __restrict__ wd,
                                                 char* __restrict__ ws) {
    extern __shared__ __attribute__((aligned(16))) char smem[];
    CODE_MARK(sq_code_k1);
    DIAG_DECL;
    const int lane = threadIdx.x & 63, wv = __builtin_amdgcn_readfirstlane(threadIdx.x >> 6), w = wv & 3;
    const int xcd = blockIdx.x & 7, idx = blockIdx.x >> 3;
    const int rb = K1_RB(xcd, idx), ct = K1_CT(xcd, idx);
    if (wv >= 4) {
        qkv_loader(x, wqkv, wo, ws, smem, rb, ct, w, lane);
        return;
    }
    if (blockIdx.x < 32 && threadIdx.x == 0) *(unsigned*)(ws + WS_CNT + (size_t)blockIdx.x * 256) = 0u;
    if (w == 0 && (idx & 3) == 0) TOUCH_CODE(sq_code_k1, sq_code_k2, lane, K1_SCR);
    auto warm = [&]() {
        const int gw = blockIdx.x * 4 + w;
        touch_lines((const char*)wg + (size_t)gw * 1792, 1792, lane < 14 ? lane : 13, K1_SCR);
        touch_lines((const char*)wu + (size_t)gw * 1792, 1792, lane < 14 ? lane : 13, K1_SCR);
        touch_lines((const char*)wd + (size_t)gw * 1792, 1792, lane < 14 ? lane : 13, K1_SCR);
        touch_lines((const char*)wo + (size_t)gw * 512, 512, lane & 3, K1_SCR);
        if (blockIdx.x < 8 && w == 1) TOUCH_CODE(sq_code_k2, sq_code_end, lane, K1_SCR);
    };
    if (ct >= 11) qkv_body<3>(ws, smem, rb, ct, w, lane, warm DIAG_PASS);
    else if (ct == 10 && w >= 2) qkv_body<2>(ws, smem, rb, ct, w, lane, warm DIAG_PASS);
    else qkv_body<0>(ws, smem, rb, ct, w, lane, warm DIAG_PASS);
    WAIT_V(0);
    DIAG_END(ws, 0);
}

constexpr int K2_VS = 2064;
constexpr int K2_K = 0, K2_V = 24576, K2_Q = K2_V + 12 * K2_VS + 192, K2_JOB = K2_Q + 8192, K2_RED = K2_JOB + 32768;
constexpr int K2_SCR = K2_RED + 8 * 128 * 4, K2_LDS = K2_SCR + 256;

DEVI f32x16 splat16(float v) {
    f32x16 r;
#pragma unroll
    for (int i = 0; i < 16; ++i) r[i] = v;
    return r;
}
constexpr unsigned long long row_mask(int r) {
    unsigned long long m = 0;
    for (int l = 0; l < 64; ++l)
        if ((r & 3) + 8 * (r >> 2) + 4 * (l >> 5) > (l & 31)) m |= 1ull << l;
    return m;
}
template <int R>
DEVI unsigned sel_row(unsigned no, unsigned yes) { return __builtin_amdgcn_inverse_ballot_w64(row_mask(R)) ? yes : no; }
template <int R>
DEVI void merge_rows(const f32x16& sf, const f32x16& sl, f32x16& mg) {
    if constexpr (R < 16) {
        const float no = sl[R], yes = sf[R];
        mg[R] = __builtin_amdgcn_inverse_ballot_w64(row_mask(R)) ? yes : no;
        merge_rows<R + 1>(sf, sl, mg);
    }
}
DEVI f32x16 attn_merge(const f32x16& sf, const f32x16& sl) {
    f32x16 mg;
    merge_rows<0>(sf, sl, mg);
    return mg;
}
template <int R>
DEVI void split_rows(const f32x16& mg, float mx, unsigned (&pf)[8], unsigned (&pl)[8]) {
    if constexpr (R < 16) {
        const unsigned pk = pack2(__builtin_amdgcn_exp2f(mg[R] - mx), __builtin_amdgcn_exp2f(mg[R + 1] - mx));
        const unsigned mk = sel_row<R>(sel_row<R + 1>(0u, 0xffff0000u), 0xffffffffu);
        pf[R >> 1] = pk & mk;
        pl[R >> 1] = pk & ~mk;
        split_rows<R + 2>(mg, mx, pf, pl);
    }
}
DEVI void attn_split(const f32x16& mg, float mx, unsigned (&pf)[8], unsigned (&pl)[8]) { split_rows<0>(mg, mx, pf, pl); }
DEVI bf16x8 as_frag(const unsigned* p) { return __builtin_bit_cast(bf16x8, u32x4{p[0], p[1], p[2], p[3]}); }

template <bool FULL>
DEVI f32x16 attn_exact(char* smem, bf16x8 q, int koff, int vb0, int vb1, int i, int umin) {
    const float NEG = -__builtin_inff();
    const f32x16 zero = {0};
    const f32x16 sl = mfma32(lds_read16(smem, koff + 8 * 2048), q, zero);
    f32x16 sf = splat16(NEG);
    if (FULL || i >= umin) sf = mfma32(lds_read16(smem, koff), q, zero);
    const f32x16 mg = attn_merge(sf, sl);
    float mx = fmaxf(mg[0], mg[1]);
#pragma unroll
    for (int r = 2; r < 16; r += 2) mx = fmaxf(fmaxf(mx, mg[r]), mg[r + 1]);
#pragma unroll 1
    for (int t = 1; t < 8; ++t) {
        if (FULL || i + t >= umin) {
            const f32x16 s = mfma32(lds_read16(smem, koff + t * 2048), q, zero);
#pragma unroll
            for (int r = 0; r < 16; r += 2) mx = fmaxf(fmaxf(mx, s[r]), s[r + 1]);
        }
    }
    mx = swap32_max(mx);
    const f32x16 negm = splat16(-mx);
    f32x16 o = zero;
    unsigned pf[8], pl[8];
    attn_split(mg, mx, pf, pl);
#pragma unroll
    for (int s = 0; s < 2; ++s) {
        o = mfma32(lds_read16(smem, (s ? vb1 : vb0) + 8 * K2_VS), as_frag(pl + 4 * s), o);
        if (FULL || i >= umin) o = mfma32(lds_read16(smem, s ? vb1 : vb0), as_frag(pf + 4 * s), o);
    }
#pragma unroll 1
    for (int t = 1; t < 8; ++t) {
        if (FULL || i + t >= umin) {
            const f32x16 s = mfma32(lds_read16(smem, koff + t * 2048), q, negm);
            unsigned pk[8];
#pragma unroll
            for (int r = 0; r < 16; r += 2) pk[r >> 1] = pack2(__builtin_amdgcn_exp2f(s[r]), __builtin_amdgcn_exp2f(s[r + 1]));
            o = mfma32(lds_read16(smem, vb0 + t * K2_VS), as_frag(pk), o);
            o = mfma32(lds_read16(smem, vb1 + t * K2_VS), as_frag(pk + 4), o);
        }
    }
    return o;
}

template <bool FULL, class Mid>
DEVI f32x16 attn_fast(char* smem, bf16x8 q, int koff, int vb0, int vb1, int i, int umin, Mid mid DIAG_ARG) {
    const float NEG = -__builtin_inff();
    const f32x16 zero = {0};
    auto cinit = [&](int t) { return FULL ? zero : splat16(i + t >= umin ? 0.0f : NEG); };
    bf16x8 kf[2], vf[2][2];
    const bf16x8 kd = lds_read16(smem, koff + 8 * 2048), k0 = lds_read16(smem, koff);
    kf[1] = lds_read16(smem, koff + 2048);
    const bf16x8 vd0 = lds_read16(smem, vb0 + 8 * K2_VS), vz0 = lds_read16(smem, vb0);
    const bf16x8 vd1 = lds_read16(smem, vb1 + 8 * K2_VS), vz1 = lds_read16(smem, vb1);
    const f32x16 sl = mfma32(kd, q, zero);
    const f32x16 sf = mfma32(k0, q, cinit(0));
    f32x16 sc[2];
    sc[1] = mfma32(kf[1], q, cinit(1));
    kf[0] = lds_read16(smem, koff + 2 * 2048);
    vf[1][0] = lds_read16(smem, vb0 + K2_VS);
    vf[1][1] = lds_read16(smem, vb1 + K2_VS);
    SB();
    f32x16 o = zero;
    {
        unsigned pf[8], pl[8];
        attn_split(attn_merge(sf, sl), 0.0f, pf, pl);
        o = mfma32(vd0, as_frag(pl), o);
        o = mfma32(vz0, as_frag(pf), o);
        o = mfma32(vd1, as_frag(pl + 4), o);
        o = mfma32(vz1, as_frag(pf + 4), o);
    }
    SB();
    STAMP(2);
#pragma unroll
    for (int t = 1; t < 8; ++t) {
        const int b = t & 1;
        if (t < 7) {
            sc[b ^ 1] = mfma32(kf[b ^ 1], q, cinit(t + 1));
            if (t < 6) kf[b] = lds_read16(smem, koff + (t + 2) * 2048);
            vf[b ^ 1][0] = lds_read16(smem, vb0 + (t + 1) * K2_VS);
            vf[b ^ 1][1] = lds_read16(smem, vb1 + (t + 1) * K2_VS);
        }
        SB();
        unsigned pk[8];
#pragma unroll
        for (int r = 0; r < 8; r += 2) pk[r >> 1] = pack2(__builtin_amdgcn_exp2f(sc[b][r]), __builtin_amdgcn_exp2f(sc[b][r + 1]));
        const float e8 = __builtin_amdgcn_exp2f(sc[b][8]), e9 = __builtin_amdgcn_exp2f(sc[b][9]);
        SB();
        o = mfma32(vf[b][0], as_frag(pk), o);
        SB();
        pk[4] = pack2(e8, e9);
#pragma unroll
        for (int r = 10; r < 16; r += 2) pk[r >> 1] = pack2(__builtin_amdgcn_exp2f(sc[b][r]), __builtin_amdgcn_exp2f(sc[b][r + 1]));
        SB();
        o = mfma32(vf[b][1], as_frag(pk + 4), o);
        SB();
        if (t == 5) {
            STAMP(3);
            mid();
            STAMP(4);
        }
    }
    return o;
}

template <bool FULL, class Mid>
DEVI void attn_task(char* smem, char* __restrict__ ws, bf16x8 q, int rb, int i, int e, int p, int lane, Mid mid DIAG_ARG) {
    const int a = lane & 31, h = lane >> 5;
    const int koff = K2_K + e * 1024 + i * 2048 + lane * 16;
    const bool isdata = a < 16;
    const int vb0 = K2_V + i * K2_VS + (isdata ? e * 1024 + h * 256 + a * 16 : 2048);
    const int vb1 = vb0 + (isdata ? 512 : 0);
    const int umin = FULL ? 0 : 8 - 4 * rb;
    f32x16 o = attn_fast<FULL>(smem, q, koff, vb0, vb1, i, umin, mid DIAG_PASS);
    float mag = 0.f;
#pragma unroll
    for (int r = 0; r < 8; ++r) mag += __builtin_fabsf(o[r]);
    const bool safe = o[8] >= 0x1p-60f && o[8] <= 0x1p60f && mag <= 0x1p120f;
    if (__builtin_amdgcn_ballot_w64(!safe) != 0) {
        o = attn_exact<FULL>(smem, q, koff, vb0, vb1, i, umin);
    }
    const float inv = __builtin_amdgcn_rcpf(o[8]);
    const u32x4 ov = {pack2(o[0] * inv, o[1] * inv), pack2(o[2] * inv, o[3] * inv), pack2(o[4] * inv, o[5] * inv), pack2(o[6] * inv, o[7] * inv)};
    st16(ws + WS_OF + (size_t)((rb * 4 + i) * 32 + 2 * p + e) * 1024 + lane * 16, ov);
}

constexpr int G64_RED = 0, G64_T = 49152;
DEVI void oproj_phase(const uint16_t* __restrict__ x, char* __restrict__ ws, char* smem, int tb, int fp, int w, int lane DIAG_ARG) {
    constexpr int NS = 8;
    const int tt = w & 1, ft = w >> 1;
    const char* wt = ws + WS_WOT + (size_t)fp * 65536 + (size_t)(w * NS) * 2048 + lane * 16;
    bf16x8 fa[NS][2], fb[NS][2];
#pragma unroll
    for (int s = 0; s < NS; ++s)
#pragma unroll
        for (int q = 0; q < 2; ++q) fa[s][q] = *(const bf16x8*)(wt + s * 2048 + (q ^ (w >> 1)) * 1024);
    u32x4 xr[2];
#pragma unroll
    for (int n = 0; n < 2; ++n) {
        const int r = (w * 2 + n) * 8 + (lane >> 3);
        xr[n] = *(const u32x4*)((const char*)x + (size_t)(tb * 64 + r) * 1024 + fp * 128 + (lane & 7) * 16);
    }
    SB();
    BAR();
    SB();
    STAMP(1);
    const __amdgpu_buffer_rsrc_t rs = __builtin_amdgcn_make_buffer_rsrc(ws + WS_OF, 0, 2 << 20, 0x00020000);
#pragma unroll
    for (int s = 0; s < NS; ++s) {
#pragma unroll
        for (int q = 0; q < 2; ++q) fb[s][q] = load_dev(rs, ((tb * 2 + (q ^ (w & 1))) * 32 + w * NS) * 1024 + lane * 16, s * 1024);
        SB();
    }
#pragma unroll
    for (int n = 0; n < 2; ++n) {
        const int r = (w * 2 + n) * 8 + (lane >> 3);
        *(u32x4*)(smem + G64_T + r * 128 + (((lane & 7) ^ ((r >> 1) & 7)) << 4)) = xr[n];
    }
    SB();
    f32x16 acc[4];
#pragma unroll
    for (int q = 0; q < 4; ++q) acc[q] = f32x16{0};
#pragma unroll
    for (int s = 0; s < NS; ++s) {
#pragma unroll
        for (int q = 0; q < 4; ++q) acc[q] = mfma32(fa[s][q >> 1], fb[s][q & 1], acc[q]);
        SB();
    }
    STAMP(2);
#pragma unroll
    for (int q = 1; q < 4; ++q) {
        char* dst = smem + G64_RED + ((w ^ q) * 3 + q - 1) * 4096 + lane * 16;
#pragma unroll
        for (int r = 0; r < 4; ++r)
            *(f32x4*)(dst + r * 1024) = f32x4{acc[q][4 * r], acc[q][4 * r + 1], acc[q][4 * r + 2], acc[q][4 * r + 3]};
    }
    WAIT_L0();
    BAR();
    f32x16 o = acc[0];
#pragma unroll
    for (int q = 0; q < 3; ++q) {
        const char* src = smem + G64_RED + (w * 3 + q) * 4096 + lane * 16;
#pragma unroll
        for (int r = 0; r < 4; ++r) {
            const f32x4 v = *(const f32x4*)(src + r * 1024);
#pragma unroll
            for (int k = 0; k < 4; ++k) o[4 * r + k] += v[k];
        }
    }
    STAMP(3);
    const int tok = lane & 31, h = lane >> 5;
    const int r = tt * 32 + tok;
    unsigned pk[8];
    float sum = 0.f, sq = 0.f;
#pragma unroll
    for (int qd = 0; qd < 4; ++qd) {
        const u32x2 xv = *(const u32x2*)(smem + G64_T + r * 128 + (((ft * 4 + qd) ^ ((r >> 1) & 7)) << 4) + h * 8);
        pk[2 * qd] = pack2(o[4 * qd] + bf_lo(xv[0]), o[4 * qd + 1] + bf_hi(xv[0]));
        pk[2 * qd + 1] = pack2(o[4 * qd + 2] + bf_lo(xv[1]), o[4 * qd + 3] + bf_hi(xv[1]));
#pragma unroll
        for (int k = 0; k < 2; ++k) {
            const float lo = bf_lo(pk[2 * qd + k]), hi = bf_hi(pk[2 * qd + k]);
            sum += lo + hi;
            sq += lo * lo + hi * hi;
        }
    }
    char* hdst = ws + WS_HF + (size_t)((tb * 2 + tt) * 32 + (fp * 2 + ft) * 2) * 1024 + lane * 16;
    st16(hdst, u32x4{pk[0], pk[1], pk[2], pk[3]});
    st16(hdst + 1024, u32x4{pk[4], pk[5], pk[6], pk[7]});
    sum = swap32_add(sum);
    sq = swap32_add(sq);
    if (h == 0) ((f32x2*)(ws + WS_ST2))[(fp * 2 + ft) * S_ + tb * 64 + r] = f32x2{sum, sq};
}

__global__ __launch_bounds__(512, 2) void k2_attn(const uint16_t* __restrict__ x, const uint16_t* __restrict__ wg, const uint16_t* __restrict__ wu,
                                                  const uint16_t* __restrict__ wd, char* __restrict__ ws) {
    extern __shared__ __attribute__((aligned(16))) char smem[];
    CODE_MARK(sq_code_k2);
    DIAG_DECL;
    const int tid = threadIdx.x, lane = tid & 63, w = __builtin_amdgcn_readfirstlane(tid >> 6);
    const int xcd = blockIdx.x & 7, idx = blockIdx.x >> 3;
    const int rb = 2 * xcd + (idx >> 4), p = idx & 15;
    const int i = w & 3, e = w >> 2;
    if (w == 7 && (idx & 3) == 0) TOUCH_CODE(sq_code_k2, sq_code_k4, lane, K2_SCR);
#pragma unroll
    for (int n = 0; n < 6; ++n) {
        const int pc = 6 * w + n;
        const int isv = pc >= 24, u = (pc % 24) >> 1, half = pc & 1;
        int kt = 4 * rb - 8 + u;
        kt = kt < 0 ? 0 : kt;
        const char* src = ws + (isv ? WS_VF : WS_KF) + (size_t)(kt * 16 + p) * 2048 + half * 1024 + lane * 16;
        glds16(src, smem, (isv ? K2_V + u * K2_VS : K2_K + u * 2048) + half * 1024);
    }
    glds16(ws + WS_QF + (size_t)((rb * 4 + i) * 32 + 2 * p + e) * 1024 + lane * 16, smem, K2_Q + w * 1024);
    if (tid < 48) *(unsigned*)(smem + K2_V + (tid >> 2) * K2_VS + 2048 + (tid & 3) * 4) = 0x3F803F80u;
    const int bid = blockIdx.x;
    const int job = K2_JOB + w * 4096;
    if (bid < 112) {
        const int cg = bid % 28, s = 8 * (bid / 28) + w;
#pragma unroll
        for (int n = 0; n < 4; ++n) {
            const uint16_t* src = (n >> 1) ? wu : wg;
            const int row = 16 * s + 8 * (n & 1) + (lane >> 3);
            glds16((const char*)src + (size_t)row * (F_ * 2) + cg * 128 + (lane & 7) * 16, smem, job + n * 1024);
        }
    } else if (bid < 224) {
        const int s = bid - 112;
#pragma unroll
        for (int n = 0; n < 2; ++n) {
            const int row = 16 * s + 8 * n + (lane >> 3);
            glds16((const char*)wd + (size_t)row * (D_ * 2) + w * 128 + (lane & 7) * 16, smem, job + n * 1024);
        }
    }
    if (bid < 112) WAIT_V(4);
    else if (bid < 224) WAIT_V(2);
    else WAIT_V(0);
    WAIT_L0();
    BAR();
    STAMP(1);
    const bf16x8 q = lds_read16(smem, K2_Q + w * 1024 + lane * 16);
    auto retile = [&]() {
        WAIT_V(0);
        const int G = lane >> 4, nh = G & 1, hh = G >> 1, rq = (lane >> 2) & 3, pp = lane & 3;
        if (bid < 112) {
            const int cg = bid % 28, kq = bid / 28, s = 8 * kq + w;
            float* red = (float*)(smem + K2_RED);
#pragma unroll
            for (int t = 0; t < 4; ++t) {
                const int o = job + nh * 2048 + (4 * hh + rq) * 128 + t * 32 + pp * 8;
                const bf16x8 f = lds_read_tr(smem, o, o + 8 * 128);
                const int T = 4 * cg + t, j = T / 7, t7 = T % 7;
                const u32x4 u = __builtin_bit_cast(u32x4, f);
                st16(ws + WS_WGU + (size_t)((j * 32 + s) * 7 + t7) * 1024 + lane * 16, u);
                float sum = 0.f;
#pragma unroll
                for (int r = 0; r < 4; ++r) sum += bf_lo(u[r]) + bf_hi(u[r]);
                sum = swap32_add(sum);
                if (lane < 32) red[w * 128 + nh * 64 + t * 16 + (lane & 15)] = sum;
            }
        } else if (bid < 224) {
            char* dst = ws + WS_WDT + (size_t)(w * 112 + bid - 112) * 2048 + lane * 16;
#pragma unroll
            for (int t = 0; t < 2; ++t) {
                const int o = job + (4 * hh + rq) * 128 + t * 64 + nh * 32 + pp * 8;
                st16(dst + t * 1024, __builtin_bit_cast(u32x4, lds_read_tr(smem, o, o + 8 * 128)));
            }
        }
        SB();
    };
    if (rb >= 2) attn_task<true>(smem, ws, q, rb, i, e, p, lane, retile DIAG_PASS);
    else attn_task<false>(smem, ws, q, rb, i, e, p, lane, retile DIAG_PASS);
    STAMP(5);
    if (bid < 112) {
        const int cg = bid % 28, kq = bid / 28;
        const float* red = (const float*)(smem + K2_RED);
        WAIT_L0();
        BAR();
        if (tid < 128) {
            float sum = 0.f;
#pragma unroll
            for (int ww = 0; ww < 8; ++ww) sum += red[ww * 128 + tid];
            ((float*)(ws + WS_CSP))[(kq * 2 + (tid >> 6)) * F_ + cg * 64 + (tid & 63)] = sum;
        }
    }
    WAIT_V0();
    BAR();
    if (tid == 0) arrive(counter(ws, 0, rb));
    if (w >= 4) {
        if (w == 4) await(counter(ws, 0, rb), 16);
        BAR();
        BAR();
        return;
    }
    oproj_phase(x, ws, smem, 4 * xcd + (idx >> 3), idx & 7, w, lane DIAG_PASS);
    WAIT_V(0);
    DIAG_END(ws, 1);
}

constexpr int K4_A = 28672, K4_SLOT = 45056, K4_RING = 3, K4_CS = K4_SLOT * K4_RING, K4_SCR = K4_CS + 224 * 4;
constexpr int K4_WX = K4_SCR + 256, K4_LDS = K4_WX + 12 * 2048, K5_T = K4_WX;

#define K5_WIN 7
#define WD_SCHED {2, 2, 2, 2, 2, 2, 2, 0}
#define WD_R0 0
constexpr int wd_first(int c) {
    constexpr int n[8] = WD_SCHED;
    int t = 0;
    for (int i = 0; i < c; ++i) t += n[i];
    return t;
}
constexpr int wd_extra(int c) { return 2 * (wd_first(c + 1) - wd_first(c)) + (c == WD_R0 ? 6 : 0); }
constexpr int wd_pending(int c) {
    return c == 0 ? 22 : c == 1 ? 11 + wd_extra(0) : c == 2 ? 11 + wd_extra(0) + wd_extra(1) : (c < 7 ? 11 : 0) + wd_extra(c - 2) + wd_extra(c - 1);
}
DEVI int wd_lds(int w, int s) { return s < 3 ? K4_WX + (w * 3 + s) * 2048 : (w < 2 ? 2 * K4_SLOT : 0) + ((w & 1) * 11 + s - 3) * 2048; }
template <int HI>
DEVI void down_phase(char* __restrict__ ws, uint16_t* __restrict__ out, char* smem, int tb, int fp, int w, int lane,
                     const bf16x8 (&fr)[14][2] DIAG_ARG) {
    constexpr int NS = 14, WIN = HI ? K5_WIN : NS;
    const int w8 = w + 4 * HI;
    const __amdgpu_buffer_rsrc_t rs = __builtin_amdgcn_make_buffer_rsrc(ws + WS_MF, 0, 7 << 20, 0x00020000);
    bf16x8 fb[NS][2];
    int vb[2];
#pragma unroll
    for (int q = 0; q < 2; ++q) vb[q] = ((tb * 2 + (q ^ (w & 1))) * 112 + w8 * NS) * 1024 + lane * 16;
    auto load = [&](int s) {
#pragma unroll
        for (int q = 0; q < 2; ++q) fb[s][q] = load_dev(rs, vb[q], s * 1024);
    };
#pragma unroll
    for (int s = 0; s < WIN; ++s) {
        load(s);
        SB();
    }
    bf16x8 fl[2][2];
    auto rdw = [&](int s) {
#pragma unroll
        for (int q = 0; q < 2; ++q) fl[s & 1][q] = lds_read16(smem, wd_lds(w, s) + (q ^ (w >> 1)) * 1024 + lane * 16);
    };
    if (!HI) rdw(0);
    const int tt = w & 1, ft = w >> 1;
    f32x16 acc[4];
#pragma unroll
    for (int q = 0; q < 4; ++q) acc[q] = f32x16{0};
    u32x4 hv;
#pragma unroll
    for (int s = 0; s < NS; ++s) {
        if (!HI && s + 1 < NS) rdw(s + 1);
        SB();
#pragma unroll
        for (int q = 0; q < 4; ++q) acc[q] = mfma32(HI ? fr[s][q >> 1] : fl[s & 1][q >> 1], fb[s][q & 1], acc[q]);
        SB();
        if (WIN + 2 * s < NS) load(WIN + 2 * s);
        if (WIN + 2 * s + 1 < NS) load(WIN + 2 * s + 1);
        if (s == (HI ? (NS - WIN) / 2 + 1 : 0)) hv = *(const u32x4*)(ws + WS_HF + (size_t)((tb * 2 + tt) * 32 + (fp * 2 + ft) * 2 + HI) * 1024 + lane * 16);
        SB();
    }
    STAMP(4);
    auto piece = [&](int sw, int hi, int p) {
        const int own = sw < 2 ? 2 * K4_SLOT + (sw & 1) * 11 * 2048 : (sw & 1) * 11 * 2048;
        return !hi ? own + p * 2048 : p < 5 ? K4_SLOT + (sw * 5 + p) * 2048 : own + (p + 2) * 2048;
    };
    if (HI) BAR();
#pragma unroll
    for (int q = 0; q < 4; ++q)
#pragma unroll
        for (int hh = 0; hh < 2; ++hh) {
            if (q == 0 && hh == HI) continue;
            const int id = q * 2 + hh;
            char* dst = smem + piece(w, HI, id - (id > HI ? 1 : 0)) + lane * 16;
#pragma unroll
            for (int r = 0; r < 2; ++r)
                *(f32x4*)(dst + r * 1024) =
                    f32x4{acc[q][8 * hh + 4 * r], acc[q][8 * hh + 4 * r + 1], acc[q][8 * hh + 4 * r + 2], acc[q][8 * hh + 4 * r + 3]};
        }
    if (!HI) BAR();
    WAIT_L0();
    BAR();
    float o[8];
#pragma unroll
    for (int k = 0; k < 8; ++k) o[k] = acc[0][8 * HI + k];
#pragma unroll
    for (int q = 0; q < 4; ++q)
#pragma unroll
        for (int hi = 0; hi < 2; ++hi) {
            if (q == 0 && hi == HI) continue;
            const int id = q * 2 + HI;
            const char* src = smem + piece(w ^ q, hi, id - (id > hi ? 1 : 0)) + lane * 16;
#pragma unroll
            for (int r = 0; r < 2; ++r) {
                const f32x4 v = *(const f32x4*)(src + r * 1024);
#pragma unroll
                for (int k = 0; k < 4; ++k) o[4 * r + k] += v[k];
            }
        }
    STAMP(5);
    const int tok = lane & 31, h = lane >> 5;
    const int r = tt * 32 + tok;
#pragma unroll
    for (int qd = 0; qd < 2; ++qd) {
        const unsigned a = hv[2 * qd], b = hv[2 * qd + 1];
        const u32x2 v = {pack2(o[4 * qd] + bf_lo(a), o[4 * qd + 1] + bf_hi(a)), pack2(o[4 * qd + 2] + bf_lo(b), o[4 * qd + 3] + bf_hi(b))};
        *(u32x2*)(smem + K5_T + r * 128 + (((ft * 4 + 2 * HI + qd) ^ ((r >> 1) & 7)) << 4) + h * 8) = v;
    }
    WAIT_L0();
    BAR();
    const int rr = w8 * 8 + (lane >> 3), cp = lane & 7;
    const u32x4 v = *(const u32x4*)(smem + K5_T + rr * 128 + cp * 16);
    *(u32x4*)((char*)out + (size_t)(tb * 64 + rr) * 1024 + fp * 128 + ((cp ^ ((rr >> 1) & 7)) << 4)) = v;
    WAIT_V(0);
}

__global__ __launch_bounds__(512, 2) void k4_mlp(char* __restrict__ ws, uint16_t* __restrict__ out) {
    extern __shared__ __attribute__((aligned(16))) char smem[];
    CODE_MARK(sq_code_k4);
    DIAG_DECL;
    const int tid = threadIdx.x, lane = tid & 63, wv = __builtin_amdgcn_readfirstlane(tid >> 6), w = wv & 3;
    const int xcd = blockIdx.x & 7, idx = blockIdx.x >> 3;
    const int rb = K4_RB(xcd, idx), j = K4_J(xcd, idx);
    const int tile = rb * 4 + w;
    const int tb = 4 * xcd + (idx >> 3), fp = idx & 7;
    if (wv >= 4) {
        const char* asrc = ws + WS_WGU + (size_t)j * (32 * 7168) + w * 7168 + lane * 16;
        const char* bsrc = ws + WS_HF + (size_t)tile * 32768 + lane * 16;
        auto issue = [&](int c) {
            const int slot = (c % K4_RING) * K4_SLOT;
#pragma unroll
            for (int n = 0; n < 4; ++n) glds16(bsrc + (c * 4 + n) * 1024, smem, slot + K4_A + (w * 4 + n) * 1024);
#pragma unroll
            for (int n = 0; n < 7; ++n) glds16(asrc + c * 28672 + n * 1024, smem, slot + w * 7168 + n * 1024);
        };
        const char* wdt = ws + WS_WDT + (size_t)fp * (112 * 2048) + lane * 16;
        bf16x8 fr[14][2];
        auto wd_regs = [&](int s0, int s1) {
#pragma unroll
            for (int s = s0; s < s1; ++s)
#pragma unroll
                for (int q = 0; q < 2; ++q) fr[s][q] = *(const bf16x8*)(wdt + (size_t)((w + 4) * 14 + s) * 2048 + (q ^ (w >> 1)) * 1024);
        };
        auto wd_stage = [&](int s0, int s1) {
#pragma unroll
            for (int s = s0; s < s1; ++s)
#pragma unroll
                for (int q = 0; q < 2; ++q) glds16(wdt + (size_t)(w * 14 + s) * 2048 + q * 1024, smem, wd_lds(w, s) + q * 1024);
        };
#pragma unroll
        for (int c = 0; c < K4_RING; ++c) issue(c);
        static_for<0, 8>([&](auto cc) {
            constexpr int c = decltype(cc)::value;
            if constexpr (c < 7) wait_vm<wd_pending(c)>();
            else if (w < 2) wait_vm<wd_pending(7) + 22>();
            else wait_vm<wd_pending(7)>();
            BAR();
            if (c >= 1 && c - 1 + K4_RING < 8) issue(c - 1 + K4_RING);
            SB();
            wd_regs(wd_first(c), wd_first(c + 1));
            if (c == WD_R0) wd_stage(0, 3);
            SB();
            if ((c == 6 && w < 2) || (c == 7 && w >= 2)) wd_stage(3, 14);
        });
        WAIT_V0();
        STAMP(1);
        BAR();
        STAMP(2);
        if (w == 0) await(counter(ws, 1, tb >> 1), 16);
        STAMP(3);
        BAR();
        down_phase<1>(ws, out, smem, tb, fp, w, lane, fr DIAG_PASS);
        DIAG_END(ws, 2);
        return;
    }
    if (w == 0 && (idx & 3) == 0) TOUCH_CODE(sq_code_k4, sq_code_end, lane, K4_SCR);
    const int tok = lane & 31, h = lane >> 5;
    f32x2 st[8];
#pragma unroll
    for (int n = 0; n < 8; ++n) st[n] = ((const f32x2*)(ws + WS_ST2))[(h * 8 + n) * S_ + tile * 32 + tok];
    {
        const int t = tid < 224 ? tid : 0;
        const int gu = t >= 112, col = j * 112 + (gu ? t - 112 : t);
        float csq[4];
#pragma unroll
        for (int n = 0; n < 4; ++n) csq[n] = ((const float*)(ws + WS_CSP))[(n * 2 + gu) * F_ + col];
        if (tid < 224) ((float*)(smem + K4_CS))[tid] = (csq[0] + csq[1]) + (csq[2] + csq[3]);
    }
    float sum = 0.f, sq = 0.f;
#pragma unroll
    for (int n = 0; n < 8; ++n) {
        sum += st[n][0];
        sq += st[n][1];
    }
    sum = swap32_add(sum);
    sq = swap32_add(sq);
    const float mean = sum * (1.0f / D_);
    const float rstd = __builtin_amdgcn_rsqf(fmaxf(sq * (1.0f / D_) - mean * mean, 0.0f) + EPS);
    f32x16 acc[7];
#pragma unroll
    for (int t = 0; t < 7; ++t) acc[t] = f32x16{0};
    bf16x8 af[2][7], bfr[2];
    auto rdB = [&](int s) {
        bfr[s & 1] = lds_read16(smem, ((s >> 2) % K4_RING) * K4_SLOT + K4_A + (w * 4 + (s & 3)) * 1024 + lane * 16);
    };
    auto rdA = [&](int s, int t) {
        af[s & 1][t] = lds_read16(smem, ((s >> 2) % K4_RING) * K4_SLOT + ((s & 3) * 7 + t) * 1024 + lane * 16);
    };
    WAIT_L0();
    BAR();
    STAMP(1);
    rdB(0);
#pragma unroll
    for (int t = 0; t < 7; ++t) rdA(0, t);
    SB();
#pragma unroll
    for (int s = 0; s < 32; ++s) {
        const int b = s & 1;
        const bool edge = (s & 3) == 3;
#pragma unroll
        for (int t = 0; t < 7; ++t) {
            if (edge && s < 31 && t == 4) {
                WAIT_L0();
                BAR();
                SB();
            }
            acc[t] = mfma32(af[b][t], bfr[b], acc[t]);
            if (s < 31) {
                if (!edge) {
                    if (t == 0) rdB(s + 1);
                    rdA(s + 1, t);
                } else if (t == 4) {
                    rdB(s + 1);
                    rdA(s + 1, 0);
                    rdA(s + 1, 1);
                } else if (t == 5) {
                    rdA(s + 1, 2);
                    rdA(s + 1, 3);
                    rdA(s + 1, 4);
                } else if (t == 6) {
                    rdA(s + 1, 5);
                    rdA(s + 1, 6);
                }
            }
            SB();
        }
    }
    STAMP(2);
    const float* cs = (const float*)(smem + K4_CS);
    char* dst = ws + WS_MF + (size_t)(tile * 112 + j * 7) * 1024 + lane * 16;
#pragma unroll
    for (int t = 0; t < 7; ++t) {
        float v[8];
#pragma unroll
        for (int qd = 0; qd < 2; ++qd) {
            const f32x4 cg = *(const f32x4*)(cs + t * 16 + 8 * qd + 4 * h);
            const f32x4 cu = *(const f32x4*)(cs + 112 + t * 16 + 8 * qd + 4 * h);
#pragma unroll
            for (int r = 0; r < 4; ++r) {
                const float gt = (acc[t][4 * qd + r] - mean * cg[r]) * rstd;
                const float up = (acc[t][8 + 4 * qd + r] - mean * cu[r]) * rstd;
                v[4 * qd + r] = fmaxf(gt, 0.0f) * up;
            }
        }
        st16(dst + t * 1024, u32x4{pack2(v[0], v[1]), pack2(v[2], v[3]), pack2(v[4], v[5]), pack2(v[6], v[7])});
    }
    WAIT_V(0);
    BAR();
    if (tid == 0) arrive(counter(ws, 1, rb));
    BAR();
    STAMP(3);
    const bf16x8 none[14][2] = {};
    down_phase<0>(ws, out, smem, tb, fp, w, lane, none DIAG_PASS);
    CODE_MARK(sq_code_end);
    DIAG_END(ws, 2);
}

extern "C" void kernel_launch(void* const* d_in, const int* in_sizes, int n_in, void* d_out, int out_size, void* d_ws,
                              size_t ws_size, hipStream_t stream) {
    const uint16_t* x = (const uint16_t*)d_in[0];
    const uint16_t* wqkv = (const uint16_t*)d_in[1];
    const uint16_t* wo = (const uint16_t*)d_in[2];
    const uint16_t* wg = (const uint16_t*)d_in[4];
    const uint16_t* wu = (const uint16_t*)d_in[5];
    const uint16_t* wd = (const uint16_t*)d_in[6];
    char* ws = (char*)d_ws;
    (void)hipFuncSetAttribute((const void*)k1_qkv, hipFuncAttributeMaxDynamicSharedMemorySize, K1_LDS);
    (void)hipFuncSetAttribute((const void*)k2_attn, hipFuncAttributeMaxDynamicSharedMemorySize, K2_LDS);
    (void)hipFuncSetAttribute((const void*)k4_mlp, hipFuncAttributeMaxDynamicSharedMemorySize, K4_LDS);
    k1_qkv<<<256, 512, K1_LDS, stream>>>(x, wqkv, wo, wg, wu, wd, ws);
    k2_attn<<<256, 512, K2_LDS, stream>>>(x, wg, wu, wd, ws);
    k4_mlp<<<256, 512, K4_LDS, stream>>>(ws, (uint16_t*)d_out);
}
```

```cpp
#include <hip/hip_runtime.h>
#include <hip/hip_bf16.h>
#include <stdint.h>
#include <type_traits>

typedef __attribute__((ext_vector_type(8))) __bf16 bf16x8;
typedef __attribute__((ext_vector_type(2))) __bf16 bf16x2;
typedef __attribute__((ext_vector_type(4))) short s16x4;
typedef __attribute__((ext_vector_type(8))) short s16x8;
typedef __attribute__((ext_vector_type(2))) float f32x2;
typedef __attribute__((ext_vector_type(4))) float f32x4;
typedef __attribute__((ext_vector_type(16))) float f32x16;
typedef __attribute__((ext_vector_type(2))) unsigned u32x2;
typedef __attribute__((ext_vector_type(4))) unsigned u32x4;
typedef __attribute__((address_space(3))) char* lds_ptr;
typedef const __attribute__((address_space(1))) void* gptr_t;

#define DEVI __device__ __forceinline__
#define WAIT_V(n) asm volatile("s_waitcnt vmcnt(" #n ")" ::: "memory")
template <int N>
__device__ __forceinline__ void wait_vm() { asm volatile("s_waitcnt vmcnt(%0)" ::"n"(N) : "memory"); }
template <int I, int N, class F>
__device__ __forceinline__ void static_for(F f) {
    if constexpr (I < N) {
        f(std::integral_constant<int, I>{});
        static_for<I + 1, N>(f);
    }
}
#define WAIT_V0() __builtin_amdgcn_s_waitcnt(0x0070)
#define WAIT_L0() asm volatile("s_waitcnt lgkmcnt(0)" ::: "memory")
#define BAR() __builtin_amdgcn_s_barrier()
#define SB() __builtin_amdgcn_sched_barrier(0)

#define DIAG_DECL do { } while (0)
#define STAMP(ph) do { } while (0)
#define DIAG_END(ws, kid) do { } while (0)
#define DIAG_ARG
#define DIAG_PASS

constexpr int S_ = 2048, D_ = 512, F_ = 1792;
constexpr float EPS = 1e-5f;
constexpr float QSCALE = 0.25f * 1.44269504088896340736f;

constexpr size_t WS_QF = 0;
constexpr size_t WS_KF = WS_QF + (2u << 20);
constexpr size_t WS_VF = WS_KF + (2u << 20);
constexpr size_t WS_OF = WS_VF + (2u << 20);
constexpr size_t WS_HF = WS_OF + (2u << 20);
constexpr size_t WS_MF = WS_HF + (2u << 20);
constexpr size_t WS_WOT = WS_MF + (7u << 20);
constexpr size_t WS_WGU = WS_WOT + (512u << 10);
constexpr size_t WS_WDT = WS_WGU + (3584u << 10);
constexpr size_t WS_ST2 = WS_WDT + (1792u << 10);
constexpr size_t WS_CSP = WS_ST2 + (256u << 10);
constexpr size_t WS_DIAG = WS_CSP + (64u << 10);
constexpr size_t WS_CNT = WS_DIAG + (1u << 20);

DEVI void glds16(const void* g, char* smem_base, int lds_off) {
    __builtin_amdgcn_global_load_lds((gptr_t)g, (__attribute__((address_space(3))) void*)((lds_ptr)smem_base + lds_off), 16, 0, 0);
}
DEVI bf16x8 lds_read16(char* smem, int off) { return *(const bf16x8*)(smem + off); }
DEVI bf16x8 lds_read_tr(char* smem, int off0, int off1) {
    s16x4 a = __builtin_amdgcn_ds_read_tr16_b64_v4i16((__attribute__((address_space(3))) s16x4*)((lds_ptr)smem + off0));
    s16x4 b = __builtin_amdgcn_ds_read_tr16_b64_v4i16((__attribute__((address_space(3))) s16x4*)((lds_ptr)smem + off1));
    s16x8 c = __builtin_shufflevector(a, b, 0, 1, 2, 3, 4, 5, 6, 7);
    return __builtin_bit_cast(bf16x8, c);
}
#define K4_XA 2
#define K4_RB(xcd, idx) (((xcd) % K4_XA) * (16 / K4_XA) + ((idx) % (16 / K4_XA)))
#define K4_J(xcd, idx) (((xcd) / K4_XA) * (2 * K4_XA) + ((idx) / (16 / K4_XA)))
#define K1_XA 4
#define K1_RB(xcd, idx) (((xcd) % K1_XA) * (16 / K1_XA) + ((idx) % (16 / K1_XA)))
#define K1_CT(xcd, idx) (((xcd) / K1_XA) * (2 * K1_XA) + ((idx) / (16 / K1_XA)))
DEVI void st16(void* p, u32x4 v) {
    asm volatile("global_store_dwordx4 %0, %1, off sc1\n\ts_nop 1" ::"v"(p), "v"(v) : "memory");
}
typedef __attribute__((address_space(1))) unsigned* gcnt_t;
DEVI gcnt_t counter(char* ws, int edge, int rb) { return (gcnt_t)(ws + WS_CNT + (size_t)(edge * 16 + rb) * 256); }
DEVI void arrive(gcnt_t c) { __hip_atomic_fetch_add(c, 1u, __ATOMIC_RELAXED, __HIP_MEMORY_SCOPE_AGENT); }
DEVI void await(gcnt_t c, unsigned n) {
    for (int spin = 0; spin < (1 << 22); ++spin) {
        if (__hip_atomic_load(c, __ATOMIC_RELAXED, __HIP_MEMORY_SCOPE_AGENT) >= n) break;
        __builtin_amdgcn_s_sleep(1);
    }
}
#define LD_AUX 16
DEVI bf16x8 load_dev(__amdgpu_buffer_rsrc_t rs, int voff, int soff) {
    return __builtin_bit_cast(bf16x8, __builtin_amdgcn_raw_buffer_load_b128(rs, voff, soff, LD_AUX));
}
DEVI unsigned pack2(float a, float b) { return __builtin_bit_cast(unsigned, __builtin_convertvector(f32x2{a, b}, bf16x2)); }
DEVI float bf_lo(unsigned u) { return __builtin_bit_cast(float, u << 16); }
DEVI float bf_hi(unsigned u) { return __builtin_bit_cast(float, u & 0xffff0000u); }
DEVI float swap32_add(float v) {
    unsigned a = __builtin_bit_cast(unsigned, v), b = a;
    asm volatile("" : "+v"(b));
    auto r = __builtin_amdgcn_permlane32_swap(a, b, false, false);
    const unsigned r0 = r[0], r1 = r[1];
    return __builtin_bit_cast(float, r0) + __builtin_bit_cast(float, r1);
}
DEVI void swap32(unsigned& a, unsigned& b) {
    auto r = __builtin_amdgcn_permlane32_swap(a, b, false, false);
    const unsigned r0 = r[0], r1 = r[1];
    a = r0;
    b = r1;
}
DEVI float swap32_max(float v) {
    unsigned a = __builtin_bit_cast(unsigned, v), b = a;
    asm volatile("" : "+v"(b));
    auto r = __builtin_amdgcn_permlane32_swap(a, b, false, false);
    const unsigned r0 = r[0], r1 = r[1];
    return fmaxf(__builtin_bit_cast(float, r0), __builtin_bit_cast(float, r1));
}
DEVI f32x4 mfma16(bf16x8 a, bf16x8 b, f32x4 c) { return __builtin_amdgcn_mfma_f32_16x16x32_bf16(a, b, c, 0, 0, 0); }
DEVI f32x16 mfma32(bf16x8 a, bf16x8 b, f32x16 c) { return __builtin_amdgcn_mfma_f32_32x32x16_bf16(a, b, c, 0, 0, 0); }
DEVI bf16x8 ones8() {
    u32x4 o = {0x3F803F80u, 0x3F803F80u, 0x3F803F80u, 0x3F803F80u};
    return __builtin_bit_cast(bf16x8, o);
}

constexpr int K1_SLOT = 28672, K1_XB = 16384, K1_RING = 5;
constexpr int K1_STAT = K1_SLOT * K1_RING;
constexpr int K1_CSP = K1_STAT + 1024;
constexpr int K1_CS = K1_CSP + 1536;
constexpr int K1_JOB = K1_CS + 384, K1_LDS = K1_JOB + 2048;

DEVI void qkv_loader(const uint16_t* __restrict__ x, const uint16_t* __restrict__ wqkv, const uint16_t* __restrict__ wo, char* __restrict__ ws,
                     char* smem, int rb, int ct, int w, int lane) {
    const char* xsrc[4];
#pragma unroll
    for (int i = 0; i < 4; ++i) {
        const int r = 32 * w + 8 * i + (lane >> 3);
        const int c = (lane & 7) ^ ((r >> 1) & 7);
        xsrc[i] = (const char*)x + (size_t)(rb * 128 + r) * 1024 + c * 16;
    }
    const char* wsrc[3];
#pragma unroll
    for (int i = 0; i < 3; ++i) {
        const int ci = 64 * (3 * w + i) + lane;
        const int r = ci / 12, cp = ci % 12;
        const int c = cp ^ (((r >> 3) & 1) << 1);
        wsrc[i] = (const char*)wqkv + (size_t)r * 3072 + ct * 192 + c * 16;
    }
    auto issue = [&](int c) {
        const int slot = (c % K1_RING) * K1_SLOT;
#pragma unroll
        for (int i = 0; i < 4; ++i) glds16(xsrc[i] + c * 128, smem, slot + (32 * w + 8 * i) * 128);
#pragma unroll
        for (int i = 0; i < 3; ++i) glds16(wsrc[i] + (size_t)c * 64 * 3072, smem, slot + K1_XB + (3 * w + i) * 1024);
    };
    const int m = lane & 15, g = lane >> 4, wq = (lane >> 2) & 3, wp = lane & 3;
    int woff[3];
#pragma unroll
    for (int nt = 0; nt < 3; ++nt)
        woff[nt] = K1_XB + ((w >> 1) * 32 + 8 * g + wq) * 192 + wp * 8 + (((3 * (w & 1) + nt) ^ (g & 1)) << 5);
    const bf16x8 ones = ones8();
    f32x4 cs[3];
#pragma unroll
    for (int nt = 0; nt < 3; ++nt) cs[nt] = f32x4{0, 0, 0, 0};
    const int jfp = blockIdx.x & 7, js = blockIdx.x >> 3;
    if (w == 0) {
#pragma unroll
        for (int n = 0; n < 2; ++n) glds16((const char*)wo + (size_t)(16 * js + 8 * n + (lane >> 3)) * (D_ * 2) + jfp * 128 + (lane & 7) * 16, smem, K1_JOB + n * 1024);
    }
#pragma unroll
    for (int c = 0; c < K1_RING; ++c) issue(c);
#pragma unroll
    for (int c = 0; c < 8; ++c) {
        if (c == 0) WAIT_V(28);
        else if (c <= 4) WAIT_V(21);
        else if (c == 5) WAIT_V(14);
        else if (c == 6) WAIT_V(7);
        else WAIT_V(0);
        WAIT_L0();
        BAR();
        const int slot = (c % K1_RING) * K1_SLOT;
        bf16x8 wf[3];
#pragma unroll
        for (int nt = 0; nt < 3; ++nt) wf[nt] = lds_read_tr(smem, slot + woff[nt], slot + woff[nt] + 4 * 192);
        SB();
        if (c >= 1 && c - 1 + K1_RING < 8) issue(c - 1 + K1_RING);
        SB();
#pragma unroll
        for (int nt = 0; nt < 3; ++nt) cs[nt] = mfma16(wf[nt], ones, cs[nt]);
        SB();
    }
    if (m == 0) {
#pragma unroll
        for (int nt = 0; nt < 3; ++nt) *(f32x4*)(smem + K1_CSP + ((w >> 1) * 96 + (3 * (w & 1) + nt) * 16 + 4 * g) * 4) = cs[nt];
    }
    WAIT_L0();
    BAR();
    if (w < 2) {
        const int t = w * 64 + lane;
        const float* csp = (const float*)(smem + K1_CSP);
        if (t < 96) ((float*)(smem + K1_CS))[t] = csp[t] + csp[96 + t];
    }
    WAIT_L0();
    BAR();
    if (w == 0) {
        const int G = lane >> 4, nh = G & 1, hh = G >> 1, rq = (lane >> 2) & 3, pp = lane & 3;
#pragma unroll
        for (int t = 0; t < 2; ++t) {
            const int o = K1_JOB + (4 * hh + rq) * 128 + t * 64 + nh * 32 + pp * 8;
            st16(ws + WS_WOT + (size_t)(jfp * 32 + js) * 2048 + t * 1024 + lane * 16, __builtin_bit_cast(u32x4, lds_read_tr(smem, o, o + 8 * 128)));
        }
    }
}

template <int NV>
DEVI void qkv_body(char* __restrict__ ws, char* smem, int rb, int ct, int w, int lane DIAG_ARG) {
    const int m = lane & 15, g = lane >> 4;
    const int wr = w & 1, wc = w >> 1;
    int xoff[4][2];
#pragma unroll
    for (int tt = 0; tt < 4; ++tt)
#pragma unroll
        for (int s = 0; s < 2; ++s) {
            const int r = 64 * wr + 16 * (tt ^ (2 * wc)) + m;
            xoff[tt][s] = r * 128 + (((4 * s + g) ^ ((r >> 1) & 7)) << 4);
        }
    const int wq = (lane >> 2) & 3, wp = lane & 3;
    int woff[3];
#pragma unroll
    for (int nt = 0; nt < 3; ++nt) woff[nt] = K1_XB + (8 * g + wq) * 192 + wp * 8 + (((3 * wc + nt) ^ (g & 1)) << 5);
    f32x4 acc[4][3], ssum[2], ssq[2];
#pragma unroll
    for (int tt = 0; tt < 4; ++tt)
#pragma unroll
        for (int nt = 0; nt < 3; ++nt) acc[tt][nt] = f32x4{0, 0, 0, 0};
#pragma unroll
    for (int tt = 0; tt < 2; ++tt) {
        ssum[tt] = f32x4{0, 0, 0, 0};
        ssq[tt] = f32x4{0, 0, 0, 0};
    }
    const bf16x8 ones = ones8();
    bf16x8 xf[2][4], wf[2][3];
    auto rd = [&](int ks, int n) {
        const int slot = ((ks >> 1) % K1_RING) * K1_SLOT;
        if (n == 0 || (n >= 2 && n <= 4)) {
            const int tt = n == 0 ? 0 : n - 1;
            xf[ks & 1][tt] = lds_read16(smem, slot + xoff[tt][ks & 1]);
        } else {
            const int nt = n == 1 ? 0 : n - 4;
            const int o = slot + woff[nt] + (ks & 1) * 32 * 192;
            wf[ks & 1][nt] = lds_read_tr(smem, o, o + 4 * 192);
        }
    };
    BAR();
    STAMP(1);
#pragma unroll
    for (int n = 0; n < 7; ++n) rd(0, n);
    SB();
#pragma unroll
    for (int ks = 0; ks < 16; ++ks) {
        const int b = ks & 1;
        const int first = b ? 6 : 0;
#pragma unroll
        for (int n = 0; n < 16; ++n) {
            if (b && ks < 15 && n == first) {
                WAIT_L0();
                BAR();
                SB();
            }
            if (n < 12) {
                const int nt = n >> 2, tt = n & 3;
                acc[tt][nt] = (nt >= 3 - NV) ? mfma16(xf[b][tt], wf[b][nt], acc[tt][nt]) : mfma16(wf[b][nt], xf[b][tt], acc[tt][nt]);
            } else if (n < 14) {
                ssum[n - 12] = mfma16(ones, xf[b][n - 12], ssum[n - 12]);
            } else {
                ssq[n - 14] = mfma16(xf[b][n - 14], xf[b][n - 14], ssq[n - 14]);
            }
            if (ks < 15 && n >= first && n < first + 7) rd(ks + 1, n - first);
            SB();
        }
    }
    STAMP(2);
    f32x2* stat = (f32x2*)(smem + K1_STAT);
    const float* csf = (const float*)(smem + K1_CS);
#pragma unroll
    for (int tt = 0; tt < 2; ++tt) {
        const int r = m & 3;
        const float sq = r == 0 ? ssq[tt][0] : r == 1 ? ssq[tt][1] : r == 2 ? ssq[tt][2] : ssq[tt][3];
        const float mean = ssum[tt][0] * (1.0f / D_);
        const float var = fmaxf(sq * (1.0f / D_) - mean * mean, 0.0f);
        if ((m >> 2) == g) stat[64 * wr + 32 * wc + 16 * tt + m] = f32x2{mean, __builtin_amdgcn_rsqf(var + EPS)};
    }
    WAIT_L0();
    BAR();
    BAR();
    STAMP(3);
#pragma unroll
    for (int nt = 0; nt < 3; ++nt) {
        const int ntl = 3 * wc + nt;
        const int col = ct * 96 + ntl * 16;
        const int head = (col & 511) >> 4;
        const bool isv = nt >= 3 - NV;
        const int isk = col >= 512;
        const float sc = (!isv && !isk) ? QSCALE : 1.0f;
        const f32x4 c4 = *(const f32x4*)(csf + ntl * 16 + 4 * g);
        const float c1 = csf[ntl * 16 + m];
        char* base = ws + (isv ? WS_VF : isk ? WS_KF : WS_QF) + head * 1024 +
                     (isv ? lane * 16 : ((lane & 15) + 16 * (lane >> 5) + 32 * ((lane >> 4) & 1)) * 16);
#pragma unroll
        for (int pr = 0; pr < 2; ++pr) {
            unsigned pk[2][2];
#pragma unroll
            for (int u = 0; u < 2; ++u) {
                const int tt = 2 * pr + u;
                const int tok0 = 64 * wr + 16 * (tt ^ (2 * wc));
                float v[4];
                if (!isv) {
                    const f32x2 st = stat[tok0 + m];
                    const float rs = st[1] * sc;
#pragma unroll
                    for (int r = 0; r < 4; ++r) v[r] = (acc[tt][nt][r] - st[0] * c4[r]) * rs;
                } else {
#pragma unroll
                    for (int r = 0; r < 4; ++r) {
                        const f32x2 st = stat[tok0 + 4 * g + r];
                        v[r] = (acc[tt][nt][r] - st[0] * c1) * st[1];
                    }
                }
                pk[u][0] = pack2(v[0], v[1]);
                pk[u][1] = pack2(v[2], v[3]);
            }
            swap32(pk[0][0], pk[1][0]);
            swap32(pk[0][1], pk[1][1]);
            const int tile = rb * 4 + 2 * wr + (pr ^ wc);
            st16(base + (size_t)tile * 32768, u32x4{pk[0][0], pk[0][1], pk[1][0], pk[1][1]});
        }
    }
}

__global__ __launch_bounds__(512, 2) void k1_qkv(const uint16_t* __restrict__ x, const uint16_t* __restrict__ wqkv, const uint16_t* __restrict__ wo,
                                                 char* __restrict__ ws) {
    extern __shared__ __attribute__((aligned(16))) char smem[];
    DIAG_DECL;
    const int lane = threadIdx.x & 63, wv = __builtin_amdgcn_readfirstlane(threadIdx.x >> 6), w = wv & 3;
    const int xcd = blockIdx.x & 7, idx = blockIdx.x >> 3;
    const int rb = K1_RB(xcd, idx), ct = K1_CT(xcd, idx);
    if (wv >= 4) {
        qkv_loader(x, wqkv, wo, ws, smem, rb, ct, w, lane);
        return;
    }
    if (blockIdx.x < 32 && threadIdx.x == 0) *(unsigned*)(ws + WS_CNT + (size_t)blockIdx.x * 256) = 0u;
    if (ct >= 11) qkv_body<3>(ws, smem, rb, ct, w, lane DIAG_PASS);
    else if (ct == 10 && w >= 2) qkv_body<2>(ws, smem, rb, ct, w, lane DIAG_PASS);
    else qkv_body<0>(ws, smem, rb, ct, w, lane DIAG_PASS);
    WAIT_V(0);
    DIAG_END(ws, 0);
}

constexpr int K2_VS = 2064;
constexpr int K2_K = 0, K2_V = 24576, K2_Q = K2_V + 12 * K2_VS + 192, K2_JOB = K2_Q + 8192, K2_RED = K2_JOB + 32768;
constexpr int K2_LDS = K2_RED + 8 * 128 * 4;

DEVI f32x16 splat16(float v) {
    f32x16 r;
#pragma unroll
    for (int i = 0; i < 16; ++i) r[i] = v;
    return r;
}
constexpr unsigned long long row_mask(int r) {
    unsigned long long m = 0;
    for (int l = 0; l < 64; ++l)
        if ((r & 3) + 8 * (r >> 2) + 4 * (l >> 5) > (l & 31)) m |= 1ull << l;
    return m;
}
template <int R>
DEVI unsigned sel_row(unsigned no, unsigned yes) { return __builtin_amdgcn_inverse_ballot_w64(row_mask(R)) ? yes : no; }
template <int R>
DEVI void merge_rows(const f32x16& sf, const f32x16& sl, f32x16& mg) {
    if constexpr (R < 16) {
        const float no = sl[R], yes = sf[R];
        mg[R] = __builtin_amdgcn_inverse_ballot_w64(row_mask(R)) ? yes : no;
        merge_rows<R + 1>(sf, sl, mg);
    }
}
DEVI f32x16 attn_merge(const f32x16& sf, const f32x16& sl) {
    f32x16 mg;
    merge_rows<0>(sf, sl, mg);
    return mg;
}
template <int R>
DEVI void split_rows(const f32x16& mg, float mx, unsigned (&pf)[8], unsigned (&pl)[8]) {
    if constexpr (R < 16) {
        const unsigned pk = pack2(__builtin_amdgcn_exp2f(mg[R] - mx), __builtin_amdgcn_exp2f(mg[R + 1] - mx));
        const unsigned mk = sel_row<R>(sel_row<R + 1>(0u, 0xffff0000u), 0xffffffffu);
        pf[R >> 1] = pk & mk;
        pl[R >> 1] = pk & ~mk;
        split_rows<R + 2>(mg, mx, pf, pl);
    }
}
DEVI void attn_split(const f32x16& mg, float mx, unsigned (&pf)[8], unsigned (&pl)[8]) { split_rows<0>(mg, mx, pf, pl); }
DEVI bf16x8 as_frag(const unsigned* p) { return __builtin_bit_cast(bf16x8, u32x4{p[0], p[1], p[2], p[3]}); }

template <bool FULL>
DEVI f32x16 attn_exact(char* smem, bf16x8 q, int koff, int vb0, int vb1, int i, int umin) {
    const float NEG = -__builtin_inff();
    const f32x16 zero = {0};
    const f32x16 sl = mfma32(lds_read16(smem, koff + 8 * 2048), q, zero);
    f32x16 sf = splat16(NEG);
    if (FULL || i >= umin) sf = mfma32(lds_read16(smem, koff), q, zero);
    const f32x16 mg = attn_merge(sf, sl);
    float mx = fmaxf(mg[0], mg[1]);
#pragma unroll
    for (int r = 2; r < 16; r += 2) mx = fmaxf(fmaxf(mx, mg[r]), mg[r + 1]);
#pragma unroll 1
    for (int t = 1; t < 8; ++t) {
        if (FULL || i + t >= umin) {
            const f32x16 s = mfma32(lds_read16(smem, koff + t * 2048), q, zero);
#pragma unroll
            for (int r = 0; r < 16; r += 2) mx = fmaxf(fmaxf(mx, s[r]), s[r + 1]);
        }
    }
    mx = swap32_max(mx);
    const f32x16 negm = splat16(-mx);
    f32x16 o = zero;
    unsigned pf[8], pl[8];
    attn_split(mg, mx, pf, pl);
#pragma unroll
    for (int s = 0; s < 2; ++s) {
        o = mfma32(lds_read16(smem, (s ? vb1 : vb0) + 8 * K2_VS), as_frag(pl + 4 * s), o);
        if (FULL || i >= umin) o = mfma32(lds_read16(smem, s ? vb1 : vb0), as_frag(pf + 4 * s), o);
    }
#pragma unroll 1
    for (int t = 1; t < 8; ++t) {
        if (FULL || i + t >= umin) {
            const f32x16 s = mfma32(lds_read16(smem, koff + t * 2048), q, negm);
            unsigned pk[8];
#pragma unroll
            for (int r = 0; r < 16; r += 2) pk[r >> 1] = pack2(__builtin_amdgcn_exp2f(s[r]), __builtin_amdgcn_exp2f(s[r + 1]));
            o = mfma32(lds_read16(smem, vb0 + t * K2_VS), as_frag(pk), o);
            o = mfma32(lds_read16(smem, vb1 + t * K2_VS), as_frag(pk + 4), o);
        }
    }
    return o;
}

template <bool FULL, class Mid>
DEVI f32x16 attn_fast(char* smem, bf16x8 q, int koff, int vb0, int vb1, int i, int umin, Mid mid DIAG_ARG) {
    const float NEG = -__builtin_inff();
    const f32x16 zero = {0};
    auto cinit = [&](int t) { return FULL ? zero : splat16(i + t >= umin ? 0.0f : NEG); };
    bf16x8 kf[2], vf[2][2];
    const bf16x8 kd = lds_read16(smem, koff + 8 * 2048), k0 = lds_read16(smem, koff);
    kf[1] = lds_read16(smem, koff + 2048);
    const bf16x8 vd0 = lds_read16(smem, vb0 + 8 * K2_VS), vz0 = lds_read16(smem, vb0);
    const bf16x8 vd1 = lds_read16(smem, vb1 + 8 * K2_VS), vz1 = lds_read16(smem, vb1);
    const f32x16 sl = mfma32(kd, q, zero);
    const f32x16 sf = mfma32(k0, q, cinit(0));
    f32x16 sc[2];
    sc[1] = mfma32(kf[1], q, cinit(1));
    kf[0] = lds_read16(smem, koff + 2 * 2048);
    vf[1][0] = lds_read16(smem, vb0 + K2_VS);
    vf[1][1] = lds_read16(smem, vb1 + K2_VS);
    SB();
    f32x16 o = zero;
    {
        unsigned pf[8], pl[8];
        attn_split(attn_merge(sf, sl), 0.0f, pf, pl);
        o = mfma32(vd0, as_frag(pl), o);
        o = mfma32(vz0, as_frag(pf), o);
        o = mfma32(vd1, as_frag(pl + 4), o);
        o = mfma32(vz1, as_frag(pf + 4), o);
    }
    SB();
    STAMP(2);
#pragma unroll
    for (int t = 1; t < 8; ++t) {
        const int b = t & 1;
        if (t < 7) {
            sc[b ^ 1] = mfma32(kf[b ^ 1], q, cinit(t + 1));
            if (t < 6) kf[b] = lds_read16(smem, koff + (t + 2) * 2048);
            vf[b ^ 1][0] = lds_read16(smem, vb0 + (t + 1) * K2_VS);
            vf[b ^ 1][1] = lds_read16(smem, vb1 + (t + 1) * K2_VS);
        }
        SB();
        unsigned pk[8];
#pragma unroll
        for (int r = 0; r < 8; r += 2) pk[r >> 1] = pack2(__builtin_amdgcn_exp2f(sc[b][r]), __builtin_amdgcn_exp2f(sc[b][r + 1]));
        const float e8 = __builtin_amdgcn_exp2f(sc[b][8]), e9 = __builtin_amdgcn_exp2f(sc[b][9]);
        SB();
        o = mfma32(vf[b][0], as_frag(pk), o);
        SB();
        pk[4] = pack2(e8, e9);
#pragma unroll
        for (int r = 10; r < 16; r += 2) pk[r >> 1] = pack2(__builtin_amdgcn_exp2f(sc[b][r]), __builtin_amdgcn_exp2f(sc[b][r + 1]));
        SB();
        o = mfma32(vf[b][1], as_frag(pk + 4), o);
        SB();
        if (t == 5) {
            STAMP(3);
            mid();
            STAMP(4);
        }
    }
    return o;
}

template <bool FULL, class Mid>
DEVI void attn_task(char* smem, char* __restrict__ ws, bf16x8 q, int rb, int i, int e, int p, int lane, Mid mid DIAG_ARG) {
    const int a = lane & 31, h = lane >> 5;
    const int koff = K2_K + e * 1024 + i * 2048 + lane * 16;
    const bool isdata = a < 16;
    const int vb0 = K2_V + i * K2_VS + (isdata ? e * 1024 + h * 256 + a * 16 : 2048);
    const int vb1 = vb0 + (isdata ? 512 : 0);
    const int umin = FULL ? 0 : 8 - 4 * rb;
    f32x16 o = attn_fast<FULL>(smem, q, koff, vb0, vb1, i, umin, mid DIAG_PASS);
    float mag = 0.f;
#pragma unroll
    for (int r = 0; r < 8; ++r) mag += __builtin_fabsf(o[r]);
    const bool safe = o[8] >= 0x1p-60f && o[8] <= 0x1p60f && mag <= 0x1p120f;
    if (__builtin_amdgcn_ballot_w64(!safe) != 0) {
        o = attn_exact<FULL>(smem, q, koff, vb0, vb1, i, umin);
    }
    const float inv = __builtin_amdgcn_rcpf(o[8]);
    const u32x4 ov = {pack2(o[0] * inv, o[1] * inv), pack2(o[2] * inv, o[3] * inv), pack2(o[4] * inv, o[5] * inv), pack2(o[6] * inv, o[7] * inv)};
    st16(ws + WS_OF + (size_t)((rb * 4 + i) * 32 + 2 * p + e) * 1024 + lane * 16, ov);
}

constexpr int G64_RED = 0, G64_T = 49152;
DEVI void oproj_phase(const uint16_t* __restrict__ x, char* __restrict__ ws, char* smem, int tb, int fp, int w, int lane DIAG_ARG) {
    constexpr int NS = 8;
    const int tt = w & 1, ft = w >> 1;
    const char* wt = ws + WS_WOT + (size_t)fp * 65536 + (size_t)(w * NS) * 2048 + lane * 16;
    bf16x8 fa[NS][2], fb[NS][2];
#pragma unroll
    for (int s = 0; s < NS; ++s)
#pragma unroll
        for (int q = 0; q < 2; ++q) fa[s][q] = *(const bf16x8*)(wt + s * 2048 + (q ^ (w >> 1)) * 1024);
    u32x4 xr[2];
#pragma unroll
    for (int n = 0; n < 2; ++n) {
        const int r = (w * 2 + n) * 8 + (lane >> 3);
        xr[n] = *(const u32x4*)((const char*)x + (size_t)(tb * 64 + r) * 1024 + fp * 128 + (lane & 7) * 16);
    }
    SB();
    BAR();
    SB();
    STAMP(1);
    const __amdgpu_buffer_rsrc_t rs = __builtin_amdgcn_make_buffer_rsrc(ws + WS_OF, 0, 2 << 20, 0x00020000);
#pragma unroll
    for (int s = 0; s < NS; ++s) {
#pragma unroll
        for (int q = 0; q < 2; ++q) fb[s][q] = load_dev(rs, ((tb * 2 + (q ^ (w & 1))) * 32 + w * NS) * 1024 + lane * 16, s * 1024);
        SB();
    }
#pragma unroll
    for (int n = 0; n < 2; ++n) {
        const int r = (w * 2 + n) * 8 + (lane >> 3);
        *(u32x4*)(smem + G64_T + r * 128 + (((lane & 7) ^ ((r >> 1) & 7)) << 4)) = xr[n];
    }
    SB();
    f32x16 acc[4];
#pragma unroll
    for (int q = 0; q < 4; ++q) acc[q] = f32x16{0};
#pragma unroll
    for (int s = 0; s < NS; ++s) {
#pragma unroll
        for (int q = 0; q < 4; ++q) acc[q] = mfma32(fa[s][q >> 1], fb[s][q & 1], acc[q]);
        SB();
    }
    STAMP(2);
#pragma unroll
    for (int q = 1; q < 4; ++q) {
        char* dst = smem + G64_RED + ((w ^ q) * 3 + q - 1) * 4096 + lane * 16;
#pragma unroll
        for (int r = 0; r < 4; ++r)
            *(f32x4*)(dst + r * 1024) = f32x4{acc[q][4 * r], acc[q][4 * r + 1], acc[q][4 * r + 2], acc[q][4 * r + 3]};
    }
    WAIT_L0();
    BAR();
    f32x16 o = acc[0];
#pragma unroll
    for (int q = 0; q < 3; ++q) {
        const char* src = smem + G64_RED + (w * 3 + q) * 4096 + lane * 16;
#pragma unroll
        for (int r = 0; r < 4; ++r) {
            const f32x4 v = *(const f32x4*)(src + r * 1024);
#pragma unroll
            for (int k = 0; k < 4; ++k) o[4 * r + k] += v[k];
        }
    }
    STAMP(3);
    const int tok = lane & 31, h = lane >> 5;
    const int r = tt * 32 + tok;
    unsigned pk[8];
    float sum = 0.f, sq = 0.f;
#pragma unroll
    for (int qd = 0; qd < 4; ++qd) {
        const u32x2 xv = *(const u32x2*)(smem + G64_T + r * 128 + (((ft * 4 + qd) ^ ((r >> 1) & 7)) << 4) + h * 8);
        pk[2 * qd] = pack2(o[4 * qd] + bf_lo(xv[0]), o[4 * qd + 1] + bf_hi(xv[0]));
        pk[2 * qd + 1] = pack2(o[4 * qd + 2] + bf_lo(xv[1]), o[4 * qd + 3] + bf_hi(xv[1]));
#pragma unroll
        for (int k = 0; k < 2; ++k) {
            const float lo = bf_lo(pk[2 * qd + k]), hi = bf_hi(pk[2 * qd + k]);
            sum += lo + hi;
            sq += lo * lo + hi * hi;
        }
    }
    char* hdst = ws + WS_HF + (size_t)((tb * 2 + tt) * 32 + (fp * 2 + ft) * 2) * 1024 + lane * 16;
    st16(hdst, u32x4{pk[0], pk[1], pk[2], pk[3]});
    st16(hdst + 1024, u32x4{pk[4], pk[5], pk[6], pk[7]});
    sum = swap32_add(sum);
    sq = swap32_add(sq);
    if (h == 0) ((f32x2*)(ws + WS_ST2))[(fp * 2 + ft) * S_ + tb * 64 + r] = f32x2{sum, sq};
}

__global__ __launch_bounds__(512, 2) void k2_attn(const uint16_t* __restrict__ x, const uint16_t* __restrict__ wg, const uint16_t* __restrict__ wu,
                                                  const uint16_t* __restrict__ wd, char* __restrict__ ws) {
    extern __shared__ __attribute__((aligned(16))) char smem[];
    DIAG_DECL;
    const int tid = threadIdx.x, lane = tid & 63, w = __builtin_amdgcn_readfirstlane(tid >> 6);
    const int xcd = blockIdx.x & 7, idx = blockIdx.x >> 3;
    const int rb = 2 * xcd + (idx >> 4), p = idx & 15;
    const int i = w & 3, e = w >> 2;
#pragma unroll
    for (int n = 0; n < 6; ++n) {
        const int pc = 6 * w + n;
        const int isv = pc >= 24, u = (pc % 24) >> 1, half = pc & 1;
        int kt = 4 * rb - 8 + u;
        kt = kt < 0 ? 0 : kt;
        const char* src = ws + (isv ? WS_VF : WS_KF) + (size_t)(kt * 16 + p) * 2048 + half * 1024 + lane * 16;
        glds16(src, smem, (isv ? K2_V + u * K2_VS : K2_K + u * 2048) + half * 1024);
    }
    glds16(ws + WS_QF + (size_t)((rb * 4 + i) * 32 + 2 * p + e) * 1024 + lane * 16, smem, K2_Q + w * 1024);
    if (tid < 48) *(unsigned*)(smem + K2_V + (tid >> 2) * K2_VS + 2048 + (tid & 3) * 4) = 0x3F803F80u;
    const int bid = blockIdx.x;
    const int job = K2_JOB + w * 4096;
    if (bid < 112) {
        const int cg = bid % 28, s = 8 * (bid / 28) + w;
#pragma unroll
        for (int n = 0; n < 4; ++n) {
            const uint16_t* src = (n >> 1) ? wu : wg;
            const int row = 16 * s + 8 * (n & 1) + (lane >> 3);
            glds16((const char*)src + (size_t)row * (F_ * 2) + cg * 128 + (lane & 7) * 16, smem, job + n * 1024);
        }
    } else if (bid < 224) {
        const int s = bid - 112;
#pragma unroll
        for (int n = 0; n < 2; ++n) {
            const int row = 16 * s + 8 * n + (lane >> 3);
            glds16((const char*)wd + (size_t)row * (D_ * 2) + w * 128 + (lane & 7) * 16, smem, job + n * 1024);
        }
    }
    if (bid < 112) WAIT_V(4);
    else if (bid < 224) WAIT_V(2);
    else WAIT_V(0);
    WAIT_L0();
    BAR();
    STAMP(1);
    const bf16x8 q = lds_read16(smem, K2_Q + w * 1024 + lane * 16);
    auto retile = [&]() {
        WAIT_V(0);
        const int G = lane >> 4, nh = G & 1, hh = G >> 1, rq = (lane >> 2) & 3, pp = lane & 3;
        if (bid < 112) {
            const int cg = bid % 28, kq = bid / 28, s = 8 * kq + w;
            float* red = (float*)(smem + K2_RED);
#pragma unroll
            for (int t = 0; t < 4; ++t) {
                const int o = job + nh * 2048 + (4 * hh + rq) * 128 + t * 32 + pp * 8;
                const bf16x8 f = lds_read_tr(smem, o, o + 8 * 128);
                const int T = 4 * cg + t, j = T / 7, t7 = T % 7;
                const u32x4 u = __builtin_bit_cast(u32x4, f);
                st16(ws + WS_WGU + (size_t)((j * 32 + s) * 7 + t7) * 1024 + lane * 16, u);
                float sum = 0.f;
#pragma unroll
                for (int r = 0; r < 4; ++r) sum += bf_lo(u[r]) + bf_hi(u[r]);
                sum = swap32_add(sum);
                if (lane < 32) red[w * 128 + nh * 64 + t * 16 + (lane & 15)] = sum;
            }
        } else if (bid < 224) {
            char* dst = ws + WS_WDT + (size_t)(w * 112 + bid - 112) * 2048 + lane * 16;
#pragma unroll
            for (int t = 0; t < 2; ++t) {
                const int o = job + (4 * hh + rq) * 128 + t * 64 + nh * 32 + pp * 8;
                st16(dst + t * 1024, __builtin_bit_cast(u32x4, lds_read_tr(smem, o, o + 8 * 128)));
            }
        }
        SB();
    };
    if (rb >= 2) attn_task<true>(smem, ws, q, rb, i, e, p, lane, retile DIAG_PASS);
    else attn_task<false>(smem, ws, q, rb, i, e, p, lane, retile DIAG_PASS);
    STAMP(5);
    if (bid < 112) {
        const int cg = bid % 28, kq = bid / 28;
        const float* red = (const float*)(smem + K2_RED);
        WAIT_L0();
        BAR();
        if (tid < 128) {
            float sum = 0.f;
#pragma unroll
            for (int ww = 0; ww < 8; ++ww) sum += red[ww * 128 + tid];
            ((float*)(ws + WS_CSP))[(kq * 2 + (tid >> 6)) * F_ + cg * 64 + (tid & 63)] = sum;
        }
    }
    WAIT_V0();
    BAR();
    if (tid == 0) arrive(counter(ws, 0, rb));
    if (w >= 4) {
        if (w == 4) await(counter(ws, 0, rb), 16);
        BAR();
        BAR();
        return;
    }
    oproj_phase(x, ws, smem, 4 * xcd + (idx >> 3), idx & 7, w, lane DIAG_PASS);
    WAIT_V(0);
    DIAG_END(ws, 1);
}

constexpr int K4_A = 28672, K4_SLOT = 45056, K4_RING = 3, K4_CS = K4_SLOT * K4_RING, K4_WX = K4_CS + 1024;
constexpr int K4_LDS = K4_WX + 12 * 2048, K5_T = K4_WX;

#define K5_WIN 7
#define WD_SCHED {2, 2, 2, 2, 2, 2, 2, 0}
#define WD_R0 0
constexpr int wd_first(int c) {
    constexpr int n[8] = WD_SCHED;
    int t = 0;
    for (int i = 0; i < c; ++i) t += n[i];
    return t;
}
constexpr int wd_extra(int c) { return 2 * (wd_first(c + 1) - wd_first(c)) + (c == WD_R0 ? 6 : 0); }
constexpr int wd_pending(int c) {
    return c == 0 ? 22 : c == 1 ? 11 + wd_extra(0) : c == 2 ? 11 + wd_extra(0) + wd_extra(1) : (c < 7 ? 11 : 0) + wd_extra(c - 2) + wd_extra(c - 1);
}
DEVI int wd_lds(int w, int s) { return s < 3 ? K4_WX + (w * 3 + s) * 2048 : (w < 2 ? 2 * K4_SLOT : 0) + ((w & 1) * 11 + s - 3) * 2048; }
template <int HI>
DEVI void down_phase(char* __restrict__ ws, uint16_t* __restrict__ out, char* smem, int tb, int fp, int w, int lane,
                     const bf16x8 (&fr)[14][2] DIAG_ARG) {
    constexpr int NS = 14, WIN = HI ? K5_WIN : NS;
    const int w8 = w + 4 * HI;
    const __amdgpu_buffer_rsrc_t rs = __builtin_amdgcn_make_buffer_rsrc(ws + WS_MF, 0, 7 << 20, 0x00020000);
    bf16x8 fb[NS][2];
    int vb[2];
#pragma unroll
    for (int q = 0; q < 2; ++q) vb[q] = ((tb * 2 + (q ^ (w & 1))) * 112 + w8 * NS) * 1024 + lane * 16;
    auto load = [&](int s) {
#pragma unroll
        for (int q = 0; q < 2; ++q) fb[s][q] = load_dev(rs, vb[q], s * 1024);
    };
#pragma unroll
    for (int s = 0; s < WIN; ++s) {
        load(s);
        SB();
    }
    bf16x8 fl[2][2];
    auto rdw = [&](int s) {
#pragma unroll
        for (int q = 0; q < 2; ++q) fl[s & 1][q] = lds_read16(smem, wd_lds(w, s) + (q ^ (w >> 1)) * 1024 + lane * 16);
    };
    if (!HI) rdw(0);
    const int tt = w & 1, ft = w >> 1;
    f32x16 acc[4];
#pragma unroll
    for (int q = 0; q < 4; ++q) acc[q] = f32x16{0};
    u32x4 hv;
#pragma unroll
    for (int s = 0; s < NS; ++s) {
        if (!HI && s + 1 < NS) rdw(s + 1);
        SB();
#pragma unroll
        for (int q = 0; q < 4; ++q) acc[q] = mfma32(HI ? fr[s][q >> 1] : fl[s & 1][q >> 1], fb[s][q & 1], acc[q]);
        SB();
        if (WIN + 2 * s < NS) load(WIN + 2 * s);
        if (WIN + 2 * s + 1 < NS) load(WIN + 2 * s + 1);
        if (s == (HI ? (NS - WIN) / 2 + 1 : 0)) hv = *(const u32x4*)(ws + WS_HF + (size_t)((tb * 2 + tt) * 32 + (fp * 2 + ft) * 2 + HI) * 1024 + lane * 16);
        SB();
    }
    STAMP(4);
    auto piece = [&](int sw, int hi, int p) {
        const int own = sw < 2 ? 2 * K4_SLOT + (sw & 1) * 11 * 2048 : (sw & 1) * 11 * 2048;
        return !hi ? own + p * 2048 : p < 5 ? K4_SLOT + (sw * 5 + p) * 2048 : own + (p + 2) * 2048;
    };
    if (HI) BAR();
#pragma unroll
    for (int q = 0; q < 4; ++q)
#pragma unroll
        for (int hh = 0; hh < 2; ++hh) {
            if (q == 0 && hh == HI) continue;
            const int id = q * 2 + hh;
            char* dst = smem + piece(w, HI, id - (id > HI ? 1 : 0)) + lane * 16;
#pragma unroll
            for (int r = 0; r < 2; ++r)
                *(f32x4*)(dst + r * 1024) =
                    f32x4{acc[q][8 * hh + 4 * r], acc[q][8 * hh + 4 * r + 1], acc[q][8 * hh + 4 * r + 2], acc[q][8 * hh + 4 * r + 3]};
        }
    if (!HI) BAR();
    WAIT_L0();
    BAR();
    float o[8];
#pragma unroll
    for (int k = 0; k < 8; ++k) o[k] = acc[0][8 * HI + k];
#pragma unroll
    for (int q = 0; q < 4; ++q)
#pragma unroll
        for (int hi = 0; hi < 2; ++hi) {
            if (q == 0 && hi == HI) continue;
            const int id = q * 2 + HI;
            const char* src = smem + piece(w ^ q, hi, id - (id > hi ? 1 : 0)) + lane * 16;
#pragma unroll
            for (int r = 0; r < 2; ++r) {
                const f32x4 v = *(const f32x4*)(src + r * 1024);
#pragma unroll
                for (int k = 0; k < 4; ++k) o[4 * r + k] += v[k];
            }
        }
    STAMP(5);
    const int tok = lane & 31, h = lane >> 5;
    const int r = tt * 32 + tok;
#pragma unroll
    for (int qd = 0; qd < 2; ++qd) {
        const unsigned a = hv[2 * qd], b = hv[2 * qd + 1];
        const u32x2 v = {pack2(o[4 * qd] + bf_lo(a), o[4 * qd + 1] + bf_hi(a)), pack2(o[4 * qd + 2] + bf_lo(b), o[4 * qd + 3] + bf_hi(b))};
        *(u32x2*)(smem + K5_T + r * 128 + (((ft * 4 + 2 * HI + qd) ^ ((r >> 1) & 7)) << 4) + h * 8) = v;
    }
    WAIT_L0();
    BAR();
    const int rr = w8 * 8 + (lane >> 3), cp = lane & 7;
    const u32x4 v = *(const u32x4*)(smem + K5_T + rr * 128 + cp * 16);
    *(u32x4*)((char*)out + (size_t)(tb * 64 + rr) * 1024 + fp * 128 + ((cp ^ ((rr >> 1) & 7)) << 4)) = v;
    WAIT_V(0);
}

__global__ __launch_bounds__(512, 2) void k4_mlp(char* __restrict__ ws, uint16_t* __restrict__ out) {
    extern __shared__ __attribute__((aligned(16))) char smem[];
    DIAG_DECL;
    const int tid = threadIdx.x, lane = tid & 63, wv = __builtin_amdgcn_readfirstlane(tid >> 6), w = wv & 3;
    const int xcd = blockIdx.x & 7, idx = blockIdx.x >> 3;
    const int rb = K4_RB(xcd, idx), j = K4_J(xcd, idx);
    const int tile = rb * 4 + w;
    const int tb = 4 * xcd + (idx >> 3), fp = idx & 7;
    if (wv >= 4) {
        const char* asrc = ws + WS_WGU + (size_t)j * (32 * 7168) + w * 7168 + lane * 16;
        const char* bsrc = ws + WS_HF + (size_t)tile * 32768 + lane * 16;
        auto issue = [&](int c) {
            const int slot = (c % K4_RING) * K4_SLOT;
#pragma unroll
            for (int n = 0; n < 4; ++n) glds16(bsrc + (c * 4 + n) * 1024, smem, slot + K4_A + (w * 4 + n) * 1024);
#pragma unroll
            for (int n = 0; n < 7; ++n) glds16(asrc + c * 28672 + n * 1024, smem, slot + w * 7168 + n * 1024);
        };
        const char* wdt = ws + WS_WDT + (size_t)fp * (112 * 2048) + lane * 16;
        bf16x8 fr[14][2];
        auto wd_regs = [&](int s0, int s1) {
#pragma unroll
            for (int s = s0; s < s1; ++s)
#pragma unroll
                for (int q = 0; q < 2; ++q) fr[s][q] = *(const bf16x8*)(wdt + (size_t)((w + 4) * 14 + s) * 2048 + (q ^ (w >> 1)) * 1024);
        };
        auto wd_stage = [&](int s0, int s1) {
#pragma unroll
            for (int s = s0; s < s1; ++s)
#pragma unroll
                for (int q = 0; q < 2; ++q) glds16(wdt + (size_t)(w * 14 + s) * 2048 + q * 1024, smem, wd_lds(w, s) + q * 1024);
        };
#pragma unroll
        for (int c = 0; c < K4_RING; ++c) issue(c);
        static_for<0, 8>([&](auto cc) {
            constexpr int c = decltype(cc)::value;
            if constexpr (c < 7) wait_vm<wd_pending(c)>();
            else if (w < 2) wait_vm<wd_pending(7) + 22>();
            else wait_vm<wd_pending(7)>();
            BAR();
            if (c >= 1 && c - 1 + K4_RING < 8) issue(c - 1 + K4_RING);
            SB();
            wd_regs(wd_first(c), wd_first(c + 1));
            if (c == WD_R0) wd_stage(0, 3);
            SB();
            if ((c == 6 && w < 2) || (c == 7 && w >= 2)) wd_stage(3, 14);
        });
        WAIT_V0();
        STAMP(1);
        BAR();
        STAMP(2);
        if (w == 0) await(counter(ws, 1, tb >> 1), 16);
        STAMP(3);
        BAR();
        down_phase<1>(ws, out, smem, tb, fp, w, lane, fr DIAG_PASS);
        DIAG_END(ws, 2);
        return;
    }
    const int tok = lane & 31, h = lane >> 5;
    f32x2 st[8];
#pragma unroll
    for (int n = 0; n < 8; ++n) st[n] = ((const f32x2*)(ws + WS_ST2))[(h * 8 + n) * S_ + tile * 32 + tok];
    {
        const int t = tid < 224 ? tid : 0;
        const int gu = t >= 112, col = j * 112 + (gu ? t - 112 : t);
        float csq[4];
#pragma unroll
        for (int n = 0; n < 4; ++n) csq[n] = ((const float*)(ws + WS_CSP))[(n * 2 + gu) * F_ + col];
        if (tid < 224) ((float*)(smem + K4_CS))[tid] = (csq[0] + csq[1]) + (csq[2] + csq[3]);
    }
    float sum = 0.f, sq = 0.f;
#pragma unroll
    for (int n = 0; n < 8; ++n) {
        sum += st[n][0];
        sq += st[n][1];
    }
    sum = swap32_add(sum);
    sq = swap32_add(sq);
    const float mean = sum * (1.0f / D_);
    const float rstd = __builtin_amdgcn_rsqf(fmaxf(sq * (1.0f / D_) - mean * mean, 0.0f) + EPS);
    f32x16 acc[7];
#pragma unroll
    for (int t = 0; t < 7; ++t) acc[t] = f32x16{0};
    bf16x8 af[2][7], bfr[2];
    auto rdB = [&](int s) {
        bfr[s & 1] = lds_read16(smem, ((s >> 2) % K4_RING) * K4_SLOT + K4_A + (w * 4 + (s & 3)) * 1024 + lane * 16);
    };
    auto rdA = [&](int s, int t) {
        af[s & 1][t] = lds_read16(smem, ((s >> 2) % K4_RING) * K4_SLOT + ((s & 3) * 7 + t) * 1024 + lane * 16);
    };
    WAIT_L0();
    BAR();
    STAMP(1);
    rdB(0);
#pragma unroll
    for (int t = 0; t < 7; ++t) rdA(0, t);
    SB();
#pragma unroll
    for (int s = 0; s < 32; ++s) {
        const int b = s & 1;
        const bool edge = (s & 3) == 3;
#pragma unroll
        for (int t = 0; t < 7; ++t) {
            if (edge && s < 31 && t == 4) {
                WAIT_L0();
                BAR();
                SB();
            }
            acc[t] = mfma32(af[b][t], bfr[b], acc[t]);
            if (s < 31) {
                if (!edge) {
                    if (t == 0) rdB(s + 1);
                    rdA(s + 1, t);
                } else if (t == 4) {
                    rdB(s + 1);
                    rdA(s + 1, 0);
                    rdA(s + 1, 1);
                } else if (t == 5) {
                    rdA(s + 1, 2);
                    rdA(s + 1, 3);
                    rdA(s + 1, 4);
                } else if (t == 6) {
                    rdA(s + 1, 5);
                    rdA(s + 1, 6);
                }
            }
            SB();
        }
    }
    STAMP(2);
    const float* cs = (const float*)(smem + K4_CS);
    char* dst = ws + WS_MF + (size_t)(tile * 112 + j * 7) * 1024 + lane * 16;
#pragma unroll
    for (int t = 0; t < 7; ++t) {
        float v[8];
#pragma unroll
        for (int qd = 0; qd < 2; ++qd) {
            const f32x4 cg = *(const f32x4*)(cs + t * 16 + 8 * qd + 4 * h);
            const f32x4 cu = *(const f32x4*)(cs + 112 + t * 16 + 8 * qd + 4 * h);
#pragma unroll
            for (int r = 0; r < 4; ++r) {
                const float gt = (acc[t][4 * qd + r] - mean * cg[r]) * rstd;
                const float up = (acc[t][8 + 4 * qd + r] - mean * cu[r]) * rstd;
                v[4 * qd + r] = fmaxf(gt, 0.0f) * up;
            }
        }
        st16(dst + t * 1024, u32x4{pack2(v[0], v[1]), pack2(v[2], v[3]), pack2(v[4], v[5]), pack2(v[6], v[7])});
    }
    WAIT_V(0);
    BAR();
    if (tid == 0) arrive(counter(ws, 1, rb));
    BAR();
    STAMP(3);
    const bf16x8 none[14][2] = {};
    down_phase<0>(ws, out, smem, tb, fp, w, lane, none DIAG_PASS);
    DIAG_END(ws, 2);
}

extern "C" void kernel_launch(void* const* d_in, const int* in_sizes, int n_in, void* d_out, int out_size, void* d_ws,
                              size_t ws_size, hipStream_t stream) {
    const uint16_t* x = (const uint16_t*)d_in[0];
    const uint16_t* wqkv = (const uint16_t*)d_in[1];
    const uint16_t* wo = (const uint16_t*)d_in[2];
    const uint16_t* wg = (const uint16_t*)d_in[4];
    const uint16_t* wu = (const uint16_t*)d_in[5];
    const uint16_t* wd = (const uint16_t*)d_in[6];
    char* ws = (char*)d_ws;
    (void)hipFuncSetAttribute((const void*)k1_qkv, hipFuncAttributeMaxDynamicSharedMemorySize, K1_LDS);
    (void)hipFuncSetAttribute((const void*)k2_attn, hipFuncAttributeMaxDynamicSharedMemorySize, K2_LDS);
    (void)hipFuncSetAttribute((const void*)k4_mlp, hipFuncAttributeMaxDynamicSharedMemorySize, K4_LDS);
    k1_qkv<<<256, 512, K1_LDS, stream>>>(x, wqkv, wo, ws);
    k2_attn<<<256, 512, K2_LDS, stream>>>(x, wg, wu, wd, ws);
    k4_mlp<<<256, 512, K4_LDS, stream>>>(ws, (uint16_t*)d_out);
}
```

```cpp
#include <hip/hip_runtime.h>
#include <hip/hip_bf16.h>
#include <stdint.h>
#include <type_traits>

typedef __attribute__((ext_vector_type(8))) __bf16 bf16x8;
typedef __attribute__((ext_vector_type(2))) __bf16 bf16x2;
typedef __attribute__((ext_vector_type(4))) short s16x4;
typedef __attribute__((ext_vector_type(8))) short s16x8;
typedef __attribute__((ext_vector_type(2))) float f32x2;
typedef __attribute__((ext_vector_type(4))) float f32x4;
typedef __attribute__((ext_vector_type(16))) float f32x16;
typedef __attribute__((ext_vector_type(2))) unsigned u32x2;
typedef __attribute__((ext_vector_type(4))) unsigned u32x4;
typedef __attribute__((address_space(3))) char* lds_ptr;
typedef const __attribute__((address_space(1))) void* gptr_t;

#define DEVI __device__ __forceinline__
#define WAIT_V(n) asm volatile("s_waitcnt vmcnt(" #n ")" ::: "memory")
template <int N>
__device__ __forceinline__ void wait_vm() { asm volatile("s_waitcnt vmcnt(%0)" ::"n"(N) : "memory"); }
template <int I, int N, class F>
__device__ __forceinline__ void static_for(F f) {
    if constexpr (I < N) {
        f(std::integral_constant<int, I>{});
        static_for<I + 1, N>(f);
    }
}
#define WAIT_V0() __builtin_amdgcn_s_waitcnt(0x0070)
#define WAIT_L0() asm volatile("s_waitcnt lgkmcnt(0)" ::: "memory")
#define BAR() __builtin_amdgcn_s_barrier()
#define SB() __builtin_amdgcn_sched_barrier(0)

#define DIAG_DECL do { } while (0)
#define STAMP(ph) do { } while (0)
#define DIAG_END(ws, kid) do { } while (0)
#define DIAG_ARG
#define DIAG_PASS

constexpr int S_ = 2048, D_ = 512, F_ = 1792;
constexpr float EPS = 1e-5f;
constexpr float QSCALE = 0.25f * 1.44269504088896340736f;

constexpr size_t WS_QF = 0;
constexpr size_t WS_KF = WS_QF + (2u << 20);
constexpr size_t WS_VF = WS_KF + (2u << 20);
constexpr size_t WS_OF = WS_VF + (2u << 20);
constexpr size_t WS_HF = WS_OF + (2u << 20);
constexpr size_t WS_MF = WS_HF + (2u << 20);
constexpr size_t WS_WOT = WS_MF + (7u << 20);
constexpr size_t WS_WGU = WS_WOT + (512u << 10);
constexpr size_t WS_WDT = WS_WGU + (3584u << 10);
constexpr size_t WS_ST2 = WS_WDT + (1792u << 10);
constexpr size_t WS_CSP = WS_ST2 + (256u << 10);
constexpr size_t WS_DIAG = WS_CSP + (64u << 10);
constexpr size_t WS_CNT = WS_DIAG + (1u << 20);

DEVI void glds16(const void* g, char* smem_base, int lds_off) {
    __builtin_amdgcn_global_load_lds((gptr_t)g, (__attribute__((address_space(3))) void*)((lds_ptr)smem_base + lds_off), 16, 0, 0);
}
DEVI bf16x8 lds_read16(char* smem, int off) { return *(const bf16x8*)(smem + off); }
DEVI bf16x8 lds_read_tr(char* smem, int off0, int off1) {
    s16x4 a = __builtin_amdgcn_ds_read_tr16_b64_v4i16((__attribute__((address_space(3))) s16x4*)((lds_ptr)smem + off0));
    s16x4 b = __builtin_amdgcn_ds_read_tr16_b64_v4i16((__attribute__((address_space(3))) s16x4*)((lds_ptr)smem + off1));
    s16x8 c = __builtin_shufflevector(a, b, 0, 1, 2, 3, 4, 5, 6, 7);
    return __builtin_bit_cast(bf16x8, c);
}
#define K4_XA 2
#define K4_RB(xcd, idx) (((xcd) % K4_XA) * (16 / K4_XA) + ((idx) % (16 / K4_XA)))
#define K4_J(xcd, idx) (((xcd) / K4_XA) * (2 * K4_XA) + ((idx) / (16 / K4_XA)))
#define K1_XA 4
#define K1_RB(xcd, idx) (((xcd) % K1_XA) * (16 / K1_XA) + ((idx) % (16 / K1_XA)))
#define K1_CT(xcd, idx) (((xcd) / K1_XA) * (2 * K1_XA) + ((idx) / (16 / K1_XA)))
DEVI void st16(void* p, u32x4 v) {
#define ST_MODE "sc1"
    asm volatile("global_store_dwordx4 %0, %1, off " ST_MODE "\n\ts_nop 1" ::"v"(p), "v"(v) : "memory");
}
typedef __attribute__((address_space(1))) unsigned* gcnt_t;
DEVI gcnt_t counter(char* ws, int edge, int rb) { return (gcnt_t)(ws + WS_CNT + (size_t)(edge * 16 + rb) * 256); }
DEVI void arrive(gcnt_t c) { __hip_atomic_fetch_add(c, 1u, __ATOMIC_RELAXED, __HIP_MEMORY_SCOPE_AGENT); }
DEVI void await(gcnt_t c, unsigned n) {
    for (int spin = 0; spin < (1 << 22); ++spin) {
        if (__hip_atomic_load(c, __ATOMIC_RELAXED, __HIP_MEMORY_SCOPE_AGENT) >= n) break;
        __builtin_amdgcn_s_sleep(1);
    }
}
#define LD_AUX 16
DEVI bf16x8 load_dev(__amdgpu_buffer_rsrc_t rs, int voff, int soff) {
    return __builtin_bit_cast(bf16x8, __builtin_amdgcn_raw_buffer_load_b128(rs, voff, soff, LD_AUX));
}
DEVI unsigned pack2(float a, float b) { return __builtin_bit_cast(unsigned, __builtin_convertvector(f32x2{a, b}, bf16x2)); }
DEVI float bf_lo(unsigned u) { return __builtin_bit_cast(float, u << 16); }
DEVI float bf_hi(unsigned u) { return __builtin_bit_cast(float, u & 0xffff0000u); }
DEVI float swap32_add(float v) {
    unsigned a = __builtin_bit_cast(unsigned, v), b = a;
    asm volatile("" : "+v"(b));
    auto r = __builtin_amdgcn_permlane32_swap(a, b, false, false);
    const unsigned r0 = r[0], r1 = r[1];
    return __builtin_bit_cast(float, r0) + __builtin_bit_cast(float, r1);
}
DEVI void swap32(unsigned& a, unsigned& b) {
    auto r = __builtin_amdgcn_permlane32_swap(a, b, false, false);
    const unsigned r0 = r[0], r1 = r[1];
    a = r0;
    b = r1;
}
DEVI float swap32_max(float v) {
    unsigned a = __builtin_bit_cast(unsigned, v), b = a;
    asm volatile("" : "+v"(b));
    auto r = __builtin_amdgcn_permlane32_swap(a, b, false, false);
    const unsigned r0 = r[0], r1 = r[1];
    return fmaxf(__builtin_bit_cast(float, r0), __builtin_bit_cast(float, r1));
}
DEVI f32x4 mfma16(bf16x8 a, bf16x8 b, f32x4 c) { return __builtin_amdgcn_mfma_f32_16x16x32_bf16(a, b, c, 0, 0, 0); }
DEVI f32x16 mfma32(bf16x8 a, bf16x8 b, f32x16 c) { return __builtin_amdgcn_mfma_f32_32x32x16_bf16(a, b, c, 0, 0, 0); }
DEVI bf16x8 ones8() {
    u32x4 o = {0x3F803F80u, 0x3F803F80u, 0x3F803F80u, 0x3F803F80u};
    return __builtin_bit_cast(bf16x8, o);
}

constexpr int K1_SLOT = 28672, K1_XB = 16384, K1_RING = 5;
constexpr int K1_STAT = K1_SLOT * K1_RING;
constexpr int K1_CSP = K1_STAT + 1024;
constexpr int K1_CS = K1_CSP + 1536;
constexpr int K1_JOB = K1_CS + 384, K1_LDS = K1_JOB + 2048;

DEVI void qkv_loader(const uint16_t* __restrict__ x, const uint16_t* __restrict__ wqkv, const uint16_t* __restrict__ wo, char* __restrict__ ws,
                     char* smem, int rb, int ct, int w, int lane) {
    const char* xsrc[4];
#pragma unroll
    for (int i = 0; i < 4; ++i) {
        const int r = 32 * w + 8 * i + (lane >> 3);
        const int c = (lane & 7) ^ ((r >> 1) & 7);
        xsrc[i] = (const char*)x + (size_t)(rb * 128 + r) * 1024 + c * 16;
    }
    const char* wsrc[3];
#pragma unroll
    for (int i = 0; i < 3; ++i) {
        const int ci = 64 * (3 * w + i) + lane;
        const int r = ci / 12, cp = ci % 12;
        const int c = cp ^ (((r >> 3) & 1) << 1);
        wsrc[i] = (const char*)wqkv + (size_t)r * 3072 + ct * 192 + c * 16;
    }
    auto issue = [&](int c) {
        const int slot = (c % K1_RING) * K1_SLOT;
#pragma unroll
        for (int i = 0; i < 4; ++i) glds16(xsrc[i] + c * 128, smem, slot + (32 * w + 8 * i) * 128);
#pragma unroll
        for (int i = 0; i < 3; ++i) glds16(wsrc[i] + (size_t)c * 64 * 3072, smem, slot + K1_XB + (3 * w + i) * 1024);
    };
    const int m = lane & 15, g = lane >> 4, wq = (lane >> 2) & 3, wp = lane & 3;
    int woff[3];
#pragma unroll
    for (int nt = 0; nt < 3; ++nt)
        woff[nt] = K1_XB + ((w >> 1) * 32 + 8 * g + wq) * 192 + wp * 8 + (((3 * (w & 1) + nt) ^ (g & 1)) << 5);
    const bf16x8 ones = ones8();
    f32x4 cs[3];
#pragma unroll
    for (int nt = 0; nt < 3; ++nt) cs[nt] = f32x4{0, 0, 0, 0};
    const int jfp = blockIdx.x & 7, js = blockIdx.x >> 3;
    if (w == 0) {
#pragma unroll
        for (int n = 0; n < 2; ++n) glds16((const char*)wo + (size_t)(16 * js + 8 * n + (lane >> 3)) * (D_ * 2) + jfp * 128 + (lane & 7) * 16, smem, K1_JOB + n * 1024);
    }
#pragma unroll
    for (int c = 0; c < K1_RING; ++c) issue(c);
#pragma unroll
    for (int c = 0; c < 8; ++c) {
        if (c == 0) WAIT_V(28);
        else if (c <= 4) WAIT_V(21);
        else if (c == 5) WAIT_V(14);
        else if (c == 6) WAIT_V(7);
        else WAIT_V(0);
        WAIT_L0();
        BAR();
        const int slot = (c % K1_RING) * K1_SLOT;
        bf16x8 wf[3];
#pragma unroll
        for (int nt = 0; nt < 3; ++nt) wf[nt] = lds_read_tr(smem, slot + woff[nt], slot + woff[nt] + 4 * 192);
        SB();
        if (c >= 1 && c - 1 + K1_RING < 8) issue(c - 1 + K1_RING);
        SB();
#pragma unroll
        for (int nt = 0; nt < 3; ++nt) cs[nt] = mfma16(wf[nt], ones, cs[nt]);
        SB();
    }
    if (m == 0) {
#pragma unroll
        for (int nt = 0; nt < 3; ++nt) *(f32x4*)(smem + K1_CSP + ((w >> 1) * 96 + (3 * (w & 1) + nt) * 16 + 4 * g) * 4) = cs[nt];
    }
    WAIT_L0();
    BAR();
    if (w < 2) {
        const int t = w * 64 + lane;
        const float* csp = (const float*)(smem + K1_CSP);
        if (t < 96) ((float*)(smem + K1_CS))[t] = csp[t] + csp[96 + t];
    }
    WAIT_L0();
    BAR();
    if (w == 0) {
        const int G = lane >> 4, nh = G & 1, hh = G >> 1, rq = (lane >> 2) & 3, pp = lane & 3;
#pragma unroll
        for (int t = 0; t < 2; ++t) {
            const int o = K1_JOB + (4 * hh + rq) * 128 + t * 64 + nh * 32 + pp * 8;
            st16(ws + WS_WOT + (size_t)(jfp * 32 + js) * 2048 + t * 1024 + lane * 16, __builtin_bit_cast(u32x4, lds_read_tr(smem, o, o + 8 * 128)));
        }
    }
}

template <int NV>
DEVI void qkv_body(char* __restrict__ ws, char* smem, int rb, int ct, int w, int lane DIAG_ARG) {
    const int m = lane & 15, g = lane >> 4;
    const int wr = w & 1, wc = w >> 1;
    int xoff[4][2];
#pragma unroll
    for (int tt = 0; tt < 4; ++tt)
#pragma unroll
        for (int s = 0; s < 2; ++s) {
            const int r = 64 * wr + 16 * (tt ^ (2 * wc)) + m;
            xoff[tt][s] = r * 128 + (((4 * s + g) ^ ((r >> 1) & 7)) << 4);
        }
    const int wq = (lane >> 2) & 3, wp = lane & 3;
    int woff[3];
#pragma unroll
    for (int nt = 0; nt < 3; ++nt) woff[nt] = K1_XB + (8 * g + wq) * 192 + wp * 8 + (((3 * wc + nt) ^ (g & 1)) << 5);
    f32x4 acc[4][3], ssum[2], ssq[2];
#pragma unroll
    for (int tt = 0; tt < 4; ++tt)
#pragma unroll
        for (int nt = 0; nt < 3; ++nt) acc[tt][nt] = f32x4{0, 0, 0, 0};
#pragma unroll
    for (int tt = 0; tt < 2; ++tt) {
        ssum[tt] = f32x4{0, 0, 0, 0};
        ssq[tt] = f32x4{0, 0, 0, 0};
    }
    const bf16x8 ones = ones8();
    bf16x8 xf[2][4], wf[2][3];
    auto rd = [&](int ks, int n) {
        const int slot = ((ks >> 1) % K1_RING) * K1_SLOT;
        if (n == 0 || (n >= 2 && n <= 4)) {
            const int tt = n == 0 ? 0 : n - 1;
            xf[ks & 1][tt] = lds_read16(smem, slot + xoff[tt][ks & 1]);
        } else {
            const int nt = n == 1 ? 0 : n - 4;
            const int o = slot + woff[nt] + (ks & 1) * 32 * 192;
            wf[ks & 1][nt] = lds_read_tr(smem, o, o + 4 * 192);
        }
    };
    BAR();
    STAMP(1);
#pragma unroll
    for (int n = 0; n < 7; ++n) rd(0, n);
    SB();
#pragma unroll
    for (int ks = 0; ks < 16; ++ks) {
        const int b = ks & 1;
        const int first = b ? 6 : 0;
#pragma unroll
        for (int n = 0; n < 16; ++n) {
            if (b && ks < 15 && n == first) {
                WAIT_L0();
                BAR();
                SB();
            }
            if (n < 12) {
                const int nt = n >> 2, tt = n & 3;
                acc[tt][nt] = (nt >= 3 - NV) ? mfma16(xf[b][tt], wf[b][nt], acc[tt][nt]) : mfma16(wf[b][nt], xf[b][tt], acc[tt][nt]);
            } else if (n < 14) {
                ssum[n - 12] = mfma16(ones, xf[b][n - 12], ssum[n - 12]);
            } else {
                ssq[n - 14] = mfma16(xf[b][n - 14], xf[b][n - 14], ssq[n - 14]);
            }
            if (ks < 15 && n >= first && n < first + 7) rd(ks + 1, n - first);
            SB();
        }
    }
    STAMP(2);
    f32x2* stat = (f32x2*)(smem + K1_STAT);
    const float* csf = (const float*)(smem + K1_CS);
#pragma unroll
    for (int tt = 0; tt < 2; ++tt) {
        const int r = m & 3;
        const float sq = r == 0 ? ssq[tt][0] : r == 1 ? ssq[tt][1] : r == 2 ? ssq[tt][2] : ssq[tt][3];
        const float mean = ssum[tt][0] * (1.0f / D_);
        const float var = fmaxf(sq * (1.0f / D_) - mean * mean, 0.0f);
        if ((m >> 2) == g) stat[64 * wr + 32 * wc + 16 * tt + m] = f32x2{mean, __builtin_amdgcn_rsqf(var + EPS)};
    }
    WAIT_L0();
    BAR();
    BAR();
    STAMP(3);
#pragma unroll
    for (int nt = 0; nt < 3; ++nt) {
        const int ntl = 3 * wc + nt;
        const int col = ct * 96 + ntl * 16;
        const int head = (col & 511) >> 4;
        const bool isv = nt >= 3 - NV;
        const int isk = col >= 512;
        const float sc = (!isv && !isk) ? QSCALE : 1.0f;
        const f32x4 c4 = *(const f32x4*)(csf + ntl * 16 + 4 * g);
        const float c1 = csf[ntl * 16 + m];
        char* base = ws + (isv ? WS_VF : isk ? WS_KF : WS_QF) + head * 1024 +
                     (isv ? lane * 16 : ((lane & 15) + 16 * (lane >> 5) + 32 * ((lane >> 4) & 1)) * 16);
#pragma unroll
        for (int pr = 0; pr < 2; ++pr) {
            unsigned pk[2][2];
#pragma unroll
            for (int u = 0; u < 2; ++u) {
                const int tt = 2 * pr + u;
                const int tok0 = 64 * wr + 16 * (tt ^ (2 * wc));
                float v[4];
                if (!isv) {
                    const f32x2 st = stat[tok0 + m];
                    const float rs = st[1] * sc;
#pragma unroll
                    for (int r = 0; r < 4; ++r) v[r] = (acc[tt][nt][r] - st[0] * c4[r]) * rs;
                } else {
#pragma unroll
                    for (int r = 0; r < 4; ++r) {
                        const f32x2 st = stat[tok0 + 4 * g + r];
                        v[r] = (acc[tt][nt][r] - st[0] * c1) * st[1];
                    }
                }
                pk[u][0] = pack2(v[0], v[1]);
                pk[u][1] = pack2(v[2], v[3]);
            }
            swap32(pk[0][0], pk[1][0]);
            swap32(pk[0][1], pk[1][1]);
            const int tile = rb * 4 + 2 * wr + (pr ^ wc);
            st16(base + (size_t)tile * 32768, u32x4{pk[0][0], pk[0][1], pk[1][0], pk[1][1]});
        }
    }
}

__global__ __launch_bounds__(512, 2) void k1_qkv(const uint16_t* __restrict__ x, const uint16_t* __restrict__ wqkv, const uint16_t* __restrict__ wo,
                                                 char* __restrict__ ws) {
    extern __shared__ __attribute__((aligned(16))) char smem[];
    DIAG_DECL;
    const int lane = threadIdx.x & 63, wv = __builtin_amdgcn_readfirstlane(threadIdx.x >> 6), w = wv & 3;
    const int xcd = blockIdx.x & 7, idx = blockIdx.x >> 3;
    const int rb = K1_RB(xcd, idx), ct = K1_CT(xcd, idx);
    if (wv >= 4) {
        qkv_loader(x, wqkv, wo, ws, smem, rb, ct, w, lane);
        return;
    }
    if (blockIdx.x < 32 && threadIdx.x == 0) *(unsigned*)(ws + WS_CNT + (size_t)blockIdx.x * 256) = 0u;
    if (ct >= 11) qkv_body<3>(ws, smem, rb, ct, w, lane DIAG_PASS);
    else if (ct == 10 && w >= 2) qkv_body<2>(ws, smem, rb, ct, w, lane DIAG_PASS);
    else qkv_body<0>(ws, smem, rb, ct, w, lane DIAG_PASS);
    WAIT_V(0);
    DIAG_END(ws, 0);
}

constexpr int K2_VS = 2064;
constexpr int K2_K = 0, K2_V = 24576, K2_Q = K2_V + 12 * K2_VS + 192, K2_JOB = K2_Q + 8192, K2_RED = K2_JOB + 32768;
constexpr int K2_LDS = K2_RED + 8 * 128 * 4;

DEVI f32x16 splat16(float v) {
    f32x16 r;
#pragma unroll
    for (int i = 0; i < 16; ++i) r[i] = v;
    return r;
}
constexpr unsigned long long row_mask(int r) {
    unsigned long long m = 0;
    for (int l = 0; l < 64; ++l)
        if ((r & 3) + 8 * (r >> 2) + 4 * (l >> 5) > (l & 31)) m |= 1ull << l;
    return m;
}
template <int R>
DEVI unsigned sel_row(unsigned no, unsigned yes) { return __builtin_amdgcn_inverse_ballot_w64(row_mask(R)) ? yes : no; }
template <int R>
DEVI void merge_rows(const f32x16& sf, const f32x16& sl, f32x16& mg) {
    if constexpr (R < 16) {
        const float no = sl[R], yes = sf[R];
        mg[R] = __builtin_amdgcn_inverse_ballot_w64(row_mask(R)) ? yes : no;
        merge_rows<R + 1>(sf, sl, mg);
    }
}
DEVI f32x16 attn_merge(const f32x16& sf, const f32x16& sl) {
    f32x16 mg;
    merge_rows<0>(sf, sl, mg);
    return mg;
}
template <int R>
DEVI void split_rows(const f32x16& mg, float mx, unsigned (&pf)[8], unsigned (&pl)[8]) {
    if constexpr (R < 16) {
        const unsigned pk = pack2(__builtin_amdgcn_exp2f(mg[R] - mx), __builtin_amdgcn_exp2f(mg[R + 1] - mx));
        const unsigned mk = sel_row<R>(sel_row<R + 1>(0u, 0xffff0000u), 0xffffffffu);
        pf[R >> 1] = pk & mk;
        pl[R >> 1] = pk & ~mk;
        split_rows<R + 2>(mg, mx, pf, pl);
    }
}
DEVI void attn_split(const f32x16& mg, float mx, unsigned (&pf)[8], unsigned (&pl)[8]) { split_rows<0>(mg, mx, pf, pl); }
DEVI bf16x8 as_frag(const unsigned* p) { return __builtin_bit_cast(bf16x8, u32x4{p[0], p[1], p[2], p[3]}); }

template <bool FULL>
DEVI f32x16 attn_exact(char* smem, bf16x8 q, int koff, int vb0, int vb1, int i, int umin) {
    const float NEG = -__builtin_inff();
    const f32x16 zero = {0};
    const f32x16 sl = mfma32(lds_read16(smem, koff + 8 * 2048), q, zero);
    f32x16 sf = splat16(NEG);
    if (FULL || i >= umin) sf = mfma32(lds_read16(smem, koff), q, zero);
    const f32x16 mg = attn_merge(sf, sl);
    float mx = fmaxf(mg[0], mg[1]);
#pragma unroll
    for (int r = 2; r < 16; r += 2) mx = fmaxf(fmaxf(mx, mg[r]), mg[r + 1]);
#pragma unroll 1
    for (int t = 1; t < 8; ++t) {
        if (FULL || i + t >= umin) {
            const f32x16 s = mfma32(lds_read16(smem, koff + t * 2048), q, zero);
#pragma unroll
            for (int r = 0; r < 16; r += 2) mx = fmaxf(fmaxf(mx, s[r]), s[r + 1]);
        }
    }
    mx = swap32_max(mx);
    const f32x16 negm = splat16(-mx);
    f32x16 o = zero;
    unsigned pf[8], pl[8];
    attn_split(mg, mx, pf, pl);
#pragma unroll
    for (int s = 0; s < 2; ++s) {
        o = mfma32(lds_read16(smem, (s ? vb1 : vb0) + 8 * K2_VS), as_frag(pl + 4 * s), o);
        if (FULL || i >= umin) o = mfma32(lds_read16(smem, s ? vb1 : vb0), as_frag(pf + 4 * s), o);
    }
#pragma unroll 1
    for (int t = 1; t < 8; ++t) {
        if (FULL || i + t >= umin) {
            const f32x16 s = mfma32(lds_read16(smem, koff + t * 2048), q, negm);
            unsigned pk[8];
#pragma unroll
            for (int r = 0; r < 16; r += 2) pk[r >> 1] = pack2(__builtin_amdgcn_exp2f(s[r]), __builtin_amdgcn_exp2f(s[r + 1]));
            o = mfma32(lds_read16(smem, vb0 + t * K2_VS), as_frag(pk), o);
            o = mfma32(lds_read16(smem, vb1 + t * K2_VS), as_frag(pk + 4), o);
        }
    }
    return o;
}

template <bool FULL, class Mid>
DEVI f32x16 attn_fast(char* smem, bf16x8 q, int koff, int vb0, int vb1, int i, int umin, Mid mid DIAG_ARG) {
    const float NEG = -__builtin_inff();
    const f32x16 zero = {0};
    auto cinit = [&](int t) { return FULL ? zero : splat16(i + t >= umin ? 0.0f : NEG); };
    bf16x8 kf[2], vf[2][2];
    const bf16x8 kd = lds_read16(smem, koff + 8 * 2048), k0 = lds_read16(smem, koff);
    kf[1] = lds_read16(smem, koff + 2048);
    const bf16x8 vd0 = lds_read16(smem, vb0 + 8 * K2_VS), vz0 = lds_read16(smem, vb0);
    const bf16x8 vd1 = lds_read16(smem, vb1 + 8 * K2_VS), vz1 = lds_read16(smem, vb1);
    const f32x16 sl = mfma32(kd, q, zero);
    const f32x16 sf = mfma32(k0, q, cinit(0));
    f32x16 sc[2];
    sc[1] = mfma32(kf[1], q, cinit(1));
    kf[0] = lds_read16(smem, koff + 2 * 2048);
    vf[1][0] = lds_read16(smem, vb0 + K2_VS);
    vf[1][1] = lds_read16(smem, vb1 + K2_VS);
    SB();
    f32x16 o = zero;
    {
        unsigned pf[8], pl[8];
        attn_split(attn_merge(sf, sl), 0.0f, pf, pl);
        o = mfma32(vd0, as_frag(pl), o);
        o = mfma32(vz0, as_frag(pf), o);
        o = mfma32(vd1, as_frag(pl + 4), o);
        o = mfma32(vz1, as_frag(pf + 4), o);
    }
    SB();
    STAMP(2);
#pragma unroll
    for (int t = 1; t < 8; ++t) {
        const int b = t & 1;
        if (t < 7) {
            sc[b ^ 1] = mfma32(kf[b ^ 1], q, cinit(t + 1));
            if (t < 6) kf[b] = lds_read16(smem, koff + (t + 2) * 2048);
            vf[b ^ 1][0] = lds_read16(smem, vb0 + (t + 1) * K2_VS);
            vf[b ^ 1][1] = lds_read16(smem, vb1 + (t + 1) * K2_VS);
        }
        SB();
        unsigned pk[8];
#pragma unroll
        for (int r = 0; r < 8; r += 2) pk[r >> 1] = pack2(__builtin_amdgcn_exp2f(sc[b][r]), __builtin_amdgcn_exp2f(sc[b][r + 1]));
        const float e8 = __builtin_amdgcn_exp2f(sc[b][8]), e9 = __builtin_amdgcn_exp2f(sc[b][9]);
        SB();
        o = mfma32(vf[b][0], as_frag(pk), o);
        SB();
        pk[4] = pack2(e8, e9);
#pragma unroll
        for (int r = 10; r < 16; r += 2) pk[r >> 1] = pack2(__builtin_amdgcn_exp2f(sc[b][r]), __builtin_amdgcn_exp2f(sc[b][r + 1]));
        SB();
        o = mfma32(vf[b][1], as_frag(pk + 4), o);
        SB();
        if (t == 5) {
            STAMP(3);
            mid();
            STAMP(4);
        }
    }
    return o;
}

template <bool FULL, class Mid>
DEVI void attn_task(char* smem, char* __restrict__ ws, bf16x8 q, int rb, int i, int e, int p, int lane, Mid mid DIAG_ARG) {
    const int a = lane & 31, h = lane >> 5;
    const int koff = K2_K + e * 1024 + i * 2048 + lane * 16;
    const bool isdata = a < 16;
    const int vb0 = K2_V + i * K2_VS + (isdata ? e * 1024 + h * 256 + a * 16 : 2048);
    const int vb1 = vb0 + (isdata ? 512 : 0);
    const int umin = FULL ? 0 : 8 - 4 * rb;
    f32x16 o = attn_fast<FULL>(smem, q, koff, vb0, vb1, i, umin, mid DIAG_PASS);
    float mag = 0.f;
#pragma unroll
    for (int r = 0; r < 8; ++r) mag += __builtin_fabsf(o[r]);
    const bool safe = o[8] >= 0x1p-60f && o[8] <= 0x1p60f && mag <= 0x1p120f;
    if (__builtin_amdgcn_ballot_w64(!safe) != 0) {
        o = attn_exact<FULL>(smem, q, koff, vb0, vb1, i, umin);
    }
    const float inv = __builtin_amdgcn_rcpf(o[8]);
    const u32x4 ov = {pack2(o[0] * inv, o[1] * inv), pack2(o[2] * inv, o[3] * inv), pack2(o[4] * inv, o[5] * inv), pack2(o[6] * inv, o[7] * inv)};
    st16(ws + WS_OF + (size_t)((rb * 4 + i) * 32 + 2 * p + e) * 1024 + lane * 16, ov);
}

constexpr int G64_RED = 0, G64_T = 49152;
DEVI void oproj_phase(const uint16_t* __restrict__ x, char* __restrict__ ws, char* smem, int tb, int fp, int w, int lane DIAG_ARG) {
    constexpr int NS = 8;
    const int tt = w & 1, ft = w >> 1;
    const char* wt = ws + WS_WOT + (size_t)fp * 65536 + (size_t)(w * NS) * 2048 + lane * 16;
    bf16x8 fa[NS][2], fb[NS][2];
#pragma unroll
    for (int s = 0; s < NS; ++s)
#pragma unroll
        for (int q = 0; q < 2; ++q) fa[s][q] = *(const bf16x8*)(wt + s * 2048 + (q ^ (w >> 1)) * 1024);
    u32x4 xr[2];
#pragma unroll
    for (int n = 0; n < 2; ++n) {
        const int r = (w * 2 + n) * 8 + (lane >> 3);
        xr[n] = *(const u32x4*)((const char*)x + (size_t)(tb * 64 + r) * 1024 + fp * 128 + (lane & 7) * 16);
    }
    SB();
    BAR();
    SB();
    STAMP(1);
    const __amdgpu_buffer_rsrc_t rs = __builtin_amdgcn_make_buffer_rsrc(ws + WS_OF, 0, 2 << 20, 0x00020000);
#pragma unroll
    for (int s = 0; s < NS; ++s) {
#pragma unroll
        for (int q = 0; q < 2; ++q) fb[s][q] = load_dev(rs, ((tb * 2 + (q ^ (w & 1))) * 32 + w * NS) * 1024 + lane * 16, s * 1024);
        SB();
    }
#pragma unroll
    for (int n = 0; n < 2; ++n) {
        const int r = (w * 2 + n) * 8 + (lane >> 3);
        *(u32x4*)(smem + G64_T + r * 128 + (((lane & 7) ^ ((r >> 1) & 7)) << 4)) = xr[n];
    }
    SB();
    f32x16 acc[4];
#pragma unroll
    for (int q = 0; q < 4; ++q) acc[q] = f32x16{0};
#pragma unroll
    for (int s = 0; s < NS; ++s) {
#pragma unroll
        for (int q = 0; q < 4; ++q) acc[q] = mfma32(fa[s][q >> 1], fb[s][q & 1], acc[q]);
        SB();
    }
    STAMP(2);
#pragma unroll
    for (int q = 1; q < 4; ++q) {
        char* dst = smem + G64_RED + ((w ^ q) * 3 + q - 1) * 4096 + lane * 16;
#pragma unroll
        for (int r = 0; r < 4; ++r)
            *(f32x4*)(dst + r * 1024) = f32x4{acc[q][4 * r], acc[q][4 * r + 1], acc[q][4 * r + 2], acc[q][4 * r + 3]};
    }
    WAIT_L0();
    BAR();
    f32x16 o = acc[0];
#pragma unroll
    for (int q = 0; q < 3; ++q) {
        const char* src = smem + G64_RED + (w * 3 + q) * 4096 + lane * 16;
#pragma unroll
        for (int r = 0; r < 4; ++r) {
            const f32x4 v = *(const f32x4*)(src + r * 1024);
#pragma unroll
            for (int k = 0; k < 4; ++k) o[4 * r + k] += v[k];
        }
    }
    STAMP(3);
    const int tok = lane & 31, h = lane >> 5;
    const int r = tt * 32 + tok;
    unsigned pk[8];
    float sum = 0.f, sq = 0.f;
#pragma unroll
    for (int qd = 0; qd < 4; ++qd) {
        const u32x2 xv = *(const u32x2*)(smem + G64_T + r * 128 + (((ft * 4 + qd) ^ ((r >> 1) & 7)) << 4) + h * 8);
        pk[2 * qd] = pack2(o[4 * qd] + bf_lo(xv[0]), o[4 * qd + 1] + bf_hi(xv[0]));
        pk[2 * qd + 1] = pack2(o[4 * qd + 2] + bf_lo(xv[1]), o[4 * qd + 3] + bf_hi(xv[1]));
#pragma unroll
        for (int k = 0; k < 2; ++k) {
            const float lo = bf_lo(pk[2 * qd + k]), hi = bf_hi(pk[2 * qd + k]);
            sum += lo + hi;
            sq += lo * lo + hi * hi;
        }
    }
    char* hdst = ws + WS_HF + (size_t)((tb * 2 + tt) * 32 + (fp * 2 + ft) * 2) * 1024 + lane * 16;
    st16(hdst, u32x4{pk[0], pk[1], pk[2], pk[3]});
    st16(hdst + 1024, u32x4{pk[4], pk[5], pk[6], pk[7]});
    sum = swap32_add(sum);
    sq = swap32_add(sq);
    if (h == 0) ((f32x2*)(ws + WS_ST2))[(fp * 2 + ft) * S_ + tb * 64 + r] = f32x2{sum, sq};
}

__global__ __launch_bounds__(512, 2) void k2_attn(const uint16_t* __restrict__ x, const uint16_t* __restrict__ wg, const uint16_t* __restrict__ wu,
                                                  const uint16_t* __restrict__ wd, char* __restrict__ ws) {
    extern __shared__ __attribute__((aligned(16))) char smem[];
    DIAG_DECL;
    const int tid = threadIdx.x, lane = tid & 63, w = __builtin_amdgcn_readfirstlane(tid >> 6);
    const int xcd = blockIdx.x & 7, idx = blockIdx.x >> 3;
    const int rb = 2 * xcd + (idx >> 4), p = idx & 15;
    const int i = w & 3, e = w >> 2;
#pragma unroll
    for (int n = 0; n < 6; ++n) {
        const int pc = 6 * w + n;
        const int isv = pc >= 24, u = (pc % 24) >> 1, half = pc & 1;
        int kt = 4 * rb - 8 + u;
        kt = kt < 0 ? 0 : kt;
        const char* src = ws + (isv ? WS_VF : WS_KF) + (size_t)(kt * 16 + p) * 2048 + half * 1024 + lane * 16;
        glds16(src, smem, (isv ? K2_V + u * K2_VS : K2_K + u * 2048) + half * 1024);
    }
    glds16(ws + WS_QF + (size_t)((rb * 4 + i) * 32 + 2 * p + e) * 1024 + lane * 16, smem, K2_Q + w * 1024);
    if (tid < 48) *(unsigned*)(smem + K2_V + (tid >> 2) * K2_VS + 2048 + (tid & 3) * 4) = 0x3F803F80u;
    const int bid = blockIdx.x;
    const int job = K2_JOB + w * 4096;
    if (bid < 112) {
        const int cg = bid % 28, s = 8 * (bid / 28) + w;
#pragma unroll
        for (int n = 0; n < 4; ++n) {
            const uint16_t* src = (n >> 1) ? wu : wg;
            const int row = 16 * s + 8 * (n & 1) + (lane >> 3);
            glds16((const char*)src + (size_t)row * (F_ * 2) + cg * 128 + (lane & 7) * 16, smem, job + n * 1024);
        }
    } else if (bid < 224) {
        const int s = bid - 112;
#pragma unroll
        for (int n = 0; n < 2; ++n) {
            const int row = 16 * s + 8 * n + (lane >> 3);
            glds16((const char*)wd + (size_t)row * (D_ * 2) + w * 128 + (lane & 7) * 16, smem, job + n * 1024);
        }
    }
    if (bid < 112) WAIT_V(4);
    else if (bid < 224) WAIT_V(2);
    else WAIT_V(0);
    WAIT_L0();
    BAR();
    STAMP(1);
    const bf16x8 q = lds_read16(smem, K2_Q + w * 1024 + lane * 16);
    auto retile = [&]() {
        WAIT_V(0);
        const int G = lane >> 4, nh = G & 1, hh = G >> 1, rq = (lane >> 2) & 3, pp = lane & 3;
        if (bid < 112) {
            const int cg = bid % 28, kq = bid / 28, s = 8 * kq + w;
            float* red = (float*)(smem + K2_RED);
#pragma unroll
            for (int t = 0; t < 4; ++t) {
                const int o = job + nh * 2048 + (4 * hh + rq) * 128 + t * 32 + pp * 8;
                const bf16x8 f = lds_read_tr(smem, o, o + 8 * 128);
                const int T = 4 * cg + t, j = T / 7, t7 = T % 7;
                const u32x4 u = __builtin_bit_cast(u32x4, f);
                st16(ws + WS_WGU + (size_t)((j * 32 + s) * 7 + t7) * 1024 + lane * 16, u);
                float sum = 0.f;
#pragma unroll
                for (int r = 0; r < 4; ++r) sum += bf_lo(u[r]) + bf_hi(u[r]);
                sum = swap32_add(sum);
                if (lane < 32) red[w * 128 + nh * 64 + t * 16 + (lane & 15)] = sum;
            }
        } else if (bid < 224) {
            char* dst = ws + WS_WDT + (size_t)(w * 112 + bid - 112) * 2048 + lane * 16;
#pragma unroll
            for (int t = 0; t < 2; ++t) {
                const int o = job + (4 * hh + rq) * 128 + t * 64 + nh * 32 + pp * 8;
                st16(dst + t * 1024, __builtin_bit_cast(u32x4, lds_read_tr(smem, o, o + 8 * 128)));
            }
        }
        SB();
    };
    if (rb >= 2) attn_task<true>(smem, ws, q, rb, i, e, p, lane, retile DIAG_PASS);
    else attn_task<false>(smem, ws, q, rb, i, e, p, lane, retile DIAG_PASS);
    STAMP(5);
    if (bid < 112) {
        const int cg = bid % 28, kq = bid / 28;
        const float* red = (const float*)(smem + K2_RED);
        WAIT_L0();
        BAR();
        if (tid < 128) {
            float sum = 0.f;
#pragma unroll
            for (int ww = 0; ww < 8; ++ww) sum += red[ww * 128 + tid];
            ((float*)(ws + WS_CSP))[(kq * 2 + (tid >> 6)) * F_ + cg * 64 + (tid & 63)] = sum;
        }
    }
    WAIT_V0();
    BAR();
    if (tid == 0) arrive(counter(ws, 0, rb));
    if (w >= 4) {
        if (w == 4) await(counter(ws, 0, rb), 16);
        BAR();
        BAR();
        return;
    }
    oproj_phase(x, ws, smem, 4 * xcd + (idx >> 3), idx & 7, w, lane DIAG_PASS);
    WAIT_V(0);
    DIAG_END(ws, 1);
}

constexpr int K4_A = 28672, K4_SLOT = 45056, K4_RING = 3, K4_CS = K4_SLOT * K4_RING, K4_WX = K4_CS + 1024;
constexpr int K4_LDS = K4_WX + 12 * 2048, K5_T = K4_WX;

#define K5_WIN 7
#define WD_SCHED {1, 1, 1, 1, 1, 1, 1, 0}
#define WD_R0 5
constexpr int wd_first(int c) {
    constexpr int n[8] = WD_SCHED;
    int t = 0;
    for (int i = 0; i < c; ++i) t += n[i];
    return t;
}
constexpr int wd_extra(int c) { return 2 * (wd_first(c + 1) - wd_first(c)) + (c == WD_R0 ? 6 : 0); }
constexpr int wd_pending(int c) {
    return c == 0 ? 22 : c == 1 ? 11 + wd_extra(0) : c == 2 ? 11 + wd_extra(0) + wd_extra(1) : (c < 7 ? 11 : 0) + wd_extra(c - 2) + wd_extra(c - 1);
}
DEVI int wd_lds(int w, int s) { return s < 3 ? K4_WX + (w * 3 + s) * 2048 : (w < 2 ? 2 * K4_SLOT : 0) + ((w & 1) * 11 + s - 3) * 2048; }
template <int HI>
DEVI void down_phase(char* __restrict__ ws, uint16_t* __restrict__ out, char* smem, int tb, int fp, int w, int lane,
                     const bf16x8 (&fr)[14][2] DIAG_ARG) {
    constexpr int NS = 14, WIN = HI ? K5_WIN : NS;
    const int w8 = w + 4 * HI;
    const __amdgpu_buffer_rsrc_t rs = __builtin_amdgcn_make_buffer_rsrc(ws + WS_MF, 0, 7 << 20, 0x00020000);
    bf16x8 fb[NS][2];
    int vb[2];
#pragma unroll
    for (int q = 0; q < 2; ++q) vb[q] = ((tb * 2 + (q ^ (w & 1))) * 112 + w8 * NS) * 1024 + lane * 16;
    auto load = [&](int s) {
#pragma unroll
        for (int q = 0; q < 2; ++q) fb[s][q] = load_dev(rs, vb[q], s * 1024);
    };
#pragma unroll
    for (int s = 0; s < WIN; ++s) {
        load(s);
        SB();
    }
    bf16x8 fl[2][2];
    auto rdw = [&](int s) {
#pragma unroll
        for (int q = 0; q < 2; ++q) fl[s & 1][q] = lds_read16(smem, wd_lds(w, s) + (q ^ (w >> 1)) * 1024 + lane * 16);
    };
    if (!HI) rdw(0);
    const int tt = w & 1, ft = w >> 1;
    f32x16 acc[4];
#pragma unroll
    for (int q = 0; q < 4; ++q) acc[q] = f32x16{0};
    u32x4 hv;
#pragma unroll
    for (int s = 0; s < NS; ++s) {
        if (!HI && s + 1 < NS) rdw(s + 1);
        SB();
#pragma unroll
        for (int q = 0; q < 4; ++q) acc[q] = mfma32(HI ? fr[s][q >> 1] : fl[s & 1][q >> 1], fb[s][q & 1], acc[q]);
        SB();
        if (WIN + 2 * s < NS) load(WIN + 2 * s);
        if (WIN + 2 * s + 1 < NS) load(WIN + 2 * s + 1);
        if (s == (HI ? (NS - WIN) / 2 + 1 : 0)) hv = *(const u32x4*)(ws + WS_HF + (size_t)((tb * 2 + tt) * 32 + (fp * 2 + ft) * 2 + HI) * 1024 + lane * 16);
        SB();
    }
    STAMP(4);
    auto piece = [&](int sw, int hi, int p) {
        const int own = sw < 2 ? 2 * K4_SLOT + (sw & 1) * 11 * 2048 : (sw & 1) * 11 * 2048;
        return !hi ? own + p * 2048 : p < 5 ? K4_SLOT + (sw * 5 + p) * 2048 : own + (p + 2) * 2048;
    };
    if (HI) BAR();
#pragma unroll
    for (int q = 0; q < 4; ++q)
#pragma unroll
        for (int hh = 0; hh < 2; ++hh) {
            if (q == 0 && hh == HI) continue;
            const int id = q * 2 + hh;
            char* dst = smem + piece(w, HI, id - (id > HI ? 1 : 0)) + lane * 16;
#pragma unroll
            for (int r = 0; r < 2; ++r)
                *(f32x4*)(dst + r * 1024) =
                    f32x4{acc[q][8 * hh + 4 * r], acc[q][8 * hh + 4 * r + 1], acc[q][8 * hh + 4 * r + 2], acc[q][8 * hh + 4 * r + 3]};
        }
    if (!HI) BAR();
    WAIT_L0();
    BAR();
    float o[8];
#pragma unroll
    for (int k = 0; k < 8; ++k) o[k] = acc[0][8 * HI + k];
#pragma unroll
    for (int q = 0; q < 4; ++q)
#pragma unroll
        for (int hi = 0; hi < 2; ++hi) {
            if (q == 0 && hi == HI) continue;
            const int id = q * 2 + HI;
            const char* src = smem + piece(w ^ q, hi, id - (id > hi ? 1 : 0)) + lane * 16;
#pragma unroll
            for (int r = 0; r < 2; ++r) {
                const f32x4 v = *(const f32x4*)(src + r * 1024);
#pragma unroll
                for (int k = 0; k < 4; ++k) o[4 * r + k] += v[k];
            }
        }
    STAMP(5);
    const int tok = lane & 31, h = lane >> 5;
    const int r = tt * 32 + tok;
#pragma unroll
    for (int qd = 0; qd < 2; ++qd) {
        const unsigned a = hv[2 * qd], b = hv[2 * qd + 1];
        const u32x2 v = {pack2(o[4 * qd] + bf_lo(a), o[4 * qd + 1] + bf_hi(a)), pack2(o[4 * qd + 2] + bf_lo(b), o[4 * qd + 3] + bf_hi(b))};
        *(u32x2*)(smem + K5_T + r * 128 + (((ft * 4 + 2 * HI + qd) ^ ((r >> 1) & 7)) << 4) + h * 8) = v;
    }
    WAIT_L0();
    BAR();
    const int rr = w8 * 8 + (lane >> 3), cp = lane & 7;
    const u32x4 v = *(const u32x4*)(smem + K5_T + rr * 128 + cp * 16);
    *(u32x4*)((char*)out + (size_t)(tb * 64 + rr) * 1024 + fp * 128 + ((cp ^ ((rr >> 1) & 7)) << 4)) = v;
    WAIT_V(0);
}

__global__ __launch_bounds__(512, 2) void k4_mlp(char* __restrict__ ws, uint16_t* __restrict__ out) {
    extern __shared__ __attribute__((aligned(16))) char smem[];
    DIAG_DECL;
    const int tid = threadIdx.x, lane = tid & 63, wv = __builtin_amdgcn_readfirstlane(tid >> 6), w = wv & 3;
    const int xcd = blockIdx.x & 7, idx = blockIdx.x >> 3;
    const int rb = K4_RB(xcd, idx), j = K4_J(xcd, idx);
    const int tile = rb * 4 + w;
    const int tb = 4 * xcd + (idx >> 3), fp = idx & 7;
    if (wv >= 4) {
        const char* asrc = ws + WS_WGU + (size_t)j * (32 * 7168) + w * 7168 + lane * 16;
        const char* bsrc = ws + WS_HF + (size_t)tile * 32768 + lane * 16;
        auto issue = [&](int c) {
            const int slot = (c % K4_RING) * K4_SLOT;
#pragma unroll
            for (int n = 0; n < 4; ++n) glds16(bsrc + (c * 4 + n) * 1024, smem, slot + K4_A + (w * 4 + n) * 1024);
#pragma unroll
            for (int n = 0; n < 7; ++n) glds16(asrc + c * 28672 + n * 1024, smem, slot + w * 7168 + n * 1024);
        };
        const char* wdt = ws + WS_WDT + (size_t)fp * (112 * 2048) + lane * 16;
        bf16x8 fr[14][2];
        auto wd_regs = [&](int s0, int s1) {
#pragma unroll
            for (int s = s0; s < s1; ++s)
#pragma unroll
                for (int q = 0; q < 2; ++q) fr[s][q] = *(const bf16x8*)(wdt + (size_t)((w + 4) * 14 + s) * 2048 + (q ^ (w >> 1)) * 1024);
        };
        auto wd_stage = [&](int s0, int s1) {
#pragma unroll
            for (int s = s0; s < s1; ++s)
#pragma unroll
                for (int q = 0; q < 2; ++q) glds16(wdt + (size_t)(w * 14 + s) * 2048 + q * 1024, smem, wd_lds(w, s) + q * 1024);
        };
#pragma unroll
        for (int c = 0; c < K4_RING; ++c) issue(c);
        static_for<0, 8>([&](auto cc) {
            constexpr int c = decltype(cc)::value;
            if constexpr (c < 7) wait_vm<wd_pending(c)>();
            else if (w < 2) wait_vm<wd_pending(7) + 22>();
            else wait_vm<wd_pending(7)>();
            BAR();
            if (c >= 1 && c - 1 + K4_RING < 8) issue(c - 1 + K4_RING);
            SB();
            wd_regs(wd_first(c), wd_first(c + 1));
            if (c == WD_R0) wd_stage(0, 3);
            SB();
            if ((c == 6 && w < 2) || (c == 7 && w >= 2)) wd_stage(3, 14);
        });
        BAR();
        wd_regs(wd_first(8), 14);
        wait_vm<2 * (14 - wd_first(8))>();
        STAMP(1);
        BAR();
        STAMP(2);
        if (w == 0) await(counter(ws, 1, tb >> 1), 16);
        WAIT_V0();
        STAMP(3);
        BAR();
        down_phase<1>(ws, out, smem, tb, fp, w, lane, fr DIAG_PASS);
        DIAG_END(ws, 2);
        return;
    }
    const int tok = lane & 31, h = lane >> 5;
    f32x2 st[8];
#pragma unroll
    for (int n = 0; n < 8; ++n) st[n] = ((const f32x2*)(ws + WS_ST2))[(h * 8 + n) * S_ + tile * 32 + tok];
    {
        const int t = tid < 224 ? tid : 0;
        const int gu = t >= 112, col = j * 112 + (gu ? t - 112 : t);
        float csq[4];
#pragma unroll
        for (int n = 0; n < 4; ++n) csq[n] = ((const float*)(ws + WS_CSP))[(n * 2 + gu) * F_ + col];
        if (tid < 224) ((float*)(smem + K4_CS))[tid] = (csq[0] + csq[1]) + (csq[2] + csq[3]);
    }
    float sum = 0.f, sq = 0.f;
#pragma unroll
    for (int n = 0; n < 8; ++n) {
        sum += st[n][0];
        sq += st[n][1];
    }
    sum = swap32_add(sum);
    sq = swap32_add(sq);
    const float mean = sum * (1.0f / D_);
    const float rstd = __builtin_amdgcn_rsqf(fmaxf(sq * (1.0f / D_) - mean * mean, 0.0f) + EPS);
    f32x16 acc[7];
#pragma unroll
    for (int t = 0; t < 7; ++t) acc[t] = f32x16{0};
    bf16x8 af[2][7], bfr[2];
    auto rdB = [&](int s) {
        bfr[s & 1] = lds_read16(smem, ((s >> 2) % K4_RING) * K4_SLOT + K4_A + (w * 4 + (s & 3)) * 1024 + lane * 16);
    };
    auto rdA = [&](int s, int t) {
        af[s & 1][t] = lds_read16(smem, ((s >> 2) % K4_RING) * K4_SLOT + ((s & 3) * 7 + t) * 1024 + lane * 16);
    };
    WAIT_L0();
    BAR();
    STAMP(1);
    rdB(0);
#pragma unroll
    for (int t = 0; t < 7; ++t) rdA(0, t);
    SB();
#pragma unroll
    for (int s = 0; s < 32; ++s) {
        const int b = s & 1;
        const bool edge = (s & 3) == 3;
#pragma unroll
        for (int t = 0; t < 7; ++t) {
            if (edge && s < 31 && t == 4) {
                WAIT_L0();
                BAR();
                SB();
            }
            acc[t] = mfma32(af[b][t], bfr[b], acc[t]);
            if (s < 31) {
                if (!edge) {
                    if (t == 0) rdB(s + 1);
                    rdA(s + 1, t);
                } else if (t == 4) {
                    rdB(s + 1);
                    rdA(s + 1, 0);
                    rdA(s + 1, 1);
                } else if (t == 5) {
                    rdA(s + 1, 2);
                    rdA(s + 1, 3);
                    rdA(s + 1, 4);
                } else if (t == 6) {
                    rdA(s + 1, 5);
                    rdA(s + 1, 6);
                }
            }
            SB();
        }
    }
    STAMP(2);
    const float* cs = (const float*)(smem + K4_CS);
    char* dst = ws + WS_MF + (size_t)(tile * 112 + j * 7) * 1024 + lane * 16;
#pragma unroll
    for (int t = 0; t < 7; ++t) {
        float v[8];
#pragma unroll
        for (int qd = 0; qd < 2; ++qd) {
            const f32x4 cg = *(const f32x4*)(cs + t * 16 + 8 * qd + 4 * h);
            const f32x4 cu = *(const f32x4*)(cs + 112 + t * 16 + 8 * qd + 4 * h);
#pragma unroll
            for (int r = 0; r < 4; ++r) {
                const float gt = (acc[t][4 * qd + r] - mean * cg[r]) * rstd;
                const float up = (acc[t][8 + 4 * qd + r] - mean * cu[r]) * rstd;
                v[4 * qd + r] = fmaxf(gt, 0.0f) * up;
            }
        }
        st16(dst + t * 1024, u32x4{pack2(v[0], v[1]), pack2(v[2], v[3]), pack2(v[4], v[5]), pack2(v[6], v[7])});
    }
    BAR();
    WAIT_V(0);
    STAMP(1);
    BAR();
    if (tid == 0) arrive(counter(ws, 1, rb));
    BAR();
    STAMP(3);
    const bf16x8 none[14][2] = {};
    down_phase<0>(ws, out, smem, tb, fp, w, lane, none DIAG_PASS);
    DIAG_END(ws, 2);
}

extern "C" void kernel_launch(void* const* d_in, const int* in_sizes, int n_in, void* d_out, int out_size, void* d_ws,
                              size_t ws_size, hipStream_t stream) {
    const uint16_t* x = (const uint16_t*)d_in[0];
    const uint16_t* wqkv = (const uint16_t*)d_in[1];
    const uint16_t* wo = (const uint16_t*)d_in[2];
    const uint16_t* wg = (const uint16_t*)d_in[4];
    const uint16_t* wu = (const uint16_t*)d_in[5];
    const uint16_t* wd = (const uint16_t*)d_in[6];
    char* ws = (char*)d_ws;
    (void)hipFuncSetAttribute((const void*)k1_qkv, hipFuncAttributeMaxDynamicSharedMemorySize, K1_LDS);
    (void)hipFuncSetAttribute((const void*)k2_attn, hipFuncAttributeMaxDynamicSharedMemorySize, K2_LDS + 256);
    (void)hipFuncSetAttribute((const void*)k4_mlp, hipFuncAttributeMaxDynamicSharedMemorySize, K4_LDS);
    k1_qkv<<<256, 512, K1_LDS, stream>>>(x, wqkv, wo, ws);
    k2_attn<<<256, 512, K2_LDS + 256, stream>>>(x, wg, wu, wd, ws);
    k4_mlp<<<256, 512, K4_LDS, stream>>>(ws, (uint16_t*)d_out);
}
```

```cpp
#include <hip/hip_runtime.h>
#include <hip/hip_bf16.h>
#include <stdint.h>
#include <type_traits>

typedef __attribute__((ext_vector_type(8))) __bf16 bf16x8;
typedef __attribute__((ext_vector_type(2))) __bf16 bf16x2;
typedef __attribute__((ext_vector_type(4))) short s16x4;
typedef __attribute__((ext_vector_type(8))) short s16x8;
typedef __attribute__((ext_vector_type(2))) float f32x2;
typedef __attribute__((ext_vector_type(4))) float f32x4;
typedef __attribute__((ext_vector_type(16))) float f32x16;
typedef __attribute__((ext_vector_type(2))) unsigned u32x2;
typedef __attribute__((ext_vector_type(4))) unsigned u32x4;
typedef __attribute__((address_space(3))) char* lds_ptr;
typedef const __attribute__((address_space(1))) void* gptr_t;

#define DEVI __device__ __forceinline__
#define WAIT_V(n) asm volatile("s_waitcnt vmcnt(" #n ")" ::: "memory")
template <int N>
__device__ __forceinline__ void wait_vm() { asm volatile("s_waitcnt vmcnt(%0)" ::"n"(N) : "memory"); }
template <int I, int N, class F>
__device__ __forceinline__ void static_for(F f) {
    if constexpr (I < N) {
        f(std::integral_constant<int, I>{});
        static_for<I + 1, N>(f);
    }
}
#define WAIT_V0() __builtin_amdgcn_s_waitcnt(0x0070)
#define WAIT_L0() asm volatile("s_waitcnt lgkmcnt(0)" ::: "memory")
#define BAR() __builtin_amdgcn_s_barrier()
#define SB() __builtin_amdgcn_sched_barrier(0)

constexpr int S_ = 2048, D_ = 512, F_ = 1792;
constexpr float EPS = 1e-5f;
constexpr float QSCALE = 0.25f * 1.44269504088896340736f;

constexpr size_t WS_QF = 0;
constexpr size_t WS_KF = WS_QF + (2u << 20);
constexpr size_t WS_VF = WS_KF + (2u << 20);
constexpr size_t WS_OF = WS_VF + (2u << 20);
constexpr size_t WS_HF = WS_OF + (2u << 20);
constexpr size_t WS_MF = WS_HF + (2u << 20);
constexpr size_t WS_WOT = WS_MF + (7u << 20);
constexpr size_t WS_WGU = WS_WOT + (512u << 10);
constexpr size_t WS_WDT = WS_WGU + (3584u << 10);
constexpr size_t WS_ST2 = WS_WDT + (1792u << 10);
constexpr size_t WS_CSP = WS_ST2 + (256u << 10);
constexpr size_t WS_CNT = WS_CSP + (64u << 10);

DEVI void glds16(const void* g, char* smem_base, int lds_off) {
    __builtin_amdgcn_global_load_lds((gptr_t)g, (__attribute__((address_space(3))) void*)((lds_ptr)smem_base + lds_off), 16, 0, 0);
}
DEVI bf16x8 lds_read16(char* smem, int off) { return *(const bf16x8*)(smem + off); }
DEVI bf16x8 lds_read_tr(char* smem, int off0, int off1) {
    s16x4 a = __builtin_amdgcn_ds_read_tr16_b64_v4i16((__attribute__((address_space(3))) s16x4*)((lds_ptr)smem + off0));
    s16x4 b = __builtin_amdgcn_ds_read_tr16_b64_v4i16((__attribute__((address_space(3))) s16x4*)((lds_ptr)smem + off1));
    s16x8 c = __builtin_shufflevector(a, b, 0, 1, 2, 3, 4, 5, 6, 7);
    return __builtin_bit_cast(bf16x8, c);
}
constexpr int K4_XA = 2;
#define K4_RB(xcd, idx) (((xcd) % K4_XA) * (16 / K4_XA) + ((idx) % (16 / K4_XA)))
#define K4_J(xcd, idx) (((xcd) / K4_XA) * (2 * K4_XA) + ((idx) / (16 / K4_XA)))
constexpr int K1_XA = 4;
#define K1_RB(xcd, idx) (((xcd) % K1_XA) * (16 / K1_XA) + ((idx) % (16 / K1_XA)))
#define K1_CT(xcd, idx) (((xcd) / K1_XA) * (2 * K1_XA) + ((idx) / (16 / K1_XA)))
DEVI void st16(void* p, u32x4 v) {
    asm volatile("global_store_dwordx4 %0, %1, off sc1\n\ts_nop 1" ::"v"(p), "v"(v) : "memory");
}
typedef __attribute__((address_space(1))) unsigned* gcnt_t;
DEVI gcnt_t counter(char* ws, int edge, int rb) { return (gcnt_t)(ws + WS_CNT + (size_t)(edge * 16 + rb) * 256); }
DEVI void arrive(gcnt_t c) { __hip_atomic_fetch_add(c, 1u, __ATOMIC_RELAXED, __HIP_MEMORY_SCOPE_AGENT); }
DEVI void await(gcnt_t c, unsigned n) {
    for (int spin = 0; spin < (1 << 16); ++spin) {
        if (__hip_atomic_load(c, __ATOMIC_RELAXED, __HIP_MEMORY_SCOPE_AGENT) >= n) break;
        __builtin_amdgcn_s_sleep(1);
    }
}
DEVI bf16x8 load_dev(__amdgpu_buffer_rsrc_t rs, int voff, int soff) {
    return __builtin_bit_cast(bf16x8, __builtin_amdgcn_raw_buffer_load_b128(rs, voff, soff, 16));
}
DEVI unsigned pack2(float a, float b) { return __builtin_bit_cast(unsigned, __builtin_convertvector(f32x2{a, b}, bf16x2)); }
DEVI float bf_lo(unsigned u) { return __builtin_bit_cast(float, u << 16); }
DEVI float bf_hi(unsigned u) { return __builtin_bit_cast(float, u & 0xffff0000u); }
DEVI float swap32_add(float v) {
    unsigned a = __builtin_bit_cast(unsigned, v), b = a;
    asm volatile("" : "+v"(b));
    auto r = __builtin_amdgcn_permlane32_swap(a, b, false, false);
    const unsigned r0 = r[0], r1 = r[1];
    return __builtin_bit_cast(float, r0) + __builtin_bit_cast(float, r1);
}
DEVI void swap32(unsigned& a, unsigned& b) {
    auto r = __builtin_amdgcn_permlane32_swap(a, b, false, false);
    const unsigned r0 = r[0], r1 = r[1];
    a = r0;
    b = r1;
}
DEVI float swap32_max(float v) {
    unsigned a = __builtin_bit_cast(unsigned, v), b = a;
    asm volatile("" : "+v"(b));
    auto r = __builtin_amdgcn_permlane32_swap(a, b, false, false);
    const unsigned r0 = r[0], r1 = r[1];
    return fmaxf(__builtin_bit_cast(float, r0), __builtin_bit_cast(float, r1));
}
DEVI f32x4 mfma16(bf16x8 a, bf16x8 b, f32x4 c) { return __builtin_amdgcn_mfma_f32_16x16x32_bf16(a, b, c, 0, 0, 0); }
DEVI f32x16 mfma32(bf16x8 a, bf16x8 b, f32x16 c) { return __builtin_amdgcn_mfma_f32_32x32x16_bf16(a, b, c, 0, 0, 0); }
DEVI bf16x8 ones8() {
    u32x4 o = {0x3F803F80u, 0x3F803F80u, 0x3F803F80u, 0x3F803F80u};
    return __builtin_bit_cast(bf16x8, o);
}

constexpr int K1_SLOT = 28672, K1_XB = 16384, K1_RING = 5;
constexpr int K1_STAT = K1_SLOT * K1_RING;
constexpr int K1_CSP = K1_STAT + 1024;
constexpr int K1_CS = K1_CSP + 1536;
constexpr int K1_JOB = K1_CS + 384, K1_LDS = K1_JOB + 2048;

DEVI void qkv_loader(const uint16_t* __restrict__ x, const uint16_t* __restrict__ wqkv, const uint16_t* __restrict__ wo, char* __restrict__ ws,
                     char* smem, int rb, int ct, int w, int lane) {
    const char* xsrc[4];
#pragma unroll
    for (int i = 0; i < 4; ++i) {
        const int r = 32 * w + 8 * i + (lane >> 3);
        const int c = (lane & 7) ^ ((r >> 1) & 7);
        xsrc[i] = (const char*)x + (size_t)(rb * 128 + r) * 1024 + c * 16;
    }
    const char* wsrc[3];
#pragma unroll
    for (int i = 0; i < 3; ++i) {
        const int ci = 64 * (3 * w + i) + lane;
        const int r = ci / 12, cp = ci % 12;
        const int c = cp ^ (((r >> 3) & 1) << 1);
        wsrc[i] = (const char*)wqkv + (size_t)r * 3072 + ct * 192 + c * 16;
    }
    auto issue = [&](int c) {
        const int slot = (c % K1_RING) * K1_SLOT;
#pragma unroll
        for (int i = 0; i < 4; ++i) glds16(xsrc[i] + c * 128, smem, slot + (32 * w + 8 * i) * 128);
#pragma unroll
        for (int i = 0; i < 3; ++i) glds16(wsrc[i] + (size_t)c * 64 * 3072, smem, slot + K1_XB + (3 * w + i) * 1024);
    };
    const int m = lane & 15, g = lane >> 4, wq = (lane >> 2) & 3, wp = lane & 3;
    int woff[3];
#pragma unroll
    for (int nt = 0; nt < 3; ++nt)
        woff[nt] = K1_XB + ((w >> 1) * 32 + 8 * g + wq) * 192 + wp * 8 + (((3 * (w & 1) + nt) ^ (g & 1)) << 5);
    const bf16x8 ones = ones8();
    f32x4 cs[3];
#pragma unroll
    for (int nt = 0; nt < 3; ++nt) cs[nt] = f32x4{0, 0, 0, 0};
    const int jfp = blockIdx.x & 7, js = blockIdx.x >> 3;
#pragma unroll
    for (int c = 0; c < K1_RING; ++c) issue(c);
    if (w == 0) {
#pragma unroll
        for (int n = 0; n < 2; ++n) glds16((const char*)wo + (size_t)(16 * js + 8 * n + (lane >> 3)) * (D_ * 2) + jfp * 128 + (lane & 7) * 16, smem, K1_JOB + n * 1024);
    }
#pragma unroll
    for (int c = 0; c < 8; ++c) {
        if (c == 0) WAIT_V(28);
        else if (c <= 4) WAIT_V(21);
        else if (c == 5) WAIT_V(14);
        else if (c == 6) WAIT_V(7);
        else WAIT_V(0);
        WAIT_L0();
        BAR();
        const int slot = (c % K1_RING) * K1_SLOT;
        bf16x8 wf[3];
#pragma unroll
        for (int nt = 0; nt < 3; ++nt) wf[nt] = lds_read_tr(smem, slot + woff[nt], slot + woff[nt] + 4 * 192);
        SB();
        if (c >= 1 && c - 1 + K1_RING < 8) issue(c - 1 + K1_RING);
        SB();
#pragma unroll
        for (int nt = 0; nt < 3; ++nt) cs[nt] = mfma16(wf[nt], ones, cs[nt]);
        SB();
    }
    if (m == 0) {
#pragma unroll
        for (int nt = 0; nt < 3; ++nt) *(f32x4*)(smem + K1_CSP + ((w >> 1) * 96 + (3 * (w & 1) + nt) * 16 + 4 * g) * 4) = cs[nt];
    }
    WAIT_L0();
    BAR();
    if (w < 2) {
        const int t = w * 64 + lane;
        const float* csp = (const float*)(smem + K1_CSP);
        if (t < 96) ((float*)(smem + K1_CS))[t] = csp[t] + csp[96 + t];
    }
    WAIT_L0();
    BAR();
    if (w == 0) {
        const int G = lane >> 4, nh = G & 1, hh = G >> 1, rq = (lane >> 2) & 3, pp = lane & 3;
#pragma unroll
        for (int t = 0; t < 2; ++t) {
            const int o = K1_JOB + (4 * hh + rq) * 128 + t * 64 + nh * 32 + pp * 8;
            st16(ws + WS_WOT + (size_t)(jfp * 32 + js) * 2048 + t * 1024 + lane * 16, __builtin_bit_cast(u32x4, lds_read_tr(smem, o, o + 8 * 128)));
        }
    }
}

template <int NV>
DEVI void qkv_body(char* __restrict__ ws, char* smem, int rb, int ct, int w, int lane) {
    const int m = lane & 15, g = lane >> 4;
    const int wr = w & 1, wc = w >> 1;
    int xoff[4][2];
#pragma unroll
    for (int tt = 0; tt < 4; ++tt)
#pragma unroll
        for (int s = 0; s < 2; ++s) {
            const int r = 64 * wr + 16 * (tt ^ (2 * wc)) + m;
            xoff[tt][s] = r * 128 + (((4 * s + g) ^ ((r >> 1) & 7)) << 4);
        }
    const int wq = (lane >> 2) & 3, wp = lane & 3;
    int woff[3];
#pragma unroll
    for (int nt = 0; nt < 3; ++nt) woff[nt] = K1_XB + (8 * g + wq) * 192 + wp * 8 + (((3 * wc + nt) ^ (g & 1)) << 5);
    f32x4 acc[4][3], ssum[2], ssq[2];
#pragma unroll
    for (int tt = 0; tt < 4; ++tt)
#pragma unroll
        for (int nt = 0; nt < 3; ++nt) acc[tt][nt] = f32x4{0, 0, 0, 0};
#pragma unroll
    for (int tt = 0; tt < 2; ++tt) {
        ssum[tt] = f32x4{0, 0, 0, 0};
        ssq[tt] = f32x4{0, 0, 0, 0};
    }
    const bf16x8 ones = ones8();
    bf16x8 xf[2][4], wf[2][3];
    auto rd = [&](int ks, int n) {
        const int slot = ((ks >> 1) % K1_RING) * K1_SLOT;
        if (n == 0 || (n >= 2 && n <= 4)) {
            const int tt = n == 0 ? 0 : n - 1;
            xf[ks & 1][tt] = lds_read16(smem, slot + xoff[tt][ks & 1]);
        } else {
            const int nt = n == 1 ? 0 : n - 4;
            const int o = slot + woff[nt] + (ks & 1) * 32 * 192;
            wf[ks & 1][nt] = lds_read_tr(smem, o, o + 4 * 192);
        }
    };
    BAR();
#pragma unroll
    for (int n = 0; n < 7; ++n) rd(0, n);
    SB();
#pragma unroll
    for (int ks = 0; ks < 16; ++ks) {
        const int b = ks & 1;
        const int first = b ? 6 : 0;
#pragma unroll
        for (int n = 0; n < 16; ++n) {
            if (b && ks < 15 && n == first) {
                WAIT_L0();
                BAR();
                SB();
            }
            if (n < 12) {
                const int nt = n >> 2, tt = n & 3;
                acc[tt][nt] = (nt >= 3 - NV) ? mfma16(xf[b][tt], wf[b][nt], acc[tt][nt]) : mfma16(wf[b][nt], xf[b][tt], acc[tt][nt]);
            } else if (n < 14) {
                ssum[n - 12] = mfma16(ones, xf[b][n - 12], ssum[n - 12]);
            } else {
                ssq[n - 14] = mfma16(xf[b][n - 14], xf[b][n - 14], ssq[n - 14]);
            }
            if (ks < 15 && n >= first && n < first + 7) rd(ks + 1, n - first);
            SB();
        }
    }
    f32x2* stat = (f32x2*)(smem + K1_STAT);
    const float* csf = (const float*)(smem + K1_CS);
#pragma unroll
    for (int tt = 0; tt < 2; ++tt) {
        const int r = m & 3;
        const float sq = r == 0 ? ssq[tt][0] : r == 1 ? ssq[tt][1] : r == 2 ? ssq[tt][2] : ssq[tt][3];
        const float mean = ssum[tt][0] * (1.0f / D_);
        const float var = fmaxf(sq * (1.0f / D_) - mean * mean, 0.0f);
        if ((m >> 2) == g) stat[64 * wr + 32 * wc + 16 * tt + m] = f32x2{mean, __builtin_amdgcn_rsqf(var + EPS)};
    }
    WAIT_L0();
    BAR();
    BAR();
#pragma unroll
    for (int nt = 0; nt < 3; ++nt) {
        const int ntl = 3 * wc + nt;
        const int col = ct * 96 + ntl * 16;
        const int head = (col & 511) >> 4;
        const bool isv = nt >= 3 - NV;
        const int isk = col >= 512;
        const float sc = (!isv && !isk) ? QSCALE : 1.0f;
        const f32x4 c4 = *(const f32x4*)(csf + ntl * 16 + 4 * g);
        const float c1 = csf[ntl * 16 + m];
        char* base = ws + (isv ? WS_VF : isk ? WS_KF : WS_QF) + head * 1024 +
                     (isv ? lane * 16 : ((lane & 15) + 16 * (lane >> 5) + 32 * ((lane >> 4) & 1)) * 16);
#pragma unroll
        for (int pr = 0; pr < 2; ++pr) {
            unsigned pk[2][2];
#pragma unroll
            for (int u = 0; u < 2; ++u) {
                const int tt = 2 * pr + u;
                const int tok0 = 64 * wr + 16 * (tt ^ (2 * wc));
                float v[4];
                if (!isv) {
                    const f32x2 st = stat[tok0 + m];
                    const float rs = st[1] * sc;
#pragma unroll
                    for (int r = 0; r < 4; ++r) v[r] = (acc[tt][nt][r] - st[0] * c4[r]) * rs;
                } else {
#pragma unroll
                    for (int r = 0; r < 4; ++r) {
                        const f32x2 st = stat[tok0 + 4 * g + r];
                        v[r] = (acc[tt][nt][r] - st[0] * c1) * st[1];
                    }
                }
                pk[u][0] = pack2(v[0], v[1]);
                pk[u][1] = pack2(v[2], v[3]);
            }
            swap32(pk[0][0], pk[1][0]);
            swap32(pk[0][1], pk[1][1]);
            const int tile = rb * 4 + 2 * wr + (pr ^ wc);
            st16(base + (size_t)tile * 32768, u32x4{pk[0][0], pk[0][1], pk[1][0], pk[1][1]});
        }
    }
}

__global__ __launch_bounds__(512, 2) void k1_qkv(const uint16_t* __restrict__ x, const uint16_t* __restrict__ wqkv, const uint16_t* __restrict__ wo,
                                                 char* __restrict__ ws) {
    extern __shared__ __attribute__((aligned(16))) char smem[];
    const int lane = threadIdx.x & 63, wv = __builtin_amdgcn_readfirstlane(threadIdx.x >> 6), w = wv & 3;
    const int xcd = blockIdx.x & 7, idx = blockIdx.x >> 3;
    const int rb = K1_RB(xcd, idx), ct = K1_CT(xcd, idx);
    if (wv >= 4) {
        qkv_loader(x, wqkv, wo, ws, smem, rb, ct, w, lane);
        return;
    }
    if (blockIdx.x < 32 && threadIdx.x == 0) *(unsigned*)(ws + WS_CNT + (size_t)blockIdx.x * 256) = 0u;
    if (ct >= 11) qkv_body<3>(ws, smem, rb, ct, w, lane);
    else if (ct == 10 && w >= 2) qkv_body<2>(ws, smem, rb, ct, w, lane);
    else qkv_body<0>(ws, smem, rb, ct, w, lane);
    WAIT_V(0);
}

constexpr int K2_VS = 2064;
constexpr int K2_K = 0, K2_V = 24576, K2_Q = K2_V + 12 * K2_VS + 192, K2_JOB = K2_Q + 8192, K2_RED = K2_JOB + 32768;
constexpr int K2_LDS = K2_RED + 8 * 128 * 4;

DEVI f32x16 splat16(float v) {
    f32x16 r;
#pragma unroll
    for (int i = 0; i < 16; ++i) r[i] = v;
    return r;
}
constexpr unsigned long long row_mask(int r) {
    unsigned long long m = 0;
    for (int l = 0; l < 64; ++l)
        if ((r & 3) + 8 * (r >> 2) + 4 * (l >> 5) > (l & 31)) m |= 1ull << l;
    return m;
}
template <int R>
DEVI unsigned sel_row(unsigned no, unsigned yes) { return __builtin_amdgcn_inverse_ballot_w64(row_mask(R)) ? yes : no; }
template <int R>
DEVI void merge_rows(const f32x16& sf, const f32x16& sl, f32x16& mg) {
    if constexpr (R < 16) {
        const float no = sl[R], yes = sf[R];
        mg[R] = __builtin_amdgcn_inverse_ballot_w64(row_mask(R)) ? yes : no;
        merge_rows<R + 1>(sf, sl, mg);
    }
}
DEVI f32x16 attn_merge(const f32x16& sf, const f32x16& sl) {
    f32x16 mg;
    merge_rows<0>(sf, sl, mg);
    return mg;
}
template <int R>
DEVI void split_rows(const f32x16& mg, float mx, unsigned (&pf)[8], unsigned (&pl)[8]) {
    if constexpr (R < 16) {
        const unsigned pk = pack2(__builtin_amdgcn_exp2f(mg[R] - mx), __builtin_amdgcn_exp2f(mg[R + 1] - mx));
        const unsigned mk = sel_row<R>(sel_row<R + 1>(0u, 0xffff0000u), 0xffffffffu);
        pf[R >> 1] = pk & mk;
        pl[R >> 1] = pk & ~mk;
        split_rows<R + 2>(mg, mx, pf, pl);
    }
}
DEVI void attn_split(const f32x16& mg, float mx, unsigned (&pf)[8], unsigned (&pl)[8]) { split_rows<0>(mg, mx, pf, pl); }
DEVI bf16x8 as_frag(const unsigned* p) { return __builtin_bit_cast(bf16x8, u32x4{p[0], p[1], p[2], p[3]}); }

template <bool FULL>
DEVI f32x16 attn_exact(char* smem, bf16x8 q, int koff, int vb0, int vb1, int i, int umin) {
    const float NEG = -__builtin_inff();
    const f32x16 zero = {0};
    const f32x16 sl = mfma32(lds_read16(smem, koff + 8 * 2048), q, zero);
    f32x16 sf = splat16(NEG);
    if (FULL || i >= umin) sf = mfma32(lds_read16(smem, koff), q, zero);
    const f32x16 mg = attn_merge(sf, sl);
    float mx = fmaxf(mg[0], mg[1]);
#pragma unroll
    for (int r = 2; r < 16; r += 2) mx = fmaxf(fmaxf(mx, mg[r]), mg[r + 1]);
#pragma unroll 1
    for (int t = 1; t < 8; ++t) {
        if (FULL || i + t >= umin) {
            const f32x16 s = mfma32(lds_read16(smem, koff + t * 2048), q, zero);
#pragma unroll
            for (int r = 0; r < 16; r += 2) mx = fmaxf(fmaxf(mx, s[r]), s[r + 1]);
        }
    }
    mx = swap32_max(mx);
    const f32x16 negm = splat16(-mx);
    f32x16 o = zero;
    unsigned pf[8], pl[8];
    attn_split(mg, mx, pf, pl);
#pragma unroll
    for (int s = 0; s < 2; ++s) {
        o = mfma32(lds_read16(smem, (s ? vb1 : vb0) + 8 * K2_VS), as_frag(pl + 4 * s), o);
        if (FULL || i >= umin) o = mfma32(lds_read16(smem, s ? vb1 : vb0), as_frag(pf + 4 * s), o);
    }
#pragma unroll 1
    for (int t = 1; t < 8; ++t) {
        if (FULL || i + t >= umin) {
            const f32x16 s = mfma32(lds_read16(smem, koff + t * 2048), q, negm);
            unsigned pk[8];
#pragma unroll
            for (int r = 0; r < 16; r += 2) pk[r >> 1] = pack2(__builtin_amdgcn_exp2f(s[r]), __builtin_amdgcn_exp2f(s[r + 1]));
            o = mfma32(lds_read16(smem, vb0 + t * K2_VS), as_frag(pk), o);
            o = mfma32(lds_read16(smem, vb1 + t * K2_VS), as_frag(pk + 4), o);
        }
    }
    return o;
}

template <bool FULL>
DEVI f32x16 attn_fast(char* smem, bf16x8 q, int koff, int vb0, int vb1, int i, int umin) {
    const float NEG = -__builtin_inff();
    const f32x16 zero = {0};
    auto cinit = [&](int t) { return FULL ? zero : splat16(i + t >= umin ? 0.0f : NEG); };
    bf16x8 kf[2], vf[2][2];
    const bf16x8 kd = lds_read16(smem, koff + 8 * 2048), k0 = lds_read16(smem, koff);
    kf[1] = lds_read16(smem, koff + 2048);
    const bf16x8 vd0 = lds_read16(smem, vb0 + 8 * K2_VS), vz0 = lds_read16(smem, vb0);
    const bf16x8 vd1 = lds_read16(smem, vb1 + 8 * K2_VS), vz1 = lds_read16(smem, vb1);
    const f32x16 sl = mfma32(kd, q, zero);
    const f32x16 sf = mfma32(k0, q, cinit(0));
    f32x16 sc[2];
    sc[1] = mfma32(kf[1], q, cinit(1));
    kf[0] = lds_read16(smem, koff + 2 * 2048);
    vf[1][0] = lds_read16(smem, vb0 + K2_VS);
    vf[1][1] = lds_read16(smem, vb1 + K2_VS);
    SB();
    f32x16 o = zero;
    {
        unsigned pf[8], pl[8];
        attn_split(attn_merge(sf, sl), 0.0f, pf, pl);
        o = mfma32(vd0, as_frag(pl), o);
        o = mfma32(vz0, as_frag(pf), o);
        o = mfma32(vd1, as_frag(pl + 4), o);
        o = mfma32(vz1, as_frag(pf + 4), o);
    }
    SB();
#pragma unroll
    for (int t = 1; t < 8; ++t) {
        const int b = t & 1;
        if (t < 7) {
            sc[b ^ 1] = mfma32(kf[b ^ 1], q, cinit(t + 1));
            if (t < 6) kf[b] = lds_read16(smem, koff + (t + 2) * 2048);
            vf[b ^ 1][0] = lds_read16(smem, vb0 + (t + 1) * K2_VS);
            vf[b ^ 1][1] = lds_read16(smem, vb1 + (t + 1) * K2_VS);
        }
        SB();
        unsigned pk[8];
#pragma unroll
        for (int r = 0; r < 8; r += 2) pk[r >> 1] = pack2(__builtin_amdgcn_exp2f(sc[b][r]), __builtin_amdgcn_exp2f(sc[b][r + 1]));
        const float e8 = __builtin_amdgcn_exp2f(sc[b][8]), e9 = __builtin_amdgcn_exp2f(sc[b][9]);
        SB();
        o = mfma32(vf[b][0], as_frag(pk), o);
        SB();
        pk[4] = pack2(e8, e9);
#pragma unroll
        for (int r = 10; r < 16; r += 2) pk[r >> 1] = pack2(__builtin_amdgcn_exp2f(sc[b][r]), __builtin_amdgcn_exp2f(sc[b][r + 1]));
        SB();
        o = mfma32(vf[b][1], as_frag(pk + 4), o);
        SB();
    }
    return o;
}

template <bool FULL>
DEVI u32x4 attn_task(char* smem, bf16x8 q, int rb, int i, int e, int lane) {
    const int a = lane & 31, h = lane >> 5;
    const int koff = K2_K + e * 1024 + i * 2048 + lane * 16;
    const bool isdata = a < 16;
    const int vb0 = K2_V + i * K2_VS + (isdata ? e * 1024 + h * 256 + a * 16 : 2048);
    const int vb1 = vb0 + (isdata ? 512 : 0);
    const int umin = FULL ? 0 : 8 - 4 * rb;
    f32x16 o = attn_fast<FULL>(smem, q, koff, vb0, vb1, i, umin);
    float mag = 0.f;
#pragma unroll
    for (int r = 0; r < 8; ++r) mag += __builtin_fabsf(o[r]);
    const bool safe = o[8] >= 0x1p-60f && o[8] <= 0x1p60f && mag <= 0x1p120f;
    if (__builtin_amdgcn_ballot_w64(!safe) != 0) {
        o = attn_exact<FULL>(smem, q, koff, vb0, vb1, i, umin);
    }
    const float inv = __builtin_amdgcn_rcpf(o[8]);
    return u32x4{pack2(o[0] * inv, o[1] * inv), pack2(o[2] * inv, o[3] * inv), pack2(o[4] * inv, o[5] * inv), pack2(o[6] * inv, o[7] * inv)};
}

constexpr int G64_RED = 0, G64_T = 49152;
DEVI void oproj_phase(const uint16_t* __restrict__ x, char* __restrict__ ws, char* smem, int tb, int fp, int w, int lane) {
    constexpr int NS = 8;
    const int tt = w & 1, ft = w >> 1;
    const char* wt = ws + WS_WOT + (size_t)fp * 65536 + (size_t)(w * NS) * 2048 + lane * 16;
    bf16x8 fa[NS][2], fb[NS][2];
#pragma unroll
    for (int s = 0; s < NS; ++s)
#pragma unroll
        for (int q = 0; q < 2; ++q) fa[s][q] = *(const bf16x8*)(wt + s * 2048 + (q ^ (w >> 1)) * 1024);
    u32x4 xr[2];
#pragma unroll
    for (int n = 0; n < 2; ++n) {
        const int r = (w * 2 + n) * 8 + (lane >> 3);
        xr[n] = *(const u32x4*)((const char*)x + (size_t)(tb * 64 + r) * 1024 + fp * 128 + (lane & 7) * 16);
    }
    SB();
    BAR();
    SB();
    const __amdgpu_buffer_rsrc_t rs = __builtin_amdgcn_make_buffer_rsrc(ws + WS_OF, 0, 2 << 20, 0x00020000);
#pragma unroll
    for (int s = 0; s < NS; ++s) {
#pragma unroll
        for (int q = 0; q < 2; ++q) fb[s][q] = load_dev(rs, ((tb * 2 + (q ^ (w & 1))) * 32 + w * NS) * 1024 + lane * 16, s * 1024);
        SB();
    }
    f32x16 acc[4];
#pragma unroll
    for (int q = 0; q < 4; ++q) acc[q] = f32x16{0};
#pragma unroll
    for (int s = 0; s < NS; ++s) {
#pragma unroll
        for (int q = 0; q < 4; ++q) acc[q] = mfma32(fa[s][q >> 1], fb[s][q & 1], acc[q]);
        SB();
    }
#pragma unroll
    for (int n = 0; n < 2; ++n) {
        const int r = (w * 2 + n) * 8 + (lane >> 3);
        *(u32x4*)(smem + G64_T + r * 128 + (((lane & 7) ^ ((r >> 1) & 7)) << 4)) = xr[n];
    }
#pragma unroll
    for (int q = 1; q < 4; ++q) {
        char* dst = smem + G64_RED + ((w ^ q) * 3 + q - 1) * 4096 + lane * 16;
#pragma unroll
        for (int r = 0; r < 4; ++r)
            *(f32x4*)(dst + r * 1024) = f32x4{acc[q][4 * r], acc[q][4 * r + 1], acc[q][4 * r + 2], acc[q][4 * r + 3]};
    }
    WAIT_L0();
    BAR();
    f32x16 o = acc[0];
#pragma unroll
    for (int q = 0; q < 3; ++q) {
        const char* src = smem + G64_RED + (w * 3 + q) * 4096 + lane * 16;
#pragma unroll
        for (int r = 0; r < 4; ++r) {
            const f32x4 v = *(const f32x4*)(src + r * 1024);
#pragma unroll
            for (int k = 0; k < 4; ++k) o[4 * r + k] += v[k];
        }
    }
    const int tok = lane & 31, h = lane >> 5;
    const int r = tt * 32 + tok;
    unsigned pk[8];
    float sum = 0.f, sq = 0.f;
#pragma unroll
    for (int qd = 0; qd < 4; ++qd) {
        const u32x2 xv = *(const u32x2*)(smem + G64_T + r * 128 + (((ft * 4 + qd) ^ ((r >> 1) & 7)) << 4) + h * 8);
        pk[2 * qd] = pack2(o[4 * qd] + bf_lo(xv[0]), o[4 * qd + 1] + bf_hi(xv[0]));
        pk[2 * qd + 1] = pack2(o[4 * qd + 2] + bf_lo(xv[1]), o[4 * qd + 3] + bf_hi(xv[1]));
#pragma unroll
        for (int k = 0; k < 2; ++k) {
            const float lo = bf_lo(pk[2 * qd + k]), hi = bf_hi(pk[2 * qd + k]);
            sum += lo + hi;
            sq += lo * lo + hi * hi;
        }
    }
    char* hdst = ws + WS_HF + (size_t)((tb * 2 + tt) * 32 + (fp * 2 + ft) * 2) * 1024 + lane * 16;
    st16(hdst, u32x4{pk[0], pk[1], pk[2], pk[3]});
    st16(hdst + 1024, u32x4{pk[4], pk[5], pk[6], pk[7]});
    sum = swap32_add(sum);
    sq = swap32_add(sq);
    if (h == 0) ((f32x2*)(ws + WS_ST2))[(fp * 2 + ft) * S_ + tb * 64 + r] = f32x2{sum, sq};
}

__global__ __launch_bounds__(512, 2) void k2_attn(const uint16_t* __restrict__ x, const uint16_t* __restrict__ wg, const uint16_t* __restrict__ wu,
                                                  const uint16_t* __restrict__ wd, char* __restrict__ ws) {
    extern __shared__ __attribute__((aligned(16))) char smem[];
    const int tid = threadIdx.x, lane = tid & 63, w = __builtin_amdgcn_readfirstlane(tid >> 6);
    const int xcd = blockIdx.x & 7, idx = blockIdx.x >> 3;
    const int rb = 2 * xcd + (idx >> 4), p = idx & 15;
    const int i = w & 3, e = w >> 2;
#pragma unroll
    for (int n = 0; n < 6; ++n) {
        const int pc = 6 * w + n;
        const int isv = pc >= 24, u = (pc % 24) >> 1, half = pc & 1;
        int kt = 4 * rb - 8 + u;
        kt = kt < 0 ? 0 : kt;
        const char* src = ws + (isv ? WS_VF : WS_KF) + (size_t)(kt * 16 + p) * 2048 + half * 1024 + lane * 16;
        glds16(src, smem, (isv ? K2_V + u * K2_VS : K2_K + u * 2048) + half * 1024);
    }
    glds16(ws + WS_QF + (size_t)((rb * 4 + i) * 32 + 2 * p + e) * 1024 + lane * 16, smem, K2_Q + w * 1024);
    if (tid < 48) *(unsigned*)(smem + K2_V + (tid >> 2) * K2_VS + 2048 + (tid & 3) * 4) = 0x3F803F80u;
    const int bid = blockIdx.x;
    const int job = K2_JOB + w * 4096;
    if (bid < 112) {
        const int cg = bid % 28, s = 8 * (bid / 28) + w;
#pragma unroll
        for (int n = 0; n < 4; ++n) {
            const uint16_t* src = (n >> 1) ? wu : wg;
            const int row = 16 * s + 8 * (n & 1) + (lane >> 3);
            glds16((const char*)src + (size_t)row * (F_ * 2) + cg * 128 + (lane & 7) * 16, smem, job + n * 1024);
        }
    } else if (bid < 224) {
        const int s = bid - 112;
#pragma unroll
        for (int n = 0; n < 2; ++n) {
            const int row = 16 * s + 8 * n + (lane >> 3);
            glds16((const char*)wd + (size_t)row * (D_ * 2) + w * 128 + (lane & 7) * 16, smem, job + n * 1024);
        }
    }
    if (bid < 112) WAIT_V(4);
    else if (bid < 224) WAIT_V(2);
    else WAIT_V(0);
    WAIT_L0();
    BAR();
    const bf16x8 q = lds_read16(smem, K2_Q + w * 1024 + lane * 16);
    auto retile = [&]() {
        const int G = lane >> 4, nh = G & 1, hh = G >> 1, rq = (lane >> 2) & 3, pp = lane & 3;
        if (bid < 112) {
            const int cg = bid % 28, kq = bid / 28, s = 8 * kq + w;
            float* red = (float*)(smem + K2_RED);
#pragma unroll
            for (int t = 0; t < 4; ++t) {
                const int o = job + nh * 2048 + (4 * hh + rq) * 128 + t * 32 + pp * 8;
                const bf16x8 f = lds_read_tr(smem, o, o + 8 * 128);
                const int T = 4 * cg + t, j = T / 7, t7 = T % 7;
                const u32x4 u = __builtin_bit_cast(u32x4, f);
                st16(ws + WS_WGU + (size_t)((j * 32 + s) * 7 + t7) * 1024 + lane * 16, u);
                float sum = 0.f;
#pragma unroll
                for (int r = 0; r < 4; ++r) sum += bf_lo(u[r]) + bf_hi(u[r]);
                sum = swap32_add(sum);
                if (lane < 32) red[w * 128 + nh * 64 + t * 16 + (lane & 15)] = sum;
            }
        } else if (bid < 224) {
            char* dst = ws + WS_WDT + (size_t)(w * 112 + bid - 112) * 2048 + lane * 16;
#pragma unroll
            for (int t = 0; t < 2; ++t) {
                const int o = job + (4 * hh + rq) * 128 + t * 64 + nh * 32 + pp * 8;
                st16(dst + t * 1024, __builtin_bit_cast(u32x4, lds_read_tr(smem, o, o + 8 * 128)));
            }
        }
        SB();
    };
    const u32x4 ov = rb >= 2 ? attn_task<true>(smem, q, rb, i, e, lane) : attn_task<false>(smem, q, rb, i, e, lane);
    WAIT_V0();
    st16(ws + WS_OF + (size_t)((rb * 4 + i) * 32 + 2 * p + e) * 1024 + lane * 16, ov);
    SB();
    retile();
    if (bid < 112) WAIT_V(4);
    else if (bid < 224) WAIT_V(2);
    else WAIT_V(0);
    WAIT_L0();
    BAR();
    if (tid == 0) arrive(counter(ws, 0, rb));
    if (w >= 4) {
        if (bid < 112 && (w == 5 || w == 6)) {
            const int cg = bid % 28, kq = bid / 28, t = tid - 320;
            const float* red = (const float*)(smem + K2_RED);
            float sum = 0.f;
#pragma unroll
            for (int ww = 0; ww < 8; ++ww) sum += red[ww * 128 + t];
            ((float*)(ws + WS_CSP))[(kq * 2 + (t >> 6)) * F_ + cg * 64 + (t & 63)] = sum;
        }
        if (w == 4) await(counter(ws, 0, rb), 16);
        BAR();
        BAR();
        return;
    }
    oproj_phase(x, ws, smem, 4 * xcd + (idx >> 3), idx & 7, w, lane);
    WAIT_V(0);
}

constexpr int K4_SLOT = 28672, K4_RING = 4, K4_CS = K4_SLOT * 5, K4_LDS = K4_CS + 1024, K5_T = 3 * K4_SLOT;

constexpr int K4_BWIN = 8;
constexpr int K5_WIN = 7;
constexpr int wd_first(int c) {
    constexpr int n[8] = {1, 1, 1, 1, 1, 1, 1, 0};
    int t = 0;
    for (int i = 0; i < c; ++i) t += n[i];
    return t;
}
constexpr int wd_extra(int c) { return 2 * (wd_first(c + 1) - wd_first(c)); }
constexpr int wd_pending(int c) {
    int n = c < K4_RING ? 7 * (K4_RING - 1 - c) : 0;
    for (int b = c < K4_RING ? 0 : c - K4_RING + 1; b < c; ++b) {
        const bool ring = b >= 1 && b - 1 + K4_RING < 8 && b - 1 + K4_RING != c;
        n += (ring ? 7 : 0) + wd_extra(b);
    }
    return n;
}
DEVI int wd_slot(int w) { return (w == 0 ? 4 : w - 1) * K4_SLOT; }
template <int HI>
DEVI void down_phase(char* __restrict__ ws, uint16_t* __restrict__ out, char* smem, int tb, int fp, int w, int lane,
                     const bf16x8 (&fr)[14][2]) {
    constexpr int NS = 14, WIN = HI ? K5_WIN : NS;
    const int w8 = w + 4 * HI;
    const __amdgpu_buffer_rsrc_t rs = __builtin_amdgcn_make_buffer_rsrc(ws + WS_MF, 0, 7 << 20, 0x00020000);
    bf16x8 fb[NS][2];
    int vb[2];
#pragma unroll
    for (int q = 0; q < 2; ++q) vb[q] = ((tb * 2 + (q ^ (w & 1))) * 112 + w8 * NS) * 1024 + lane * 16;
    auto load = [&](int s) {
#pragma unroll
        for (int q = 0; q < 2; ++q) fb[s][q] = load_dev(rs, vb[q], s * 1024);
    };
#pragma unroll
    for (int s = 0; s < WIN; ++s) {
        load(s);
        SB();
    }
    bf16x8 fl[2][2];
    auto rdw = [&](int s) {
#pragma unroll
        for (int q = 0; q < 2; ++q) fl[s & 1][q] = lds_read16(smem, wd_slot(w) + s * 2048 + (q ^ (w >> 1)) * 1024 + lane * 16);
    };
    if (!HI) rdw(0);
    const int tt = w & 1, ft = w >> 1;
    f32x16 acc[4];
#pragma unroll
    for (int q = 0; q < 4; ++q) acc[q] = f32x16{0};
    u32x4 hv;
#pragma unroll
    for (int s = 0; s < NS; ++s) {
        if (!HI && s + 1 < NS) rdw(s + 1);
        SB();
#pragma unroll
        for (int q = 0; q < 4; ++q) acc[q] = mfma32(HI ? fr[s][q >> 1] : fl[s & 1][q >> 1], fb[s][q & 1], acc[q]);
        SB();
        if (WIN + 2 * s < NS) load(WIN + 2 * s);
        if (WIN + 2 * s + 1 < NS) load(WIN + 2 * s + 1);
        if (s == (HI ? (NS - WIN) / 2 + 1 : 0)) hv = *(const u32x4*)(ws + WS_HF + (size_t)((tb * 2 + tt) * 32 + (fp * 2 + ft) * 2 + HI) * 1024 + lane * 16);
        SB();
    }
    auto piece = [&](int sw, int hi, int p) { return wd_slot(sw) + (hi * 7 + p) * 2048; };
    if (HI) BAR();
#pragma unroll
    for (int q = 0; q < 4; ++q)
#pragma unroll
        for (int hh = 0; hh < 2; ++hh) {
            if (q == 0 && hh == HI) continue;
            const int id = q * 2 + hh;
            char* dst = smem + piece(w, HI, id - (id > HI ? 1 : 0)) + lane * 16;
#pragma unroll
            for (int r = 0; r < 2; ++r)
                *(f32x4*)(dst + r * 1024) =
                    f32x4{acc[q][8 * hh + 4 * r], acc[q][8 * hh + 4 * r + 1], acc[q][8 * hh + 4 * r + 2], acc[q][8 * hh + 4 * r + 3]};
        }
    if (!HI) BAR();
    WAIT_L0();
    BAR();
    float o[8];
#pragma unroll
    for (int k = 0; k < 8; ++k) o[k] = acc[0][8 * HI + k];
#pragma unroll
    for (int q = 0; q < 4; ++q)
#pragma unroll
        for (int hi = 0; hi < 2; ++hi) {
            if (q == 0 && hi == HI) continue;
            const int id = q * 2 + HI;
            const char* src = smem + piece(w ^ q, hi, id - (id > hi ? 1 : 0)) + lane * 16;
#pragma unroll
            for (int r = 0; r < 2; ++r) {
                const f32x4 v = *(const f32x4*)(src + r * 1024);
#pragma unroll
                for (int k = 0; k < 4; ++k) o[4 * r + k] += v[k];
            }
        }
    const int tok = lane & 31, h = lane >> 5;
    const int r = tt * 32 + tok;
#pragma unroll
    for (int qd = 0; qd < 2; ++qd) {
        const unsigned a = hv[2 * qd], b = hv[2 * qd + 1];
        const u32x2 v = {pack2(o[4 * qd] + bf_lo(a), o[4 * qd + 1] + bf_hi(a)), pack2(o[4 * qd + 2] + bf_lo(b), o[4 * qd + 3] + bf_hi(b))};
        *(u32x2*)(smem + K5_T + r * 128 + (((ft * 4 + 2 * HI + qd) ^ ((r >> 1) & 7)) << 4) + h * 8) = v;
    }
    WAIT_L0();
    BAR();
    const int rr = w8 * 8 + (lane >> 3), cp = lane & 7;
    const u32x4 v = *(const u32x4*)(smem + K5_T + rr * 128 + cp * 16);
    *(u32x4*)((char*)out + (size_t)(tb * 64 + rr) * 1024 + fp * 128 + ((cp ^ ((rr >> 1) & 7)) << 4)) = v;
    WAIT_V(0);
}

__global__ __launch_bounds__(512, 2) void k4_mlp(char* __restrict__ ws, uint16_t* __restrict__ out) {
    extern __shared__ __attribute__((aligned(16))) char smem[];
    const int tid = threadIdx.x, lane = tid & 63, wv = __builtin_amdgcn_readfirstlane(tid >> 6), w = wv & 3;
    const int xcd = blockIdx.x & 7, idx = blockIdx.x >> 3;
    const int rb = K4_RB(xcd, idx), j = K4_J(xcd, idx);
    const int tile = rb * 4 + w;
    const int tb = 4 * xcd + (idx >> 3), fp = idx & 7;
    if (wv >= 4) {
        const char* asrc = ws + WS_WGU + (size_t)j * (32 * 7168) + w * 7168 + lane * 16;
        auto issue = [&](int c) {
#pragma unroll
            for (int n = 0; n < 7; ++n) glds16(asrc + c * 28672 + n * 1024, smem, (c % K4_RING) * K4_SLOT + w * 7168 + n * 1024);
        };
        const char* wdt = ws + WS_WDT + (size_t)fp * (112 * 2048) + lane * 16;
        bf16x8 fr[14][2];
        auto wd_regs = [&](int s0, int s1) {
#pragma unroll
            for (int s = s0; s < s1; ++s)
#pragma unroll
                for (int q = 0; q < 2; ++q) fr[s][q] = *(const bf16x8*)(wdt + (size_t)((w + 4) * 14 + s) * 2048 + (q ^ (w >> 1)) * 1024);
        };
        auto wd_stage = [&]() {
#pragma unroll
            for (int n = 0; n < 28; ++n) glds16(wdt + (size_t)(w * 14) * 2048 + n * 1024, smem, wd_slot(w) + n * 1024);
        };
#pragma unroll
        for (int c = 0; c < K4_RING; ++c) issue(c);
        static_for<0, 8>([&](auto cc) {
            constexpr int c = decltype(cc)::value;
            if (c >= 5 && w <= c - 5) wait_vm<wd_pending(c) + 28>();
            else wait_vm<wd_pending(c)>();
            BAR();
            if (c >= 1 && c - 1 + K4_RING < 8) issue(c - 1 + K4_RING);
            SB();
            wd_regs(wd_first(c), wd_first(c + 1));
            SB();
            if (c >= 4 && w == c - 4) wd_stage();
        });
        BAR();
        wd_regs(wd_first(8), 14);
        wait_vm<2 * (14 - wd_first(8))>();
        BAR();
        if (w == 0) await(counter(ws, 1, tb >> 1), 16);
        WAIT_V0();
        BAR();
        down_phase<1>(ws, out, smem, tb, fp, w, lane, fr);
        return;
    }
    const int tok = lane & 31, h = lane >> 5;
    const char* bsrc = ws + WS_HF + (size_t)tile * 32768 + lane * 16;
    bf16x8 bq[K4_BWIN];
#pragma unroll
    for (int s = 0; s < K4_BWIN; ++s) bq[s] = *(const bf16x8*)(bsrc + s * 1024);
    SB();
    f32x2 st[8];
#pragma unroll
    for (int n = 0; n < 8; ++n) st[n] = ((const f32x2*)(ws + WS_ST2))[(h * 8 + n) * S_ + tile * 32 + tok];
    {
        const int t = tid < 224 ? tid : 0;
        const int gu = t >= 112, col = j * 112 + (gu ? t - 112 : t);
        float csq[4];
#pragma unroll
        for (int n = 0; n < 4; ++n) csq[n] = ((const float*)(ws + WS_CSP))[(n * 2 + gu) * F_ + col];
        if (tid < 224) ((float*)(smem + K4_CS))[tid] = (csq[0] + csq[1]) + (csq[2] + csq[3]);
    }
    float sum = 0.f, sq = 0.f;
#pragma unroll
    for (int n = 0; n < 8; ++n) {
        sum += st[n][0];
        sq += st[n][1];
    }
    sum = swap32_add(sum);
    sq = swap32_add(sq);
    const float mean = sum * (1.0f / D_);
    const float rstd = __builtin_amdgcn_rsqf(fmaxf(sq * (1.0f / D_) - mean * mean, 0.0f) + EPS);
    f32x16 acc[7];
#pragma unroll
    for (int t = 0; t < 7; ++t) acc[t] = f32x16{0};
    bf16x8 af[2][7];
    auto rdA = [&](int s, int t) {
        af[s & 1][t] = lds_read16(smem, ((s >> 2) % K4_RING) * K4_SLOT + ((s & 3) * 7 + t) * 1024 + lane * 16);
    };
    WAIT_L0();
    BAR();
#pragma unroll
    for (int t = 0; t < 7; ++t) rdA(0, t);
    SB();
#pragma unroll
    for (int s = 0; s < 32; ++s) {
        const int b = s & 1;
        const bool edge = (s & 3) == 3;
#pragma unroll
        for (int t = 0; t < 7; ++t) {
            if (edge && s < 31 && t == 4) {
                WAIT_L0();
                BAR();
                SB();
            }
            acc[t] = mfma32(af[b][t], bq[s % K4_BWIN], acc[t]);
            if (s < 31) {
                if (!edge) {
                    rdA(s + 1, t);
                } else if (t == 4) {
                    rdA(s + 1, 0);
                    rdA(s + 1, 1);
                } else if (t == 5) {
                    rdA(s + 1, 2);
                    rdA(s + 1, 3);
                    rdA(s + 1, 4);
                } else if (t == 6) {
                    rdA(s + 1, 5);
                    rdA(s + 1, 6);
                }
            }
            SB();
        }
        if (s + K4_BWIN < 32) bq[s % K4_BWIN] = *(const bf16x8*)(bsrc + (s + K4_BWIN) * 1024);
        SB();
    }
    const float* cs = (const float*)(smem + K4_CS);
    char* dst = ws + WS_MF + (size_t)(tile * 112 + j * 7) * 1024 + lane * 16;
#pragma unroll
    for (int t = 0; t < 7; ++t) {
        float v[8];
#pragma unroll
        for (int qd = 0; qd < 2; ++qd) {
            const f32x4 cg = *(const f32x4*)(cs + t * 16 + 8 * qd + 4 * h);
            const f32x4 cu = *(const f32x4*)(cs + 112 + t * 16 + 8 * qd + 4 * h);
#pragma unroll
            for (int r = 0; r < 4; ++r) {
                const float gt = (acc[t][4 * qd + r] - mean * cg[r]) * rstd;
                const float up = (acc[t][8 + 4 * qd + r] - mean * cu[r]) * rstd;
                v[4 * qd + r] = fmaxf(gt, 0.0f) * up;
            }
        }
        st16(dst + t * 1024, u32x4{pack2(v[0], v[1]), pack2(v[2], v[3]), pack2(v[4], v[5]), pack2(v[6], v[7])});
    }
    BAR();
    WAIT_V(0);
    BAR();
    if (tid == 0) arrive(counter(ws, 1, rb));
    BAR();
    const bf16x8 none[14][2] = {};
    down_phase<0>(ws, out, smem, tb, fp, w, lane, none);
}

extern "C" void kernel_launch(void* const* d_in, const int* in_sizes, int n_in, void* d_out, int out_size, void* d_ws,
                              size_t ws_size, hipStream_t stream) {
    const uint16_t* x = (const uint16_t*)d_in[0];
    const uint16_t* wqkv = (const uint16_t*)d_in[1];
    const uint16_t* wo = (const uint16_t*)d_in[2];
    const uint16_t* wg = (const uint16_t*)d_in[4];
    const uint16_t* wu = (const uint16_t*)d_in[5];
    const uint16_t* wd = (const uint16_t*)d_in[6];
    char* ws = (char*)d_ws;
    (void)hipFuncSetAttribute((const void*)k1_qkv, hipFuncAttributeMaxDynamicSharedMemorySize, K1_LDS);
    (void)hipFuncSetAttribute((const void*)k2_attn, hipFuncAttributeMaxDynamicSharedMemorySize, K2_LDS);
    (void)hipFuncSetAttribute((const void*)k4_mlp, hipFuncAttributeMaxDynamicSharedMemorySize, K4_LDS);
    k1_qkv<<<256, 512, K1_LDS, stream>>>(x, wqkv, wo, ws);
    k2_attn<<<256, 512, K2_LDS, stream>>>(x, wg, wu, wd, ws);
    k4_mlp<<<256, 512, K4_LDS, stream>>>(ws, (uint16_t*)d_out);
}
```

```cpp
#include <hip/hip_runtime.h>
#include <hip/hip_bf16.h>
#include <stdint.h>
#include <type_traits>

typedef __attribute__((ext_vector_type(8))) __bf16 bf16x8;
typedef __attribute__((ext_vector_type(2))) __bf16 bf16x2;
typedef __attribute__((ext_vector_type(4))) short s16x4;
typedef __attribute__((ext_vector_type(8))) short s16x8;
typedef __attribute__((ext_vector_type(2))) float f32x2;
typedef __attribute__((ext_vector_type(4))) float f32x4;
typedef __attribute__((ext_vector_type(16))) float f32x16;
typedef __attribute__((ext_vector_type(2))) unsigned u32x2;
typedef __attribute__((ext_vector_type(4))) unsigned u32x4;
typedef __attribute__((address_space(3))) char* lds_ptr;
typedef const __attribute__((address_space(1))) void* gptr_t;

#define DEVI __device__ __forceinline__
#define WAIT_V(n) asm volatile("s_waitcnt vmcnt(" #n ")" ::: "memory")
template <int N>
__device__ __forceinline__ void wait_vm() { asm volatile("s_waitcnt vmcnt(%0)" ::"n"(N) : "memory"); }
template <int I, int N, class F>
__device__ __forceinline__ void static_for(F f) {
    if constexpr (I < N) {
        f(std::integral_constant<int, I>{});
        static_for<I + 1, N>(f);
    }
}
#define WAIT_V0() __builtin_amdgcn_s_waitcnt(0x0070)
#define WAIT_L0() asm volatile("s_waitcnt lgkmcnt(0)" ::: "memory")
#define BAR() __builtin_amdgcn_s_barrier()
#define SB() __builtin_amdgcn_sched_barrier(0)

constexpr int S_ = 2048, D_ = 512, F_ = 1792;
constexpr float EPS = 1e-5f;
constexpr float QSCALE = 0.25f * 1.44269504088896340736f;

constexpr size_t WS_QF = 0;
constexpr size_t WS_KF = WS_QF + (2u << 20);
constexpr size_t WS_VF = WS_KF + (2u << 20);
constexpr size_t WS_OF = WS_VF + (2u << 20);
constexpr size_t WS_HF = WS_OF + (2u << 20);
constexpr size_t WS_MF = WS_HF + (2u << 20);
constexpr size_t WS_WOT = WS_MF + (7u << 20);
constexpr size_t WS_WGU = WS_WOT + (512u << 10);
constexpr size_t WS_WDT = WS_WGU + (3584u << 10);
constexpr size_t WS_ST2 = WS_WDT + (1792u << 10);
constexpr size_t WS_CSP = WS_ST2 + (256u << 10);
constexpr size_t WS_CNT = WS_CSP + (64u << 10);

DEVI void glds16(const void* g, char* smem_base, int lds_off) {
    __builtin_amdgcn_global_load_lds((gptr_t)g, (__attribute__((address_space(3))) void*)((lds_ptr)smem_base + lds_off), 16, 0, 0);
}
DEVI bf16x8 lds_read16(char* smem, int off) { return *(const bf16x8*)(smem + off); }
DEVI bf16x8 lds_read_tr(char* smem, int off0, int off1) {
    s16x4 a = __builtin_amdgcn_ds_read_tr16_b64_v4i16((__attribute__((address_space(3))) s16x4*)((lds_ptr)smem + off0));
    s16x4 b = __builtin_amdgcn_ds_read_tr16_b64_v4i16((__attribute__((address_space(3))) s16x4*)((lds_ptr)smem + off1));
    s16x8 c = __builtin_shufflevector(a, b, 0, 1, 2, 3, 4, 5, 6, 7);
    return __builtin_bit_cast(bf16x8, c);
}
constexpr int K4_XA = 2;
#define K4_RB(xcd, idx) (((xcd) % K4_XA) * (16 / K4_XA) + ((idx) % (16 / K4_XA)))
#define K4_J(xcd, idx) (((xcd) / K4_XA) * (2 * K4_XA) + ((idx) / (16 / K4_XA)))
constexpr int K1_XA = 4;
#define K1_RB(xcd, idx) (((xcd) % K1_XA) * (16 / K1_XA) + ((idx) % (16 / K1_XA)))
#define K1_CT(xcd, idx) (((xcd) / K1_XA) * (2 * K1_XA) + ((idx) / (16 / K1_XA)))
DEVI void st16(void* p, u32x4 v) {
    asm volatile("global_store_dwordx4 %0, %1, off sc1\n\ts_nop 1" ::"v"(p), "v"(v) : "memory");
}
typedef __attribute__((address_space(1))) unsigned* gcnt_t;
DEVI gcnt_t counter(char* ws, int edge, int rb) { return (gcnt_t)(ws + WS_CNT + (size_t)(edge * 16 + rb) * 256); }
DEVI void arrive(gcnt_t c) { __hip_atomic_fetch_add(c, 1u, __ATOMIC_RELAXED, __HIP_MEMORY_SCOPE_AGENT); }
DEVI void await(gcnt_t c, unsigned n) {
    for (int spin = 0; spin < (1 << 16); ++spin) {
        if (__hip_atomic_load(c, __ATOMIC_RELAXED, __HIP_MEMORY_SCOPE_AGENT) >= n) break;
        __builtin_amdgcn_s_sleep(1);
    }
}
DEVI bf16x8 load_dev(__amdgpu_buffer_rsrc_t rs, int voff, int soff) {
    return __builtin_bit_cast(bf16x8, __builtin_amdgcn_raw_buffer_load_b128(rs, voff, soff, 16));
}
DEVI unsigned pack2(float a, float b) { return __builtin_bit_cast(unsigned, __builtin_convertvector(f32x2{a, b}, bf16x2)); }
DEVI float bf_lo(unsigned u) { return __builtin_bit_cast(float, u << 16); }
DEVI float bf_hi(unsigned u) { return __builtin_bit_cast(float, u & 0xffff0000u); }
DEVI float swap32_add(float v) {
    unsigned a = __builtin_bit_cast(unsigned, v), b = a;
    asm volatile("" : "+v"(b));
    auto r = __builtin_amdgcn_permlane32_swap(a, b, false, false);
    const unsigned r0 = r[0], r1 = r[1];
    return __builtin_bit_cast(float, r0) + __builtin_bit_cast(float, r1);
}
DEVI void swap32(unsigned& a, unsigned& b) {
    auto r = __builtin_amdgcn_permlane32_swap(a, b, false, false);
    const unsigned r0 = r[0], r1 = r[1];
    a = r0;
    b = r1;
}
DEVI float swap32_max(float v) {
    unsigned a = __builtin_bit_cast(unsigned, v), b = a;
    asm volatile("" : "+v"(b));
    auto r = __builtin_amdgcn_permlane32_swap(a, b, false, false);
    const unsigned r0 = r[0], r1 = r[1];
    return fmaxf(__builtin_bit_cast(float, r0), __builtin_bit_cast(float, r1));
}
DEVI f32x4 mfma16(bf16x8 a, bf16x8 b, f32x4 c) { return __builtin_amdgcn_mfma_f32_16x16x32_bf16(a, b, c, 0, 0, 0); }
DEVI f32x16 mfma32(bf16x8 a, bf16x8 b, f32x16 c) { return __builtin_amdgcn_mfma_f32_32x32x16_bf16(a, b, c, 0, 0, 0); }
DEVI bf16x8 ones8() {
    u32x4 o = {0x3F803F80u, 0x3F803F80u, 0x3F803F80u, 0x3F803F80u};
    return __builtin_bit_cast(bf16x8, o);
}

constexpr int K1_SLOT = 28672, K1_XB = 16384, K1_RING = 5;
constexpr int K1_STAT = K1_SLOT * K1_RING;
constexpr int K1_CSP = K1_STAT + 1024;
constexpr int K1_CS = K1_CSP + 1536;
constexpr int K1_JOB = K1_CS + 384, K1_LDS = K1_JOB + 2048;

DEVI void qkv_loader(const uint16_t* __restrict__ x, const uint16_t* __restrict__ wqkv, const uint16_t* __restrict__ wo, char* __restrict__ ws,
                     char* smem, int rb, int ct, int w, int lane) {
    const char* xsrc[4];
#pragma unroll
    for (int i = 0; i < 4; ++i) {
        const int r = 32 * w + 8 * i + (lane >> 3);
        const int c = (lane & 7) ^ ((r >> 1) & 7);
        xsrc[i] = (const char*)x + (size_t)(rb * 128 + r) * 1024 + c * 16;
    }
    const char* wsrc[3];
#pragma unroll
    for (int i = 0; i < 3; ++i) {
        const int ci = 64 * (3 * w + i) + lane;
        const int r = ci / 12, cp = ci % 12;
        const int c = cp ^ (((r >> 3) & 1) << 1);
        wsrc[i] = (const char*)wqkv + (size_t)r * 3072 + ct * 192 + c * 16;
    }
    auto issue = [&](int c) {
        const int slot = (c % K1_RING) * K1_SLOT;
#pragma unroll
        for (int i = 0; i < 4; ++i) glds16(xsrc[i] + c * 128, smem, slot + (32 * w + 8 * i) * 128);
#pragma unroll
        for (int i = 0; i < 3; ++i) glds16(wsrc[i] + (size_t)c * 64 * 3072, smem, slot + K1_XB + (3 * w + i) * 1024);
    };
    const int m = lane & 15, g = lane >> 4, wq = (lane >> 2) & 3, wp = lane & 3;
    int woff[3];
#pragma unroll
    for (int nt = 0; nt < 3; ++nt)
        woff[nt] = K1_XB + ((w >> 1) * 32 + 8 * g + wq) * 192 + wp * 8 + (((3 * (w & 1) + nt) ^ (g & 1)) << 5);
    const bf16x8 ones = ones8();
    f32x4 cs[3];
#pragma unroll
    for (int nt = 0; nt < 3; ++nt) cs[nt] = f32x4{0, 0, 0, 0};
    const int jfp = blockIdx.x & 7, js = blockIdx.x >> 3;
#pragma unroll
    for (int c = 0; c < K1_RING; ++c) issue(c);
    if (w == 0) {
#pragma unroll
        for (int n = 0; n < 2; ++n) glds16((const char*)wo + (size_t)(16 * js + 8 * n + (lane >> 3)) * (D_ * 2) + jfp * 128 + (lane & 7) * 16, smem, K1_JOB + n * 1024);
    }
#pragma unroll
    for (int c = 0; c < 8; ++c) {
        if (c == 0) WAIT_V(28);
        else if (c <= 4) WAIT_V(21);
        else if (c == 5) WAIT_V(14);
        else if (c == 6) WAIT_V(7);
        else WAIT_V(0);
        WAIT_L0();
        BAR();
        const int slot = (c % K1_RING) * K1_SLOT;
        bf16x8 wf[3];
#pragma unroll
        for (int nt = 0; nt < 3; ++nt) wf[nt] = lds_read_tr(smem, slot + woff[nt], slot + woff[nt] + 4 * 192);
        SB();
        if (c >= 1 && c - 1 + K1_RING < 8) issue(c - 1 + K1_RING);
        SB();
#pragma unroll
        for (int nt = 0; nt < 3; ++nt) cs[nt] = mfma16(wf[nt], ones, cs[nt]);
        SB();
    }
    if (m == 0) {
#pragma unroll
        for (int nt = 0; nt < 3; ++nt) *(f32x4*)(smem + K1_CSP + ((w >> 1) * 96 + (3 * (w & 1) + nt) * 16 + 4 * g) * 4) = cs[nt];
    }
    WAIT_L0();
    BAR();
    if (w < 2) {
        const int t = w * 64 + lane;
        const float* csp = (const float*)(smem + K1_CSP);
        if (t < 96) ((float*)(smem + K1_CS))[t] = csp[t] + csp[96 + t];
    }
    WAIT_L0();
    BAR();
    if (w == 0) {
        const int G = lane >> 4, nh = G & 1, hh = G >> 1, rq = (lane >> 2) & 3, pp = lane & 3;
#pragma unroll
        for (int t = 0; t < 2; ++t) {
            const int o = K1_JOB + (4 * hh + rq) * 128 + t * 64 + nh * 32 + pp * 8;
            st16(ws + WS_WOT + (size_t)(jfp * 32 + js) * 2048 + t * 1024 + lane * 16, __builtin_bit_cast(u32x4, lds_read_tr(smem, o, o + 8 * 128)));
        }
    }
}

template <int NV>
DEVI void qkv_body(char* __restrict__ ws, char* smem, int rb, int ct, int w, int lane) {
    const int m = lane & 15, g = lane >> 4;
    const int wr = w & 1, wc = w >> 1;
    int xoff[4][2];
#pragma unroll
    for (int tt = 0; tt < 4; ++tt)
#pragma unroll
        for (int s = 0; s < 2; ++s) {
            const int r = 64 * wr + 16 * (tt ^ (2 * wc)) + m;
            xoff[tt][s] = r * 128 + (((4 * s + g) ^ ((r >> 1) & 7)) << 4);
        }
    const int wq = (lane >> 2) & 3, wp = lane & 3;
    int woff[3];
#pragma unroll
    for (int nt = 0; nt < 3; ++nt) woff[nt] = K1_XB + (8 * g + wq) * 192 + wp * 8 + (((3 * wc + nt) ^ (g & 1)) << 5);
    f32x4 acc[4][3], ssum[2], ssq[2];
#pragma unroll
    for (int tt = 0; tt < 4; ++tt)
#pragma unroll
        for (int nt = 0; nt < 3; ++nt) acc[tt][nt] = f32x4{0, 0, 0, 0};
#pragma unroll
    for (int tt = 0; tt < 2; ++tt) {
        ssum[tt] = f32x4{0, 0, 0, 0};
        ssq[tt] = f32x4{0, 0, 0, 0};
    }
    const bf16x8 ones = ones8();
    bf16x8 xf[2][4], wf[2][3];
    auto rd = [&](int ks, int n) {
        const int slot = ((ks >> 1) % K1_RING) * K1_SLOT;
        if (n == 0 || (n >= 2 && n <= 4)) {
            const int tt = n == 0 ? 0 : n - 1;
            xf[ks & 1][tt] = lds_read16(smem, slot + xoff[tt][ks & 1]);
        } else {
            const int nt = n == 1 ? 0 : n - 4;
            const int o = slot + woff[nt] + (ks & 1) * 32 * 192;
            wf[ks & 1][nt] = lds_read_tr(smem, o, o + 4 * 192);
        }
    };
    BAR();
#pragma unroll
    for (int n = 0; n < 7; ++n) rd(0, n);
    SB();
#pragma unroll
    for (int ks = 0; ks < 16; ++ks) {
        const int b = ks & 1;
        const int first = b ? 6 : 0;
#pragma unroll
        for (int n = 0; n < 16; ++n) {
            if (b && ks < 15 && n == first) {
                WAIT_L0();
                BAR();
                SB();
            }
            if (n < 12) {
                const int nt = n >> 2, tt = n & 3;
                acc[tt][nt] = (nt >= 3 - NV) ? mfma16(xf[b][tt], wf[b][nt], acc[tt][nt]) : mfma16(wf[b][nt], xf[b][tt], acc[tt][nt]);
            } else if (n < 14) {
                ssum[n - 12] = mfma16(ones, xf[b][n - 12], ssum[n - 12]);
            } else {
                ssq[n - 14] = mfma16(xf[b][n - 14], xf[b][n - 14], ssq[n - 14]);
            }
            if (ks < 15 && n >= first && n < first + 7) rd(ks + 1, n - first);
            SB();
        }
    }
    f32x2* stat = (f32x2*)(smem + K1_STAT);
    const float* csf = (const float*)(smem + K1_CS);
#pragma unroll
    for (int tt = 0; tt < 2; ++tt) {
        const int r = m & 3;
        const float sq = r == 0 ? ssq[tt][0] : r == 1 ? ssq[tt][1] : r == 2 ? ssq[tt][2] : ssq[tt][3];
        const float mean = ssum[tt][0] * (1.0f / D_);
        const float var = fmaxf(sq * (1.0f / D_) - mean * mean, 0.0f);
        if ((m >> 2) == g) stat[64 * wr + 32 * wc + 16 * tt + m] = f32x2{mean, __builtin_amdgcn_rsqf(var + EPS)};
    }
    WAIT_L0();
    BAR();
    BAR();
#pragma unroll
    for (int nt = 0; nt < 3; ++nt) {
        const int ntl = 3 * wc + nt;
        const int col = ct * 96 + ntl * 16;
        const int head = (col & 511) >> 4;
        const bool isv = nt >= 3 - NV;
        const int isk = col >= 512;
        const float sc = (!isv && !isk) ? QSCALE : 1.0f;
        const f32x4 c4 = *(const f32x4*)(csf + ntl * 16 + 4 * g);
        const float c1 = csf[ntl * 16 + m];
        char* base = ws + (isv ? WS_VF : isk ? WS_KF : WS_QF) + head * 1024 +
                     (isv ? lane * 16 : ((lane & 15) + 16 * (lane >> 5) + 32 * ((lane >> 4) & 1)) * 16);
#pragma unroll
        for (int pr = 0; pr < 2; ++pr) {
            unsigned pk[2][2];
#pragma unroll
            for (int u = 0; u < 2; ++u) {
                const int tt = 2 * pr + u;
                const int tok0 = 64 * wr + 16 * (tt ^ (2 * wc));
                float v[4];
                if (!isv) {
                    const f32x2 st = stat[tok0 + m];
                    const float rs = st[1] * sc;
#pragma unroll
                    for (int r = 0; r < 4; ++r) v[r] = (acc[tt][nt][r] - st[0] * c4[r]) * rs;
                } else {
#pragma unroll
                    for (int r = 0; r < 4; ++r) {
                        const f32x2 st = stat[tok0 + 4 * g + r];
                        v[r] = (acc[tt][nt][r] - st[0] * c1) * st[1];
                    }
                }
                pk[u][0] = pack2(v[0], v[1]);
                pk[u][1] = pack2(v[2], v[3]);
            }
            swap32(pk[0][0], pk[1][0]);
            swap32(pk[0][1], pk[1][1]);
            const int tile = rb * 4 + 2 * wr + (pr ^ wc);
            st16(base + (size_t)tile * 32768, u32x4{pk[0][0], pk[0][1], pk[1][0], pk[1][1]});
        }
    }
}

__global__ __launch_bounds__(512, 2) void k1_qkv(const uint16_t* __restrict__ x, const uint16_t* __restrict__ wqkv, const uint16_t* __restrict__ wo,
                                                 char* __restrict__ ws) {
    extern __shared__ __attribute__((aligned(16))) char smem[];
    const int lane = threadIdx.x & 63, wv = __builtin_amdgcn_readfirstlane(threadIdx.x >> 6), w = wv & 3;
    const int xcd = blockIdx.x & 7, idx = blockIdx.x >> 3;
    const int rb = K1_RB(xcd, idx), ct = K1_CT(xcd, idx);
    asm volatile(".Lcode_begin:");
    if (wv >= 4) {
        qkv_loader(x, wqkv, wo, ws, smem, rb, ct, w, lane);
        return;
    }
    if (blockIdx.x < 32 && threadIdx.x == 0) *(unsigned*)(ws + WS_CNT + (size_t)blockIdx.x * 256) = 0u;
    unsigned warm = 0;
    if (idx < 4) {
        uint64_t pc;
        int len;
        int skip;
        asm volatile("s_getpc_b64 %0\n.Lcode_pc:\n\ts_mov_b32 %1, .Lcode_end-.Lcode_begin\n\ts_mov_b32 %2, .Lcode_begin-.Lcode_pc" : "=s"(pc), "=s"(len), "=s"(skip));
        pc += skip;
        const int off = min((idx * 256 + (int)threadIdx.x) * 64, len - 4);
        warm = *(const __attribute__((address_space(1))) unsigned*)(pc + off);
    }
    if (ct >= 11) qkv_body<3>(ws, smem, rb, ct, w, lane);
    else if (ct == 10 && w >= 2) qkv_body<2>(ws, smem, rb, ct, w, lane);
    else qkv_body<0>(ws, smem, rb, ct, w, lane);
    WAIT_V(0);
    asm volatile("" ::"v"(warm));
}

constexpr int K2_VS = 2064;
constexpr int K2_K = 0, K2_V = 24576, K2_Q = K2_V + 12 * K2_VS + 192, K2_JOB = K2_Q + 8192, K2_RED = K2_JOB + 32768;
constexpr int K2_LDS = K2_RED + 8 * 128 * 4;

DEVI f32x16 splat16(float v) {
    f32x16 r;
#pragma unroll
    for (int i = 0; i < 16; ++i) r[i] = v;
    return r;
}
constexpr unsigned long long row_mask(int r) {
    unsigned long long m = 0;
    for (int l = 0; l < 64; ++l)
        if ((r & 3) + 8 * (r >> 2) + 4 * (l >> 5) > (l & 31)) m |= 1ull << l;
    return m;
}
template <int R>
DEVI unsigned sel_row(unsigned no, unsigned yes) { return __builtin_amdgcn_inverse_ballot_w64(row_mask(R)) ? yes : no; }
template <int R>
DEVI void merge_rows(const f32x16& sf, const f32x16& sl, f32x16& mg) {
    if constexpr (R < 16) {
        const float no = sl[R], yes = sf[R];
        mg[R] = __builtin_amdgcn_inverse_ballot_w64(row_mask(R)) ? yes : no;
        merge_rows<R + 1>(sf, sl, mg);
    }
}
DEVI f32x16 attn_merge(const f32x16& sf, const f32x16& sl) {
    f32x16 mg;
    merge_rows<0>(sf, sl, mg);
    return mg;
}
template <int R>
DEVI void split_rows(const f32x16& mg, float mx, unsigned (&pf)[8], unsigned (&pl)[8]) {
    if constexpr (R < 16) {
        const unsigned pk = pack2(__builtin_amdgcn_exp2f(mg[R] - mx), __builtin_amdgcn_exp2f(mg[R + 1] - mx));
        const unsigned mk = sel_row<R>(sel_row<R + 1>(0u, 0xffff0000u), 0xffffffffu);
        pf[R >> 1] = pk & mk;
        pl[R >> 1] = pk & ~mk;
        split_rows<R + 2>(mg, mx, pf, pl);
    }
}
DEVI void attn_split(const f32x16& mg, float mx, unsigned (&pf)[8], unsigned (&pl)[8]) { split_rows<0>(mg, mx, pf, pl); }
DEVI bf16x8 as_frag(const unsigned* p) { return __builtin_bit_cast(bf16x8, u32x4{p[0], p[1], p[2], p[3]}); }

template <bool FULL>
DEVI f32x16 attn_exact(char* smem, bf16x8 q, int koff, int vb0, int vb1, int i, int umin) {
    const float NEG = -__builtin_inff();
    const f32x16 zero = {0};
    const f32x16 sl = mfma32(lds_read16(smem, koff + 8 * 2048), q, zero);
    f32x16 sf = splat16(NEG);
    if (FULL || i >= umin) sf = mfma32(lds_read16(smem, koff), q, zero);
    const f32x16 mg = attn_merge(sf, sl);
    float mx = fmaxf(mg[0], mg[1]);
#pragma unroll
    for (int r = 2; r < 16; r += 2) mx = fmaxf(fmaxf(mx, mg[r]), mg[r + 1]);
#pragma unroll 1
    for (int t = 1; t < 8; ++t) {
        if (FULL || i + t >= umin) {
            const f32x16 s = mfma32(lds_read16(smem, koff + t * 2048), q, zero);
#pragma unroll
            for (int r = 0; r < 16; r += 2) mx = fmaxf(fmaxf(mx, s[r]), s[r + 1]);
        }
    }
    mx = swap32_max(mx);
    const f32x16 negm = splat16(-mx);
    f32x16 o = zero;
    unsigned pf[8], pl[8];
    attn_split(mg, mx, pf, pl);
#pragma unroll
    for (int s = 0; s < 2; ++s) {
        o = mfma32(lds_read16(smem, (s ? vb1 : vb0) + 8 * K2_VS), as_frag(pl + 4 * s), o);
        if (FULL || i >= umin) o = mfma32(lds_read16(smem, s ? vb1 : vb0), as_frag(pf + 4 * s), o);
    }
#pragma unroll 1
    for (int t = 1; t < 8; ++t) {
        if (FULL || i + t >= umin) {
            const f32x16 s = mfma32(lds_read16(smem, koff + t * 2048), q, negm);
            unsigned pk[8];
#pragma unroll
            for (int r = 0; r < 16; r += 2) pk[r >> 1] = pack2(__builtin_amdgcn_exp2f(s[r]), __builtin_amdgcn_exp2f(s[r + 1]));
            o = mfma32(lds_read16(smem, vb0 + t * K2_VS), as_frag(pk), o);
            o = mfma32(lds_read16(smem, vb1 + t * K2_VS), as_frag(pk + 4), o);
        }
    }
    return o;
}

template <bool FULL>
DEVI f32x16 attn_fast(char* smem, bf16x8 q, int koff, int vb0, int vb1, int i, int umin) {
    const float NEG = -__builtin_inff();
    const f32x16 zero = {0};
    auto cinit = [&](int t) { return FULL ? zero : splat16(i + t >= umin ? 0.0f : NEG); };
    bf16x8 kf[2], vf[2][2];
    const bf16x8 kd = lds_read16(smem, koff + 8 * 2048), k0 = lds_read16(smem, koff);
    kf[1] = lds_read16(smem, koff + 2048);
    const bf16x8 vd0 = lds_read16(smem, vb0 + 8 * K2_VS), vz0 = lds_read16(smem, vb0);
    const bf16x8 vd1 = lds_read16(smem, vb1 + 8 * K2_VS), vz1 = lds_read16(smem, vb1);
    const f32x16 sl = mfma32(kd, q, zero);
    const f32x16 sf = mfma32(k0, q, cinit(0));
    f32x16 sc[2];
    sc[1] = mfma32(kf[1], q, cinit(1));
    kf[0] = lds_read16(smem, koff + 2 * 2048);
    vf[1][0] = lds_read16(smem, vb0 + K2_VS);
    vf[1][1] = lds_read16(smem, vb1 + K2_VS);
    SB();
    f32x16 o = zero;
    {
        unsigned pf[8], pl[8];
        attn_split(attn_merge(sf, sl), 0.0f, pf, pl);
        o = mfma32(vd0, as_frag(pl), o);
        o = mfma32(vz0, as_frag(pf), o);
        o = mfma32(vd1, as_frag(pl + 4), o);
        o = mfma32(vz1, as_frag(pf + 4), o);
    }
    SB();
#pragma unroll
    for (int t = 1; t < 8; ++t) {
        const int b = t & 1;
        if (t < 7) {
            sc[b ^ 1] = mfma32(kf[b ^ 1], q, cinit(t + 1));
            if (t < 6) kf[b] = lds_read16(smem, koff + (t + 2) * 2048);
            vf[b ^ 1][0] = lds_read16(smem, vb0 + (t + 1) * K2_VS);
            vf[b ^ 1][1] = lds_read16(smem, vb1 + (t + 1) * K2_VS);
        }
        SB();
        unsigned pk[8];
#pragma unroll
        for (int r = 0; r < 8; r += 2) pk[r >> 1] = pack2(__builtin_amdgcn_exp2f(sc[b][r]), __builtin_amdgcn_exp2f(sc[b][r + 1]));
        const float e8 = __builtin_amdgcn_exp2f(sc[b][8]), e9 = __builtin_amdgcn_exp2f(sc[b][9]);
        SB();
        o = mfma32(vf[b][0], as_frag(pk), o);
        SB();
        pk[4] = pack2(e8, e9);
#pragma unroll
        for (int r = 10; r < 16; r += 2) pk[r >> 1] = pack2(__builtin_amdgcn_exp2f(sc[b][r]), __builtin_amdgcn_exp2f(sc[b][r + 1]));
        SB();
        o = mfma32(vf[b][1], as_frag(pk + 4), o);
        SB();
    }
    return o;
}

template <bool FULL>
DEVI u32x4 attn_task(char* smem, bf16x8 q, int rb, int i, int e, int lane) {
    const int a = lane & 31, h = lane >> 5;
    const int koff = K2_K + e * 1024 + i * 2048 + lane * 16;
    const bool isdata = a < 16;
    const int vb0 = K2_V + i * K2_VS + (isdata ? e * 1024 + h * 256 + a * 16 : 2048);
    const int vb1 = vb0 + (isdata ? 512 : 0);
    const int umin = FULL ? 0 : 8 - 4 * rb;
    f32x16 o = attn_fast<FULL>(smem, q, koff, vb0, vb1, i, umin);
    float mag = 0.f;
#pragma unroll
    for (int r = 0; r < 8; ++r) mag += __builtin_fabsf(o[r]);
    const bool safe = o[8] >= 0x1p-60f && o[8] <= 0x1p60f && mag <= 0x1p120f;
    if (__builtin_amdgcn_ballot_w64(!safe) != 0) {
        o = attn_exact<FULL>(smem, q, koff, vb0, vb1, i, umin);
    }
    const float inv = __builtin_amdgcn_rcpf(o[8]);
    return u32x4{pack2(o[0] * inv, o[1] * inv), pack2(o[2] * inv, o[3] * inv), pack2(o[4] * inv, o[5] * inv), pack2(o[6] * inv, o[7] * inv)};
}

constexpr int G64_RED = 0, G64_T = 49152;
DEVI void oproj_phase(const uint16_t* __restrict__ x, char* __restrict__ ws, char* smem, int tb, int fp, int w, int lane) {
    constexpr int NS = 8;
    const int tt = w & 1, ft = w >> 1;
    const char* wt = ws + WS_WOT + (size_t)fp * 65536 + (size_t)(w * NS) * 2048 + lane * 16;
    bf16x8 fa[NS][2], fb[NS][2];
#pragma unroll
    for (int s = 0; s < NS; ++s)
#pragma unroll
        for (int q = 0; q < 2; ++q) fa[s][q] = *(const bf16x8*)(wt + s * 2048 + (q ^ (w >> 1)) * 1024);
    u32x4 xr[2];
#pragma unroll
    for (int n = 0; n < 2; ++n) {
        const int r = (w * 2 + n) * 8 + (lane >> 3);
        xr[n] = *(const u32x4*)((const char*)x + (size_t)(tb * 64 + r) * 1024 + fp * 128 + (lane & 7) * 16);
    }
    SB();
    BAR();
    SB();
    const __amdgpu_buffer_rsrc_t rs = __builtin_amdgcn_make_buffer_rsrc(ws + WS_OF, 0, 2 << 20, 0x00020000);
#pragma unroll
    for (int s = 0; s < NS; ++s) {
#pragma unroll
        for (int q = 0; q < 2; ++q) fb[s][q] = load_dev(rs, ((tb * 2 + (q ^ (w & 1))) * 32 + w * NS) * 1024 + lane * 16, s * 1024);
        SB();
    }
    f32x16 acc[4];
#pragma unroll
    for (int q = 0; q < 4; ++q) acc[q] = f32x16{0};
#pragma unroll
    for (int s = 0; s < NS; ++s) {
#pragma unroll
        for (int q = 0; q < 4; ++q) acc[q] = mfma32(fa[s][q >> 1], fb[s][q & 1], acc[q]);
        SB();
    }
#pragma unroll
    for (int n = 0; n < 2; ++n) {
        const int r = (w * 2 + n) * 8 + (lane >> 3);
        *(u32x4*)(smem + G64_T + r * 128 + (((lane & 7) ^ ((r >> 1) & 7)) << 4)) = xr[n];
    }
#pragma unroll
    for (int q = 1; q < 4; ++q) {
        char* dst = smem + G64_RED + ((w ^ q) * 3 + q - 1) * 4096 + lane * 16;
#pragma unroll
        for (int r = 0; r < 4; ++r)
            *(f32x4*)(dst + r * 1024) = f32x4{acc[q][4 * r], acc[q][4 * r + 1], acc[q][4 * r + 2], acc[q][4 * r + 3]};
    }
    WAIT_L0();
    BAR();
    f32x16 o = acc[0];
#pragma unroll
    for (int q = 0; q < 3; ++q) {
        const char* src = smem + G64_RED + (w * 3 + q) * 4096 + lane * 16;
#pragma unroll
        for (int r = 0; r < 4; ++r) {
            const f32x4 v = *(const f32x4*)(src + r * 1024);
#pragma unroll
            for (int k = 0; k < 4; ++k) o[4 * r + k] += v[k];
        }
    }
    const int tok = lane & 31, h = lane >> 5;
    const int r = tt * 32 + tok;
    unsigned pk[8];
    float sum = 0.f, sq = 0.f;
#pragma unroll
    for (int qd = 0; qd < 4; ++qd) {
        const u32x2 xv = *(const u32x2*)(smem + G64_T + r * 128 + (((ft * 4 + qd) ^ ((r >> 1) & 7)) << 4) + h * 8);
        pk[2 * qd] = pack2(o[4 * qd] + bf_lo(xv[0]), o[4 * qd + 1] + bf_hi(xv[0]));
        pk[2 * qd + 1] = pack2(o[4 * qd + 2] + bf_lo(xv[1]), o[4 * qd + 3] + bf_hi(xv[1]));
#pragma unroll
        for (int k = 0; k < 2; ++k) {
            const float lo = bf_lo(pk[2 * qd + k]), hi = bf_hi(pk[2 * qd + k]);
            sum += lo + hi;
            sq += lo * lo + hi * hi;
        }
    }
    char* hdst = ws + WS_HF + (size_t)((tb * 2 + tt) * 32 + (fp * 2 + ft) * 2) * 1024 + lane * 16;
    st16(hdst, u32x4{pk[0], pk[1], pk[2], pk[3]});
    st16(hdst + 1024, u32x4{pk[4], pk[5], pk[6], pk[7]});
    sum = swap32_add(sum);
    sq = swap32_add(sq);
    if (h == 0) ((f32x2*)(ws + WS_ST2))[(fp * 2 + ft) * S_ + tb * 64 + r] = f32x2{sum, sq};
}

__global__ __launch_bounds__(512, 2) void k2_attn(const uint16_t* __restrict__ x, const uint16_t* __restrict__ wg, const uint16_t* __restrict__ wu,
                                                  const uint16_t* __restrict__ wd, char* __restrict__ ws) {
    extern __shared__ __attribute__((aligned(16))) char smem[];
    const int tid = threadIdx.x, lane = tid & 63, w = __builtin_amdgcn_readfirstlane(tid >> 6);
    const int xcd = blockIdx.x & 7, idx = blockIdx.x >> 3;
    const int rb = 2 * xcd + (idx >> 4), p = idx & 15;
    const int i = w & 3, e = w >> 2;
#pragma unroll
    for (int n = 0; n < 6; ++n) {
        const int pc = 6 * w + n;
        const int isv = pc >= 24, u = (pc % 24) >> 1, half = pc & 1;
        int kt = 4 * rb - 8 + u;
        kt = kt < 0 ? 0 : kt;
        const char* src = ws + (isv ? WS_VF : WS_KF) + (size_t)(kt * 16 + p) * 2048 + half * 1024 + lane * 16;
        glds16(src, smem, (isv ? K2_V + u * K2_VS : K2_K + u * 2048) + half * 1024);
    }
    glds16(ws + WS_QF + (size_t)((rb * 4 + i) * 32 + 2 * p + e) * 1024 + lane * 16, smem, K2_Q + w * 1024);
    if (tid < 48) *(unsigned*)(smem + K2_V + (tid >> 2) * K2_VS + 2048 + (tid & 3) * 4) = 0x3F803F80u;
    const int bid = blockIdx.x;
    const int job = K2_JOB + w * 4096;
    if (bid < 112) {
        const int cg = bid % 28, s = 8 * (bid / 28) + w;
#pragma unroll
        for (int n = 0; n < 4; ++n) {
            const uint16_t* src = (n >> 1) ? wu : wg;
            const int row = 16 * s + 8 * (n & 1) + (lane >> 3);
            glds16((const char*)src + (size_t)row * (F_ * 2) + cg * 128 + (lane & 7) * 16, smem, job + n * 1024);
        }
    } else if (bid < 224) {
        const int s = bid - 112;
#pragma unroll
        for (int n = 0; n < 2; ++n) {
            const int row = 16 * s + 8 * n + (lane >> 3);
            glds16((const char*)wd + (size_t)row * (D_ * 2) + w * 128 + (lane & 7) * 16, smem, job + n * 1024);
        }
    }
    if (bid < 112) WAIT_V(4);
    else if (bid < 224) WAIT_V(2);
    else WAIT_V(0);
    WAIT_L0();
    BAR();
    const bf16x8 q = lds_read16(smem, K2_Q + w * 1024 + lane * 16);
    auto retile = [&]() {
        const int G = lane >> 4, nh = G & 1, hh = G >> 1, rq = (lane >> 2) & 3, pp = lane & 3;
        if (bid < 112) {
            const int cg = bid % 28, kq = bid / 28, s = 8 * kq + w;
            float* red = (float*)(smem + K2_RED);
#pragma unroll
            for (int t = 0; t < 4; ++t) {
                const int o = job + nh * 2048 + (4 * hh + rq) * 128 + t * 32 + pp * 8;
                const bf16x8 f = lds_read_tr(smem, o, o + 8 * 128);
                const int T = 4 * cg + t, j = T / 7, t7 = T % 7;
                const u32x4 u = __builtin_bit_cast(u32x4, f);
                st16(ws + WS_WGU + (size_t)((j * 32 + s) * 7 + t7) * 1024 + lane * 16, u);
                float sum = 0.f;
#pragma unroll
                for (int r = 0; r < 4; ++r) sum += bf_lo(u[r]) + bf_hi(u[r]);
                sum = swap32_add(sum);
                if (lane < 32) red[w * 128 + nh * 64 + t * 16 + (lane & 15)] = sum;
            }
        } else if (bid < 224) {
            char* dst = ws + WS_WDT + (size_t)(w * 112 + bid - 112) * 2048 + lane * 16;
#pragma unroll
            for (int t = 0; t < 2; ++t) {
                const int o = job + (4 * hh + rq) * 128 + t * 64 + nh * 32 + pp * 8;
                st16(dst + t * 1024, __builtin_bit_cast(u32x4, lds_read_tr(smem, o, o + 8 * 128)));
            }
        }
        SB();
    };
    const u32x4 ov = rb >= 2 ? attn_task<true>(smem, q, rb, i, e, lane) : attn_task<false>(smem, q, rb, i, e, lane);
    WAIT_V0();
    st16(ws + WS_OF + (size_t)((rb * 4 + i) * 32 + 2 * p + e) * 1024 + lane * 16, ov);
    SB();
    retile();
    if (bid < 112) WAIT_V(4);
    else if (bid < 224) WAIT_V(2);
    else WAIT_V(0);
    WAIT_L0();
    BAR();
    if (tid == 0) arrive(counter(ws, 0, rb));
    if (w >= 4) {
        if (bid < 112 && (w == 5 || w == 6)) {
            const int cg = bid % 28, kq = bid / 28, t = tid - 320;
            const float* red = (const float*)(smem + K2_RED);
            float sum = 0.f;
#pragma unroll
            for (int ww = 0; ww < 8; ++ww) sum += red[ww * 128 + t];
            ((float*)(ws + WS_CSP))[(kq * 2 + (t >> 6)) * F_ + cg * 64 + (t & 63)] = sum;
        }
        if (w == 4) await(counter(ws, 0, rb), 16);
        BAR();
        BAR();
        return;
    }
    oproj_phase(x, ws, smem, 4 * xcd + (idx >> 3), idx & 7, w, lane);
    WAIT_V(0);
}

constexpr int K4_SLOT = 28672, K4_RING = 4, K4_CS = K4_SLOT * 5, K4_LDS = K4_CS + 1024, K5_T = 3 * K4_SLOT;

constexpr int K4_BWIN = 8;
constexpr int K5_WIN = 7;
constexpr int wd_first(int c) {
    constexpr int n[8] = {1, 1, 1, 1, 1, 1, 1, 0};
    int t = 0;
    for (int i = 0; i < c; ++i) t += n[i];
    return t;
}
constexpr int wd_extra(int c) { return 2 * (wd_first(c + 1) - wd_first(c)); }
constexpr int wd_pending(int c) {
    int n = c < K4_RING ? 7 * (K4_RING - 1 - c) : 0;
    for (int b = c < K4_RING ? 0 : c - K4_RING + 1; b < c; ++b) {
        const bool ring = b >= 1 && b - 1 + K4_RING < 8 && b - 1 + K4_RING != c;
        n += (ring ? 7 : 0) + wd_extra(b);
    }
    return n;
}
DEVI int wd_slot(int w) { return (w == 0 ? 4 : w - 1) * K4_SLOT; }
template <int HI>
DEVI void down_phase(char* __restrict__ ws, uint16_t* __restrict__ out, char* smem, int tb, int fp, int w, int lane,
                     const bf16x8 (&fr)[14][2]) {
    constexpr int NS = 14, WIN = HI ? K5_WIN : NS;
    const int w8 = w + 4 * HI;
    const __amdgpu_buffer_rsrc_t rs = __builtin_amdgcn_make_buffer_rsrc(ws + WS_MF, 0, 7 << 20, 0x00020000);
    bf16x8 fb[NS][2];
    int vb[2];
#pragma unroll
    for (int q = 0; q < 2; ++q) vb[q] = ((tb * 2 + (q ^ (w & 1))) * 112 + w8 * NS) * 1024 + lane * 16;
    auto load = [&](int s) {
#pragma unroll
        for (int q = 0; q < 2; ++q) fb[s][q] = load_dev(rs, vb[q], s * 1024);
    };
#pragma unroll
    for (int s = 0; s < WIN; ++s) {
        load(s);
        SB();
    }
    bf16x8 fl[2][2];
    auto rdw = [&](int s) {
#pragma unroll
        for (int q = 0; q < 2; ++q) fl[s & 1][q] = lds_read16(smem, wd_slot(w) + s * 2048 + (q ^ (w >> 1)) * 1024 + lane * 16);
    };
    if (!HI) rdw(0);
    const int tt = w & 1, ft = w >> 1;
    f32x16 acc[4];
#pragma unroll
    for (int q = 0; q < 4; ++q) acc[q] = f32x16{0};
    u32x4 hv;
#pragma unroll
    for (int s = 0; s < NS; ++s) {
        if (!HI && s + 1 < NS) rdw(s + 1);
        SB();
#pragma unroll
        for (int q = 0; q < 4; ++q) acc[q] = mfma32(HI ? fr[s][q >> 1] : fl[s & 1][q >> 1], fb[s][q & 1], acc[q]);
        SB();
        if (WIN + 2 * s < NS) load(WIN + 2 * s);
        if (WIN + 2 * s + 1 < NS) load(WIN + 2 * s + 1);
        if (s == (HI ? (NS - WIN) / 2 + 1 : 0)) hv = *(const u32x4*)(ws + WS_HF + (size_t)((tb * 2 + tt) * 32 + (fp * 2 + ft) * 2 + HI) * 1024 + lane * 16);
        SB();
    }
    auto piece = [&](int sw, int hi, int p) { return wd_slot(sw) + (hi * 7 + p) * 2048; };
    if (HI) BAR();
#pragma unroll
    for (int q = 0; q < 4; ++q)
#pragma unroll
        for (int hh = 0; hh < 2; ++hh) {
            if (q == 0 && hh == HI) continue;
            const int id = q * 2 + hh;
            char* dst = smem + piece(w, HI, id - (id > HI ? 1 : 0)) + lane * 16;
#pragma unroll
            for (int r = 0; r < 2; ++r)
                *(f32x4*)(dst + r * 1024) =
                    f32x4{acc[q][8 * hh + 4 * r], acc[q][8 * hh + 4 * r + 1], acc[q][8 * hh + 4 * r + 2], acc[q][8 * hh + 4 * r + 3]};
        }
    if (!HI) BAR();
    WAIT_L0();
    BAR();
    float o[8];
#pragma unroll
    for (int k = 0; k < 8; ++k) o[k] = acc[0][8 * HI + k];
#pragma unroll
    for (int q = 0; q < 4; ++q)
#pragma unroll
        for (int hi = 0; hi < 2; ++hi) {
            if (q == 0 && hi == HI) continue;
            const int id = q * 2 + HI;
            const char* src = smem + piece(w ^ q, hi, id - (id > hi ? 1 : 0)) + lane * 16;
#pragma unroll
            for (int r = 0; r < 2; ++r) {
                const f32x4 v = *(const f32x4*)(src + r * 1024);
#pragma unroll
                for (int k = 0; k < 4; ++k) o[4 * r + k] += v[k];
            }
        }
    const int tok = lane & 31, h = lane >> 5;
    const int r = tt * 32 + tok;
#pragma unroll
    for (int qd = 0; qd < 2; ++qd) {
        const unsigned a = hv[2 * qd], b = hv[2 * qd + 1];
        const u32x2 v = {pack2(o[4 * qd] + bf_lo(a), o[4 * qd + 1] + bf_hi(a)), pack2(o[4 * qd + 2] + bf_lo(b), o[4 * qd + 3] + bf_hi(b))};
        *(u32x2*)(smem + K5_T + r * 128 + (((ft * 4 + 2 * HI + qd) ^ ((r >> 1) & 7)) << 4) + h * 8) = v;
    }
    WAIT_L0();
    BAR();
    const int rr = w8 * 8 + (lane >> 3), cp = lane & 7;
    const u32x4 v = *(const u32x4*)(smem + K5_T + rr * 128 + cp * 16);
    *(u32x4*)((char*)out + (size_t)(tb * 64 + rr) * 1024 + fp * 128 + ((cp ^ ((rr >> 1) & 7)) << 4)) = v;
    WAIT_V(0);
}

__global__ __launch_bounds__(512, 2) void k4_mlp(char* __restrict__ ws, uint16_t* __restrict__ out) {
    extern __shared__ __attribute__((aligned(16))) char smem[];
    const int tid = threadIdx.x, lane = tid & 63, wv = __builtin_amdgcn_readfirstlane(tid >> 6), w = wv & 3;
    const int xcd = blockIdx.x & 7, idx = blockIdx.x >> 3;
    const int rb = K4_RB(xcd, idx), j = K4_J(xcd, idx);
    const int tile = rb * 4 + w;
    const int tb = 4 * xcd + (idx >> 3), fp = idx & 7;
    if (wv >= 4) {
        const char* asrc = ws + WS_WGU + (size_t)j * (32 * 7168) + w * 7168 + lane * 16;
        auto issue = [&](int c) {
#pragma unroll
            for (int n = 0; n < 7; ++n) glds16(asrc + c * 28672 + n * 1024, smem, (c % K4_RING) * K4_SLOT + w * 7168 + n * 1024);
        };
        const char* wdt = ws + WS_WDT + (size_t)fp * (112 * 2048) + lane * 16;
        bf16x8 fr[14][2];
        auto wd_regs = [&](int s0, int s1) {
#pragma unroll
            for (int s = s0; s < s1; ++s)
#pragma unroll
                for (int q = 0; q < 2; ++q) fr[s][q] = *(const bf16x8*)(wdt + (size_t)((w + 4) * 14 + s) * 2048 + (q ^ (w >> 1)) * 1024);
        };
        auto wd_stage = [&]() {
#pragma unroll
            for (int n = 0; n < 28; ++n) glds16(wdt + (size_t)(w * 14) * 2048 + n * 1024, smem, wd_slot(w) + n * 1024);
        };
#pragma unroll
        for (int c = 0; c < K4_RING; ++c) issue(c);
        static_for<0, 8>([&](auto cc) {
            constexpr int c = decltype(cc)::value;
            if (c >= 5 && w <= c - 5) wait_vm<wd_pending(c) + 28>();
            else wait_vm<wd_pending(c)>();
            BAR();
            if (c >= 1 && c - 1 + K4_RING < 8) issue(c - 1 + K4_RING);
            SB();
            wd_regs(wd_first(c), wd_first(c + 1));
            SB();
            if (c >= 4 && w == c - 4) wd_stage();
        });
        BAR();
        wd_regs(wd_first(8), 14);
        wait_vm<2 * (14 - wd_first(8))>();
        BAR();
        if (w == 0) await(counter(ws, 1, tb >> 1), 16);
        WAIT_V0();
        BAR();
        down_phase<1>(ws, out, smem, tb, fp, w, lane, fr);
        return;
    }
    const int tok = lane & 31, h = lane >> 5;
    const char* bsrc = ws + WS_HF + (size_t)tile * 32768 + lane * 16;
    bf16x8 bq[K4_BWIN];
#pragma unroll
    for (int s = 0; s < K4_BWIN; ++s) bq[s] = *(const bf16x8*)(bsrc + s * 1024);
    SB();
    f32x2 st[8];
#pragma unroll
    for (int n = 0; n < 8; ++n) st[n] = ((const f32x2*)(ws + WS_ST2))[(h * 8 + n) * S_ + tile * 32 + tok];
    {
        const int t = tid < 224 ? tid : 0;
        const int gu = t >= 112, col = j * 112 + (gu ? t - 112 : t);
        float csq[4];
#pragma unroll
        for (int n = 0; n < 4; ++n) csq[n] = ((const float*)(ws + WS_CSP))[(n * 2 + gu) * F_ + col];
        if (tid < 224) ((float*)(smem + K4_CS))[tid] = (csq[0] + csq[1]) + (csq[2] + csq[3]);
    }
    float sum = 0.f, sq = 0.f;
#pragma unroll
    for (int n = 0; n < 8; ++n) {
        sum += st[n][0];
        sq += st[n][1];
    }
    sum = swap32_add(sum);
    sq = swap32_add(sq);
    const float mean = sum * (1.0f / D_);
    const float rstd = __builtin_amdgcn_rsqf(fmaxf(sq * (1.0f / D_) - mean * mean, 0.0f) + EPS);
    f32x16 acc[7];
#pragma unroll
    for (int t = 0; t < 7; ++t) acc[t] = f32x16{0};
    bf16x8 af[2][7];
    auto rdA = [&](int s, int t) {
        af[s & 1][t] = lds_read16(smem, ((s >> 2) % K4_RING) * K4_SLOT + ((s & 3) * 7 + t) * 1024 + lane * 16);
    };
    WAIT_L0();
    BAR();
#pragma unroll
    for (int t = 0; t < 7; ++t) rdA(0, t);
    SB();
#pragma unroll
    for (int s = 0; s < 32; ++s) {
        const int b = s & 1;
        const bool edge = (s & 3) == 3;
#pragma unroll
        for (int t = 0; t < 7; ++t) {
            if (edge && s < 31 && t == 4) {
                WAIT_L0();
                BAR();
                SB();
            }
            acc[t] = mfma32(af[b][t], bq[s % K4_BWIN], acc[t]);
            if (s < 31) {
                if (!edge) {
                    rdA(s + 1, t);
                } else if (t == 4) {
                    rdA(s + 1, 0);
                    rdA(s + 1, 1);
                } else if (t == 5) {
                    rdA(s + 1, 2);
                    rdA(s + 1, 3);
                    rdA(s + 1, 4);
                } else if (t == 6) {
                    rdA(s + 1, 5);
                    rdA(s + 1, 6);
                }
            }
            SB();
        }
        if (s + K4_BWIN < 32) bq[s % K4_BWIN] = *(const bf16x8*)(bsrc + (s + K4_BWIN) * 1024);
        SB();
    }
    const float* cs = (const float*)(smem + K4_CS);
    char* dst = ws + WS_MF + (size_t)(tile * 112 + j * 7) * 1024 + lane * 16;
#pragma unroll
    for (int t = 0; t < 7; ++t) {
        float v[8];
#pragma unroll
        for (int qd = 0; qd < 2; ++qd) {
            const f32x4 cg = *(const f32x4*)(cs + t * 16 + 8 * qd + 4 * h);
            const f32x4 cu = *(const f32x4*)(cs + 112 + t * 16 + 8 * qd + 4 * h);
#pragma unroll
            for (int r = 0; r < 4; ++r) {
                const float gt = (acc[t][4 * qd + r] - mean * cg[r]) * rstd;
                const float up = (acc[t][8 + 4 * qd + r] - mean * cu[r]) * rstd;
                v[4 * qd + r] = fmaxf(gt, 0.0f) * up;
            }
        }
        st16(dst + t * 1024, u32x4{pack2(v[0], v[1]), pack2(v[2], v[3]), pack2(v[4], v[5]), pack2(v[6], v[7])});
    }
    BAR();
    WAIT_V(0);
    BAR();
    if (tid == 0) arrive(counter(ws, 1, rb));
    BAR();
    const bf16x8 none[14][2] = {};
    down_phase<0>(ws, out, smem, tb, fp, w, lane, none);
    asm volatile(".subsection 1\n.Lcode_end:\n.subsection 0");
}

extern "C" void kernel_launch(void* const* d_in, const int* in_sizes, int n_in, void* d_out, int out_size, void* d_ws,
                              size_t ws_size, hipStream_t stream) {
    const uint16_t* x = (const uint16_t*)d_in[0];
    const uint16_t* wqkv = (const uint16_t*)d_in[1];
    const uint16_t* wo = (const uint16_t*)d_in[2];
    const uint16_t* wg = (const uint16_t*)d_in[4];
    const uint16_t* wu = (const uint16_t*)d_in[5];
    const uint16_t* wd = (const uint16_t*)d_in[6];
    char* ws = (char*)d_ws;
    (void)hipFuncSetAttribute((const void*)k1_qkv, hipFuncAttributeMaxDynamicSharedMemorySize, K1_LDS);
    (void)hipFuncSetAttribute((const void*)k2_attn, hipFuncAttributeMaxDynamicSharedMemorySize, K2_LDS);
    (void)hipFuncSetAttribute((const void*)k4_mlp, hipFuncAttributeMaxDynamicSharedMemorySize, K4_LDS);
    k1_qkv<<<256, 512, K1_LDS, stream>>>(x, wqkv, wo, ws);
    k2_attn<<<256, 512, K2_LDS, stream>>>(x, wg, wu, wd, ws);
    k4_mlp<<<256, 512, K4_LDS, stream>>>(ws, (uint16_t*)d_out);
}
```

```cpp
#include <hip/hip_runtime.h>
#include <hip/hip_bf16.h>
#include <stdint.h>
#include <type_traits>

typedef __attribute__((ext_vector_type(8))) __bf16 bf16x8;
typedef __attribute__((ext_vector_type(2))) __bf16 bf16x2;
typedef __attribute__((ext_vector_type(4))) short s16x4;
typedef __attribute__((ext_vector_type(8))) short s16x8;
typedef __attribute__((ext_vector_type(2))) float f32x2;
typedef __attribute__((ext_vector_type(4))) float f32x4;
typedef __attribute__((ext_vector_type(16))) float f32x16;
typedef __attribute__((ext_vector_type(2))) unsigned u32x2;
typedef __attribute__((ext_vector_type(4))) unsigned u32x4;
typedef __attribute__((address_space(3))) char* lds_ptr;
typedef const __attribute__((address_space(1))) void* gptr_t;

#define DEVI __device__ __forceinline__
#define WAIT_V(n) asm volatile("s_waitcnt vmcnt(" #n ")" ::: "memory")
template <int N>
__device__ __forceinline__ void wait_vm() { asm volatile("s_waitcnt vmcnt(%0)" ::"n"(N) : "memory"); }
template <int I, int N, class F>
__device__ __forceinline__ void static_for(F f) {
    if constexpr (I < N) {
        f(std::integral_constant<int, I>{});
        static_for<I + 1, N>(f);
    }
}
#define WAIT_V0() __builtin_amdgcn_s_waitcnt(0x0070)
#define WAIT_L0() asm volatile("s_waitcnt lgkmcnt(0)" ::: "memory")
#define BAR() __builtin_amdgcn_s_barrier()
#define SB() __builtin_amdgcn_sched_barrier(0)

constexpr int S_ = 2048, D_ = 512, F_ = 1792;
constexpr float EPS = 1e-5f;
constexpr float QSCALE = 0.25f * 1.44269504088896340736f;

constexpr size_t WS_QF = 0;
constexpr size_t WS_KF = WS_QF + (2u << 20);
constexpr size_t WS_VF = WS_KF + (2u << 20);
constexpr size_t WS_OF = WS_VF + (2u << 20);
constexpr size_t WS_HF = WS_OF + (2u << 20);
constexpr size_t WS_MF = WS_HF + (2u << 20);
constexpr size_t WS_WOT = WS_MF + (7u << 20);
constexpr size_t WS_WGU = WS_WOT + (512u << 10);
constexpr size_t WS_WDT = WS_WGU + (3584u << 10);
constexpr size_t WS_ST2 = WS_WDT + (1792u << 10);
constexpr size_t WS_CSP = WS_ST2 + (256u << 10);
constexpr size_t WS_CNT = WS_CSP + (64u << 10);

DEVI void glds16(const void* g, char* smem_base, int lds_off) {
    __builtin_amdgcn_global_load_lds((gptr_t)g, (__attribute__((address_space(3))) void*)((lds_ptr)smem_base + lds_off), 16, 0, 0);
}
DEVI bf16x8 lds_read16(char* smem, int off) { return *(const bf16x8*)(smem + off); }
DEVI bf16x8 lds_read_tr(char* smem, int off0, int off1) {
    s16x4 a = __builtin_amdgcn_ds_read_tr16_b64_v4i16((__attribute__((address_space(3))) s16x4*)((lds_ptr)smem + off0));
    s16x4 b = __builtin_amdgcn_ds_read_tr16_b64_v4i16((__attribute__((address_space(3))) s16x4*)((lds_ptr)smem + off1));
    s16x8 c = __builtin_shufflevector(a, b, 0, 1, 2, 3, 4, 5, 6, 7);
    return __builtin_bit_cast(bf16x8, c);
}
constexpr int K4_XA = 2;
#define K4_RB(xcd, idx) (((xcd) % K4_XA) * (16 / K4_XA) + ((idx) % (16 / K4_XA)))
#define K4_J(xcd, idx) (((xcd) / K4_XA) * (2 * K4_XA) + ((idx) / (16 / K4_XA)))
constexpr int K1_XA = 4;
#define K1_RB(xcd, idx) (((xcd) % K1_XA) * (16 / K1_XA) + ((idx) % (16 / K1_XA)))
#define K1_CT(xcd, idx) (((xcd) / K1_XA) * (2 * K1_XA) + ((idx) / (16 / K1_XA)))
DEVI void st16(void* p, u32x4 v) {
    asm volatile("global_store_dwordx4 %0, %1, off sc1\n\ts_nop 1" ::"v"(p), "v"(v) : "memory");
}
typedef __attribute__((address_space(1))) unsigned* gcnt_t;
DEVI gcnt_t counter(char* ws, int edge, int rb) { return (gcnt_t)(ws + WS_CNT + (size_t)(edge * 16 + rb) * 256); }
DEVI void arrive(gcnt_t c) { __hip_atomic_fetch_add(c, 1u, __ATOMIC_RELAXED, __HIP_MEMORY_SCOPE_AGENT); }
DEVI void await(gcnt_t c, unsigned n) {
    for (int spin = 0; spin < (1 << 16); ++spin) {
        if (__hip_atomic_load(c, __ATOMIC_RELAXED, __HIP_MEMORY_SCOPE_AGENT) >= n) break;
        __builtin_amdgcn_s_sleep(1);
    }
}
DEVI bf16x8 load_dev(__amdgpu_buffer_rsrc_t rs, int voff, int soff) {
    return __builtin_bit_cast(bf16x8, __builtin_amdgcn_raw_buffer_load_b128(rs, voff, soff, 16));
}
DEVI unsigned pack2(float a, float b) { return __builtin_bit_cast(unsigned, __builtin_convertvector(f32x2{a, b}, bf16x2)); }
DEVI float bf_lo(unsigned u) { return __builtin_bit_cast(float, u << 16); }
DEVI float bf_hi(unsigned u) { return __builtin_bit_cast(float, u & 0xffff0000u); }
DEVI float swap32_add(float v) {
    unsigned a = __builtin_bit_cast(unsigned, v), b = a;
    asm volatile("" : "+v"(b));
    auto r = __builtin_amdgcn_permlane32_swap(a, b, false, false);
    const unsigned r0 = r[0], r1 = r[1];
    return __builtin_bit_cast(float, r0) + __builtin_bit_cast(float, r1);
}
DEVI void swap32(unsigned& a, unsigned& b) {
    auto r = __builtin_amdgcn_permlane32_swap(a, b, false, false);
    const unsigned r0 = r[0], r1 = r[1];
    a = r0;
    b = r1;
}
DEVI float swap32_max(float v) {
    unsigned a = __builtin_bit_cast(unsigned, v), b = a;
    asm volatile("" : "+v"(b));
    auto r = __builtin_amdgcn_permlane32_swap(a, b, false, false);
    const unsigned r0 = r[0], r1 = r[1];
    return fmaxf(__builtin_bit_cast(float, r0), __builtin_bit_cast(float, r1));
}
DEVI f32x4 mfma16(bf16x8 a, bf16x8 b, f32x4 c) { return __builtin_amdgcn_mfma_f32_16x16x32_bf16(a, b, c, 0, 0, 0); }
DEVI f32x16 mfma32(bf16x8 a, bf16x8 b, f32x16 c) { return __builtin_amdgcn_mfma_f32_32x32x16_bf16(a, b, c, 0, 0, 0); }
DEVI bf16x8 ones8() {
    u32x4 o = {0x3F803F80u, 0x3F803F80u, 0x3F803F80u, 0x3F803F80u};
    return __builtin_bit_cast(bf16x8, o);
}

constexpr int K1_SLOT = 28672, K1_XB = 16384, K1_RING = 5;
constexpr int K1_STAT = K1_SLOT * K1_RING;
constexpr int K1_CSP = K1_STAT + 1024;
constexpr int K1_CS = K1_CSP + 1536;
constexpr int K1_JOB = K1_CS + 384, K1_LDS = K1_JOB + 2048;

DEVI void qkv_loader(const uint16_t* __restrict__ x, const uint16_t* __restrict__ wqkv, const uint16_t* __restrict__ wo, char* __restrict__ ws,
                     char* smem, int rb, int ct, int w, int lane) {
    const char* xsrc[4];
#pragma unroll
    for (int i = 0; i < 4; ++i) {
        const int r = 32 * w + 8 * i + (lane >> 3);
        const int c = (lane & 7) ^ ((r >> 1) & 7);
        xsrc[i] = (const char*)x + (size_t)(rb * 128 + r) * 1024 + c * 16;
    }
    const char* wsrc[3];
#pragma unroll
    for (int i = 0; i < 3; ++i) {
        const int ci = 64 * (3 * w + i) + lane;
        const int r = ci / 12, cp = ci % 12;
        const int c = cp ^ (((r >> 3) & 1) << 1);
        wsrc[i] = (const char*)wqkv + (size_t)r * 3072 + ct * 192 + c * 16;
    }
    auto issue = [&](int c) {
        const int slot = (c % K1_RING) * K1_SLOT;
#pragma unroll
        for (int i = 0; i < 4; ++i) glds16(xsrc[i] + c * 128, smem, slot + (32 * w + 8 * i) * 128);
#pragma unroll
        for (int i = 0; i < 3; ++i) glds16(wsrc[i] + (size_t)c * 64 * 3072, smem, slot + K1_XB + (3 * w + i) * 1024);
    };
    const int m = lane & 15, g = lane >> 4, wq = (lane >> 2) & 3, wp = lane & 3;
    int woff[3];
#pragma unroll
    for (int nt = 0; nt < 3; ++nt)
        woff[nt] = K1_XB + ((w >> 1) * 32 + 8 * g + wq) * 192 + wp * 8 + (((3 * (w & 1) + nt) ^ (g & 1)) << 5);
    const bf16x8 ones = ones8();
    f32x4 cs[3];
#pragma unroll
    for (int nt = 0; nt < 3; ++nt) cs[nt] = f32x4{0, 0, 0, 0};
    const int jfp = blockIdx.x & 7, js = blockIdx.x >> 3;
#pragma unroll
    for (int c = 0; c < K1_RING; ++c) issue(c);
    if (w == 0) {
#pragma unroll
        for (int n = 0; n < 2; ++n) glds16((const char*)wo + (size_t)(16 * js + 8 * n + (lane >> 3)) * (D_ * 2) + jfp * 128 + (lane & 7) * 16, smem, K1_JOB + n * 1024);
    }
#pragma unroll
    for (int c = 0; c < 8; ++c) {
        if (c == 0) WAIT_V(28);
        else if (c <= 4) WAIT_V(21);
        else if (c == 5) WAIT_V(14);
        else if (c == 6) WAIT_V(7);
        else WAIT_V(0);
        WAIT_L0();
        BAR();
        const int slot = (c % K1_RING) * K1_SLOT;
        bf16x8 wf[3];
#pragma unroll
        for (int nt = 0; nt < 3; ++nt) wf[nt] = lds_read_tr(smem, slot + woff[nt], slot + woff[nt] + 4 * 192);
        SB();
        if (c >= 1 && c - 1 + K1_RING < 8) issue(c - 1 + K1_RING);
        SB();
#pragma unroll
        for (int nt = 0; nt < 3; ++nt) cs[nt] = mfma16(wf[nt], ones, cs[nt]);
        SB();
    }
    if (m == 0) {
#pragma unroll
        for (int nt = 0; nt < 3; ++nt) *(f32x4*)(smem + K1_CSP + ((w >> 1) * 96 + (3 * (w & 1) + nt) * 16 + 4 * g) * 4) = cs[nt];
    }
    WAIT_L0();
    BAR();
    if (w < 2) {
        const int t = w * 64 + lane;
        const float* csp = (const float*)(smem + K1_CSP);
        if (t < 96) ((float*)(smem + K1_CS))[t] = csp[t] + csp[96 + t];
    }
    WAIT_L0();
    BAR();
    if (w == 0) {
        const int G = lane >> 4, nh = G & 1, hh = G >> 1, rq = (lane >> 2) & 3, pp = lane & 3;
#pragma unroll
        for (int t = 0; t < 2; ++t) {
            const int o = K1_JOB + (4 * hh + rq) * 128 + t * 64 + nh * 32 + pp * 8;
            st16(ws + WS_WOT + (size_t)(jfp * 32 + js) * 2048 + t * 1024 + lane * 16, __builtin_bit_cast(u32x4, lds_read_tr(smem, o, o + 8 * 128)));
        }
    }
}

template <int NV>
DEVI void qkv_body(char* __restrict__ ws, char* smem, int rb, int ct, int w, int lane) {
    const int m = lane & 15, g = lane >> 4;
    const int wr = w & 1, wc = w >> 1;
    int xoff[4][2];
#pragma unroll
    for (int tt = 0; tt < 4; ++tt)
#pragma unroll
        for (int s = 0; s < 2; ++s) {
            const int r = 64 * wr + 16 * (tt ^ (2 * wc)) + m;
            xoff[tt][s] = r * 128 + (((4 * s + g) ^ ((r >> 1) & 7)) << 4);
        }
    const int wq = (lane >> 2) & 3, wp = lane & 3;
    int woff[3];
#pragma unroll
    for (int nt = 0; nt < 3; ++nt) woff[nt] = K1_XB + (8 * g + wq) * 192 + wp * 8 + (((3 * wc + nt) ^ (g & 1)) << 5);
    f32x4 acc[4][3], ssum[2], ssq[2];
#pragma unroll
    for (int tt = 0; tt < 4; ++tt)
#pragma unroll
        for (int nt = 0; nt < 3; ++nt) acc[tt][nt] = f32x4{0, 0, 0, 0};
#pragma unroll
    for (int tt = 0; tt < 2; ++tt) {
        ssum[tt] = f32x4{0, 0, 0, 0};
        ssq[tt] = f32x4{0, 0, 0, 0};
    }
    const bf16x8 ones = ones8();
    bf16x8 xf[2][4], wf[2][3];
    auto rd = [&](int ks, int n) {
        const int slot = ((ks >> 1) % K1_RING) * K1_SLOT;
        if (n == 0 || (n >= 2 && n <= 4)) {
            const int tt = n == 0 ? 0 : n - 1;
            xf[ks & 1][tt] = lds_read16(smem, slot + xoff[tt][ks & 1]);
        } else {
            const int nt = n == 1 ? 0 : n - 4;
            const int o = slot + woff[nt] + (ks & 1) * 32 * 192;
            wf[ks & 1][nt] = lds_read_tr(smem, o, o + 4 * 192);
        }
    };
    BAR();
#pragma unroll
    for (int n = 0; n < 7; ++n) rd(0, n);
    SB();
#pragma unroll
    for (int ks = 0; ks < 16; ++ks) {
        const int b = ks & 1;
        const int first = b ? 6 : 0;
#pragma unroll
        for (int n = 0; n < 16; ++n) {
            if (b && ks < 15 && n == first) {
                WAIT_L0();
                BAR();
                SB();
            }
            if (n < 12) {
                const int nt = n >> 2, tt = n & 3;
                acc[tt][nt] = (nt >= 3 - NV) ? mfma16(xf[b][tt], wf[b][nt], acc[tt][nt]) : mfma16(wf[b][nt], xf[b][tt], acc[tt][nt]);
            } else if (n < 14) {
                ssum[n - 12] = mfma16(ones, xf[b][n - 12], ssum[n - 12]);
            } else {
                ssq[n - 14] = mfma16(xf[b][n - 14], xf[b][n - 14], ssq[n - 14]);
            }
            if (ks < 15 && n >= first && n < first + 7) rd(ks + 1, n - first);
            SB();
        }
    }
    f32x2* stat = (f32x2*)(smem + K1_STAT);
    const float* csf = (const float*)(smem + K1_CS);
#pragma unroll
    for (int tt = 0; tt < 2; ++tt) {
        const int r = m & 3;
        const float sq = r == 0 ? ssq[tt][0] : r == 1 ? ssq[tt][1] : r == 2 ? ssq[tt][2] : ssq[tt][3];
        const float mean = ssum[tt][0] * (1.0f / D_);
        const float var = fmaxf(sq * (1.0f / D_) - mean * mean, 0.0f);
        if ((m >> 2) == g) stat[64 * wr + 32 * wc + 16 * tt + m] = f32x2{mean, __builtin_amdgcn_rsqf(var + EPS)};
    }
    WAIT_L0();
    BAR();
    BAR();
#pragma unroll
    for (int nt = 0; nt < 3; ++nt) {
        const int ntl = 3 * wc + nt;
        const int col = ct * 96 + ntl * 16;
        const int head = (col & 511) >> 4;
        const bool isv = nt >= 3 - NV;
        const int isk = col >= 512;
        const float sc = (!isv && !isk) ? QSCALE : 1.0f;
        const f32x4 c4 = *(const f32x4*)(csf + ntl * 16 + 4 * g);
        const float c1 = csf[ntl * 16 + m];
        char* base = ws + (isv ? WS_VF : isk ? WS_KF : WS_QF) + head * 1024 +
                     (isv ? lane * 16 : ((lane & 15) + 16 * (lane >> 5) + 32 * ((lane >> 4) & 1)) * 16);
#pragma unroll
        for (int pr = 0; pr < 2; ++pr) {
            unsigned pk[2][2];
#pragma unroll
            for (int u = 0; u < 2; ++u) {
                const int tt = 2 * pr + u;
                const int tok0 = 64 * wr + 16 * (tt ^ (2 * wc));
                float v[4];
                if (!isv) {
                    const f32x2 st = stat[tok0 + m];
                    const float rs = st[1] * sc;
#pragma unroll
                    for (int r = 0; r < 4; ++r) v[r] = (acc[tt][nt][r] - st[0] * c4[r]) * rs;
                } else {
#pragma unroll
                    for (int r = 0; r < 4; ++r) {
                        const f32x2 st = stat[tok0 + 4 * g + r];
                        v[r] = (acc[tt][nt][r] - st[0] * c1) * st[1];
                    }
                }
                pk[u][0] = pack2(v[0], v[1]);
                pk[u][1] = pack2(v[2], v[3]);
            }
            swap32(pk[0][0], pk[1][0]);
            swap32(pk[0][1], pk[1][1]);
            const int tile = rb * 4 + 2 * wr + (pr ^ wc);
            st16(base + (size_t)tile * 32768, u32x4{pk[0][0], pk[0][1], pk[1][0], pk[1][1]});
        }
    }
}

__global__ __launch_bounds__(512, 2) void k1_qkv(const uint16_t* __restrict__ x, const uint16_t* __restrict__ wqkv, const uint16_t* __restrict__ wo,
                                                 char* __restrict__ ws) {
    extern __shared__ __attribute__((aligned(16))) char smem[];
    const int lane = threadIdx.x & 63, wv = __builtin_amdgcn_readfirstlane(threadIdx.x >> 6), w = wv & 3;
    const int xcd = blockIdx.x & 7, idx = blockIdx.x >> 3;
    const int rb = K1_RB(xcd, idx), ct = K1_CT(xcd, idx);
    asm volatile(".Lcode_begin:");
    if (wv >= 4) {
        qkv_loader(x, wqkv, wo, ws, smem, rb, ct, w, lane);
        return;
    }
    if (blockIdx.x < 32 && threadIdx.x == 0) *(unsigned*)(ws + WS_CNT + (size_t)blockIdx.x * 256) = 0u;
    unsigned warm = 0;
    if (idx < 4) {
        uint64_t pc;
        int len;
        int skip;
        asm volatile("s_getpc_b64 %0\n.Lcode_pc:\n\ts_mov_b32 %1, .Lcode_end-.Lcode_begin\n\ts_mov_b32 %2, .Lcode_begin-.Lcode_pc" : "=s"(pc), "=s"(len), "=s"(skip));
        pc += skip;
        const int off = min((idx * 256 + (int)threadIdx.x) * 64, len - 4);
        warm = *(const __attribute__((address_space(1))) unsigned*)(pc + off);
    }
    if (ct >= 11) qkv_body<3>(ws, smem, rb, ct, w, lane);
    else if (ct == 10 && w >= 2) qkv_body<2>(ws, smem, rb, ct, w, lane);
    else qkv_body<0>(ws, smem, rb, ct, w, lane);
    WAIT_V(0);
    asm volatile("" ::"v"(warm));
}

constexpr int K2_VS = 2064;
constexpr int K2_K = 0, K2_V = 24576, K2_Q = K2_V + 12 * K2_VS + 192, K2_JOB = K2_Q + 8192, K2_RED = K2_JOB + 32768;
constexpr int K2_LDS = K2_RED + 8 * 128 * 4;

DEVI f32x16 splat16(float v) {
    f32x16 r;
#pragma unroll
    for (int i = 0; i < 16; ++i) r[i] = v;
    return r;
}
constexpr unsigned long long row_mask(int r) {
    unsigned long long m = 0;
    for (int l = 0; l < 64; ++l)
        if ((r & 3) + 8 * (r >> 2) + 4 * (l >> 5) > (l & 31)) m |= 1ull << l;
    return m;
}
template <int R>
DEVI unsigned sel_row(unsigned no, unsigned yes) { return __builtin_amdgcn_inverse_ballot_w64(row_mask(R)) ? yes : no; }
template <int R>
DEVI void merge_rows(const f32x16& sf, const f32x16& sl, f32x16& mg) {
    if constexpr (R < 16) {
        const float no = sl[R], yes = sf[R];
        mg[R] = __builtin_amdgcn_inverse_ballot_w64(row_mask(R)) ? yes : no;
        merge_rows<R + 1>(sf, sl, mg);
    }
}
DEVI f32x16 attn_merge(const f32x16& sf, const f32x16& sl) {
    f32x16 mg;
    merge_rows<0>(sf, sl, mg);
    return mg;
}
template <int R>
DEVI void split_rows(const f32x16& mg, float mx, unsigned (&pf)[8], unsigned (&pl)[8]) {
    if constexpr (R < 16) {
        const unsigned pk = pack2(__builtin_amdgcn_exp2f(mg[R] - mx), __builtin_amdgcn_exp2f(mg[R + 1] - mx));
        const unsigned mk = sel_row<R>(sel_row<R + 1>(0u, 0xffff0000u), 0xffffffffu);
        pf[R >> 1] = pk & mk;
        pl[R >> 1] = pk & ~mk;
        split_rows<R + 2>(mg, mx, pf, pl);
    }
}
DEVI void attn_split(const f32x16& mg, float mx, unsigned (&pf)[8], unsigned (&pl)[8]) { split_rows<0>(mg, mx, pf, pl); }
DEVI bf16x8 as_frag(const unsigned* p) { return __builtin_bit_cast(bf16x8, u32x4{p[0], p[1], p[2], p[3]}); }

template <bool FULL>
DEVI f32x16 attn_exact(char* smem, bf16x8 q, int koff, int vb0, int vb1, int i, int umin) {
    const float NEG = -__builtin_inff();
    const f32x16 zero = {0};
    const f32x16 sl = mfma32(lds_read16(smem, koff + 8 * 2048), q, zero);
    f32x16 sf = splat16(NEG);
    if (FULL || i >= umin) sf = mfma32(lds_read16(smem, koff), q, zero);
    const f32x16 mg = attn_merge(sf, sl);
    float mx = fmaxf(mg[0], mg[1]);
#pragma unroll
    for (int r = 2; r < 16; r += 2) mx = fmaxf(fmaxf(mx, mg[r]), mg[r + 1]);
#pragma unroll 1
    for (int t = 1; t < 8; ++t) {
        if (FULL || i + t >= umin) {
            const f32x16 s = mfma32(lds_read16(smem, koff + t * 2048), q, zero);
#pragma unroll
            for (int r = 0; r < 16; r += 2) mx = fmaxf(fmaxf(mx, s[r]), s[r + 1]);
        }
    }
    mx = swap32_max(mx);
    const f32x16 negm = splat16(-mx);
    f32x16 o = zero;
    unsigned pf[8], pl[8];
    attn_split(mg, mx, pf, pl);
#pragma unroll
    for (int s = 0; s < 2; ++s) {
        o = mfma32(lds_read16(smem, (s ? vb1 : vb0) + 8 * K2_VS), as_frag(pl + 4 * s), o);
        if (FULL || i >= umin) o = mfma32(lds_read16(smem, s ? vb1 : vb0), as_frag(pf + 4 * s), o);
    }
#pragma unroll 1
    for (int t = 1; t < 8; ++t) {
        if (FULL || i + t >= umin) {
            const f32x16 s = mfma32(lds_read16(smem, koff + t * 2048), q, negm);
            unsigned pk[8];
#pragma unroll
            for (int r = 0; r < 16; r += 2) pk[r >> 1] = pack2(__builtin_amdgcn_exp2f(s[r]), __builtin_amdgcn_exp2f(s[r + 1]));
            o = mfma32(lds_read16(smem, vb0 + t * K2_VS), as_frag(pk), o);
            o = mfma32(lds_read16(smem, vb1 + t * K2_VS), as_frag(pk + 4), o);
        }
    }
    return o;
}

template <bool FULL>
DEVI f32x16 attn_fast(char* smem, bf16x8 q, int koff, int vb0, int vb1, int i, int umin) {
    const float NEG = -__builtin_inff();
    const f32x16 zero = {0};
    auto cinit = [&](int t) { return FULL ? zero : splat16(i + t >= umin ? 0.0f : NEG); };
    bf16x8 kf[2], vf[2][2];
    const bf16x8 kd = lds_read16(smem, koff + 8 * 2048), k0 = lds_read16(smem, koff);
    kf[1] = lds_read16(smem, koff + 2048);
    const bf16x8 vd0 = lds_read16(smem, vb0 + 8 * K2_VS), vz0 = lds_read16(smem, vb0);
    const bf16x8 vd1 = lds_read16(smem, vb1 + 8 * K2_VS), vz1 = lds_read16(smem, vb1);
    const f32x16 sl = mfma32(kd, q, zero);
    const f32x16 sf = mfma32(k0, q, cinit(0));
    f32x16 sc[2];
    sc[1] = mfma32(kf[1], q, cinit(1));
    kf[0] = lds_read16(smem, koff + 2 * 2048);
    vf[1][0] = lds_read16(smem, vb0 + K2_VS);
    vf[1][1] = lds_read16(smem, vb1 + K2_VS);
    SB();
    f32x16 o = zero;
    {
        unsigned pf[8], pl[8];
        attn_split(attn_merge(sf, sl), 0.0f, pf, pl);
        o = mfma32(vd0, as_frag(pl), o);
        o = mfma32(vz0, as_frag(pf), o);
        o = mfma32(vd1, as_frag(pl + 4), o);
        o = mfma32(vz1, as_frag(pf + 4), o);
    }
    SB();
#pragma unroll
    for (int t = 1; t < 8; ++t) {
        const int b = t & 1;
        if (t < 7) {
            sc[b ^ 1] = mfma32(kf[b ^ 1], q, cinit(t + 1));
            if (t < 6) kf[b] = lds_read16(smem, koff + (t + 2) * 2048);
            vf[b ^ 1][0] = lds_read16(smem, vb0 + (t + 1) * K2_VS);
            vf[b ^ 1][1] = lds_read16(smem, vb1 + (t + 1) * K2_VS);
        }
        SB();
        unsigned pk[8];
#pragma unroll
        for (int r = 0; r < 8; r += 2) pk[r >> 1] = pack2(__builtin_amdgcn_exp2f(sc[b][r]), __builtin_amdgcn_exp2f(sc[b][r + 1]));
        const float e8 = __builtin_amdgcn_exp2f(sc[b][8]), e9 = __builtin_amdgcn_exp2f(sc[b][9]);
        SB();
        o = mfma32(vf[b][0], as_frag(pk), o);
        SB();
        pk[4] = pack2(e8, e9);
#pragma unroll
        for (int r = 10; r < 16; r += 2) pk[r >> 1] = pack2(__builtin_amdgcn_exp2f(sc[b][r]), __builtin_amdgcn_exp2f(sc[b][r + 1]));
        SB();
        o = mfma32(vf[b][1], as_frag(pk + 4), o);
        SB();
    }
    return o;
}

template <bool FULL>
DEVI u32x4 attn_task(char* smem, bf16x8 q, int rb, int i, int e, int lane) {
    const int a = lane & 31, h = lane >> 5;
    const int koff = K2_K + e * 1024 + i * 2048 + lane * 16;
    const bool isdata = a < 16;
    const int vb0 = K2_V + i * K2_VS + (isdata ? e * 1024 + h * 256 + a * 16 : 2048);
    const int vb1 = vb0 + (isdata ? 512 : 0);
    const int umin = FULL ? 0 : 8 - 4 * rb;
    f32x16 o = attn_fast<FULL>(smem, q, koff, vb0, vb1, i, umin);
    float mag = 0.f;
#pragma unroll
    for (int r = 0; r < 8; ++r) mag += __builtin_fabsf(o[r]);
    const bool safe = o[8] >= 0x1p-60f && o[8] <= 0x1p60f && mag <= 0x1p120f;
    if (__builtin_amdgcn_ballot_w64(!safe) != 0) {
        o = attn_exact<FULL>(smem, q, koff, vb0, vb1, i, umin);
    }
    const float inv = __builtin_amdgcn_rcpf(o[8]);
    return u32x4{pack2(o[0] * inv, o[1] * inv), pack2(o[2] * inv, o[3] * inv), pack2(o[4] * inv, o[5] * inv), pack2(o[6] * inv, o[7] * inv)};
}

constexpr int G64_RED = 0, G64_T = 49152;
DEVI void oproj_phase(const uint16_t* __restrict__ x, char* __restrict__ ws, char* smem, int tb, int fp, int w, int lane) {
    constexpr int NS = 8;
    const int tt = w & 1, ft = w >> 1;
    const char* wt = ws + WS_WOT + (size_t)fp * 65536 + (size_t)(w * NS) * 2048 + lane * 16;
    bf16x8 fa[NS][2], fb[NS][2];
#pragma unroll
    for (int s = 0; s < NS; ++s)
#pragma unroll
        for (int q = 0; q < 2; ++q) fa[s][q] = *(const bf16x8*)(wt + s * 2048 + (q ^ (w >> 1)) * 1024);
    u32x4 xr[2];
#pragma unroll
    for (int n = 0; n < 2; ++n) {
        const int r = (w * 2 + n) * 8 + (lane >> 3);
        xr[n] = *(const u32x4*)((const char*)x + (size_t)(tb * 64 + r) * 1024 + fp * 128 + (lane & 7) * 16);
    }
    SB();
    BAR();
    SB();
    const __amdgpu_buffer_rsrc_t rs = __builtin_amdgcn_make_buffer_rsrc(ws + WS_OF, 0, 2 << 20, 0x00020000);
#pragma unroll
    for (int s = 0; s < NS; ++s) {
#pragma unroll
        for (int q = 0; q < 2; ++q) fb[s][q] = load_dev(rs, ((tb * 2 + (q ^ (w & 1))) * 32 + w * NS) * 1024 + lane * 16, s * 1024);
        SB();
    }
    f32x16 acc[4];
#pragma unroll
    for (int q = 0; q < 4; ++q) acc[q] = f32x16{0};
#pragma unroll
    for (int s = 0; s < NS; ++s) {
#pragma unroll
        for (int q = 0; q < 4; ++q) acc[q] = mfma32(fa[s][q >> 1], fb[s][q & 1], acc[q]);
        SB();
    }
#pragma unroll
    for (int n = 0; n < 2; ++n) {
        const int r = (w * 2 + n) * 8 + (lane >> 3);
        *(u32x4*)(smem + G64_T + r * 128 + (((lane & 7) ^ ((r >> 1) & 7)) << 4)) = xr[n];
    }
#pragma unroll
    for (int q = 1; q < 4; ++q) {
        char* dst = smem + G64_RED + ((w ^ q) * 3 + q - 1) * 4096 + lane * 16;
#pragma unroll
        for (int r = 0; r < 4; ++r)
            *(f32x4*)(dst + r * 1024) = f32x4{acc[q][4 * r], acc[q][4 * r + 1], acc[q][4 * r + 2], acc[q][4 * r + 3]};
    }
    WAIT_L0();
    BAR();
    f32x16 o = acc[0];
#pragma unroll
    for (int q = 0; q < 3; ++q) {
        const char* src = smem + G64_RED + (w * 3 + q) * 4096 + lane * 16;
#pragma unroll
        for (int r = 0; r < 4; ++r) {
            const f32x4 v = *(const f32x4*)(src + r * 1024);
#pragma unroll
            for (int k = 0; k < 4; ++k) o[4 * r + k] += v[k];
        }
    }
    const int tok = lane & 31, h = lane >> 5;
    const int r = tt * 32 + tok;
    unsigned pk[8];
    float sum = 0.f, sq = 0.f;
#pragma unroll
    for (int qd = 0; qd < 4; ++qd) {
        const u32x2 xv = *(const u32x2*)(smem + G64_T + r * 128 + (((ft * 4 + qd) ^ ((r >> 1) & 7)) << 4) + h * 8);
        pk[2 * qd] = pack2(o[4 * qd] + bf_lo(xv[0]), o[4 * qd + 1] + bf_hi(xv[0]));
        pk[2 * qd + 1] = pack2(o[4 * qd + 2] + bf_lo(xv[1]), o[4 * qd + 3] + bf_hi(xv[1]));
#pragma unroll
        for (int k = 0; k < 2; ++k) {
            const float lo = bf_lo(pk[2 * qd + k]), hi = bf_hi(pk[2 * qd + k]);
            sum += lo + hi;
            sq += lo * lo + hi * hi;
        }
    }
    char* hdst = ws + WS_HF + (size_t)((tb * 2 + tt) * 32 + (fp * 2 + ft) * 2) * 1024 + lane * 16;
    st16(hdst, u32x4{pk[0], pk[1], pk[2], pk[3]});
    st16(hdst + 1024, u32x4{pk[4], pk[5], pk[6], pk[7]});
    sum = swap32_add(sum);
    sq = swap32_add(sq);
    if (h == 0) ((f32x2*)(ws + WS_ST2))[(fp * 2 + ft) * S_ + tb * 64 + r] = f32x2{sum, sq};
}

__global__ __launch_bounds__(512, 2) void k2_attn(const uint16_t* __restrict__ x, const uint16_t* __restrict__ wg, const uint16_t* __restrict__ wu,
                                                  const uint16_t* __restrict__ wd, char* __restrict__ ws) {
    extern __shared__ __attribute__((aligned(16))) char smem[];
    const int tid = threadIdx.x, lane = tid & 63, w = __builtin_amdgcn_readfirstlane(tid >> 6);
    const int xcd = blockIdx.x & 7, idx = blockIdx.x >> 3;
    const int rb = 2 * xcd + (idx >> 4), p = idx & 15;
    const int i = w & 3, e = w >> 2;
#pragma unroll
    for (int n = 0; n < 6; ++n) {
        const int pc = 6 * w + n;
        const int isv = pc >= 24, u = (pc % 24) >> 1, half = pc & 1;
        int kt = 4 * rb - 8 + u;
        kt = kt < 0 ? 0 : kt;
        const char* src = ws + (isv ? WS_VF : WS_KF) + (size_t)(kt * 16 + p) * 2048 + half * 1024 + lane * 16;
        glds16(src, smem, (isv ? K2_V + u * K2_VS : K2_K + u * 2048) + half * 1024);
    }
    glds16(ws + WS_QF + (size_t)((rb * 4 + i) * 32 + 2 * p + e) * 1024 + lane * 16, smem, K2_Q + w * 1024);
    if (tid < 48) *(unsigned*)(smem + K2_V + (tid >> 2) * K2_VS + 2048 + (tid & 3) * 4) = 0x3F803F80u;
    const int bid = blockIdx.x;
    const int job = K2_JOB + w * 4096;
    if (bid < 112) {
        const int cg = bid % 28, s = 8 * (bid / 28) + w;
#pragma unroll
        for (int n = 0; n < 4; ++n) {
            const uint16_t* src = (n >> 1) ? wu : wg;
            const int row = 16 * s + 8 * (n & 1) + (lane >> 3);
            glds16((const char*)src + (size_t)row * (F_ * 2) + cg * 128 + (lane & 7) * 16, smem, job + n * 1024);
        }
    } else if (bid < 224) {
        const int s = bid - 112;
#pragma unroll
        for (int n = 0; n < 2; ++n) {
            const int row = 16 * s + 8 * n + (lane >> 3);
            glds16((const char*)wd + (size_t)row * (D_ * 2) + w * 128 + (lane & 7) * 16, smem, job + n * 1024);
        }
    }
    if (bid < 112) WAIT_V(4);
    else if (bid < 224) WAIT_V(2);
    else WAIT_V(0);
    WAIT_L0();
    BAR();
    const bf16x8 q = lds_read16(smem, K2_Q + w * 1024 + lane * 16);
    auto retile = [&]() {
        const int G = lane >> 4, nh = G & 1, hh = G >> 1, rq = (lane >> 2) & 3, pp = lane & 3;
        if (bid < 112) {
            const int cg = bid % 28, kq = bid / 28, s = 8 * kq + w;
            float* red = (float*)(smem + K2_RED);
#pragma unroll
            for (int t = 0; t < 4; ++t) {
                const int o = job + nh * 2048 + (4 * hh + rq) * 128 + t * 32 + pp * 8;
                const bf16x8 f = lds_read_tr(smem, o, o + 8 * 128);
                const int T = 4 * cg + t, j = T / 7, t7 = T % 7;
                const u32x4 u = __builtin_bit_cast(u32x4, f);
                st16(ws + WS_WGU + (size_t)((j * 32 + s) * 7 + t7) * 1024 + lane * 16, u);
                float sum = 0.f;
#pragma unroll
                for (int r = 0; r < 4; ++r) sum += bf_lo(u[r]) + bf_hi(u[r]);
                sum = swap32_add(sum);
                if (lane < 32) red[w * 128 + nh * 64 + t * 16 + (lane & 15)] = sum;
            }
        } else if (bid < 224) {
            char* dst = ws + WS_WDT + (size_t)(w * 112 + bid - 112) * 2048 + lane * 16;
#pragma unroll
            for (int t = 0; t < 2; ++t) {
                const int o = job + (4 * hh + rq) * 128 + t * 64 + nh * 32 + pp * 8;
                st16(dst + t * 1024, __builtin_bit_cast(u32x4, lds_read_tr(smem, o, o + 8 * 128)));
            }
        }
        SB();
    };
    const u32x4 ov = rb >= 2 ? attn_task<true>(smem, q, rb, i, e, lane) : attn_task<false>(smem, q, rb, i, e, lane);
    WAIT_V0();
    st16(ws + WS_OF + (size_t)((rb * 4 + i) * 32 + 2 * p + e) * 1024 + lane * 16, ov);
    SB();
    retile();
    if (bid < 112) WAIT_V(4);
    else if (bid < 224) WAIT_V(2);
    else WAIT_V(0);
    WAIT_L0();
    BAR();
    if (tid == 0) arrive(counter(ws, 0, rb));
    if (w >= 4) {
        if (bid < 112 && (w == 5 || w == 6)) {
            const int cg = bid % 28, kq = bid / 28, t = tid - 320;
            const float* red = (const float*)(smem + K2_RED);
            float sum = 0.f;
#pragma unroll
            for (int ww = 0; ww < 8; ++ww) sum += red[ww * 128 + t];
            ((float*)(ws + WS_CSP))[(kq * 2 + (t >> 6)) * F_ + cg * 64 + (t & 63)] = sum;
        }
        unsigned warm = 0;
        if (w == 7 && idx < 6) {
            uint64_t pc;
            int len, skip;
            asm volatile("s_getpc_b64 %0\n.Lmlp_pc:\n\ts_mov_b32 %1, .Lcode_end-.Lmlp_begin\n\ts_mov_b32 %2, .Lmlp_begin-.Lmlp_pc" : "=s"(pc), "=s"(len), "=s"(skip));
            pc += skip;
            const int off = min((idx * 64 + lane) * 64, len - 4);
            warm = *(const __attribute__((address_space(1))) unsigned*)(pc + off);
        }
        if (w == 4) await(counter(ws, 0, rb), 16);
        BAR();
        BAR();
        asm volatile("" ::"v"(warm));
        return;
    }
    oproj_phase(x, ws, smem, 4 * xcd + (idx >> 3), idx & 7, w, lane);
    WAIT_V(0);
}

constexpr int K4_SLOT = 28672, K4_RING = 4, K4_CS = K4_SLOT * 5, K4_LDS = K4_CS + 1024, K5_T = 3 * K4_SLOT;

constexpr int K4_BWIN = 8;
constexpr int K5_WIN = 7;
constexpr int wd_first(int c) {
    constexpr int n[8] = {1, 1, 1, 1, 1, 1, 1, 0};
    int t = 0;
    for (int i = 0; i < c; ++i) t += n[i];
    return t;
}
constexpr int wd_extra(int c) { return 2 * (wd_first(c + 1) - wd_first(c)); }
constexpr int wd_pending(int c) {
    int n = c < K4_RING ? 7 * (K4_RING - 1 - c) : 0;
    for (int b = c < K4_RING ? 0 : c - K4_RING + 1; b < c; ++b) {
        const bool ring = b >= 1 && b - 1 + K4_RING < 8 && b - 1 + K4_RING != c;
        n += (ring ? 7 : 0) + wd_extra(b);
    }
    return n;
}
DEVI int wd_slot(int w) { return (w == 0 ? 4 : w - 1) * K4_SLOT; }
template <int HI>
DEVI void down_phase(char* __restrict__ ws, uint16_t* __restrict__ out, char* smem, int tb, int fp, int w, int lane,
                     const bf16x8 (&fr)[14][2]) {
    constexpr int NS = 14, WIN = HI ? K5_WIN : NS;
    const int w8 = w + 4 * HI;
    const __amdgpu_buffer_rsrc_t rs = __builtin_amdgcn_make_buffer_rsrc(ws + WS_MF, 0, 7 << 20, 0x00020000);
    bf16x8 fb[NS][2];
    int vb[2];
#pragma unroll
    for (int q = 0; q < 2; ++q) vb[q] = ((tb * 2 + (q ^ (w & 1))) * 112 + w8 * NS) * 1024 + lane * 16;
    auto load = [&](int s) {
#pragma unroll
        for (int q = 0; q < 2; ++q) fb[s][q] = load_dev(rs, vb[q], s * 1024);
    };
#pragma unroll
    for (int s = 0; s < WIN; ++s) {
        load(s);
        SB();
    }
    bf16x8 fl[2][2];
    auto rdw = [&](int s) {
#pragma unroll
        for (int q = 0; q < 2; ++q) fl[s & 1][q] = lds_read16(smem, wd_slot(w) + s * 2048 + (q ^ (w >> 1)) * 1024 + lane * 16);
    };
    if (!HI) rdw(0);
    const int tt = w & 1, ft = w >> 1;
    f32x16 acc[4];
#pragma unroll
    for (int q = 0; q < 4; ++q) acc[q] = f32x16{0};
    u32x4 hv;
#pragma unroll
    for (int s = 0; s < NS; ++s) {
        if (!HI && s + 1 < NS) rdw(s + 1);
        SB();
#pragma unroll
        for (int q = 0; q < 4; ++q) acc[q] = mfma32(HI ? fr[s][q >> 1] : fl[s & 1][q >> 1], fb[s][q & 1], acc[q]);
        SB();
        if (WIN + 2 * s < NS) load(WIN + 2 * s);
        if (WIN + 2 * s + 1 < NS) load(WIN + 2 * s + 1);
        if (s == (HI ? (NS - WIN) / 2 + 1 : 0)) hv = *(const u32x4*)(ws + WS_HF + (size_t)((tb * 2 + tt) * 32 + (fp * 2 + ft) * 2 + HI) * 1024 + lane * 16);
        SB();
    }
    auto piece = [&](int sw, int hi, int p) { return wd_slot(sw) + (hi * 7 + p) * 2048; };
    if (HI) BAR();
#pragma unroll
    for (int q = 0; q < 4; ++q)
#pragma unroll
        for (int hh = 0; hh < 2; ++hh) {
            if (q == 0 && hh == HI) continue;
            const int id = q * 2 + hh;
            char* dst = smem + piece(w, HI, id - (id > HI ? 1 : 0)) + lane * 16;
#pragma unroll
            for (int r = 0; r < 2; ++r)
                *(f32x4*)(dst + r * 1024) =
                    f32x4{acc[q][8 * hh + 4 * r], acc[q][8 * hh + 4 * r + 1], acc[q][8 * hh + 4 * r + 2], acc[q][8 * hh + 4 * r + 3]};
        }
    if (!HI) BAR();
    WAIT_L0();
    BAR();
    float o[8];
#pragma unroll
    for (int k = 0; k < 8; ++k) o[k] = acc[0][8 * HI + k];
#pragma unroll
    for (int q = 0; q < 4; ++q)
#pragma unroll
        for (int hi = 0; hi < 2; ++hi) {
            if (q == 0 && hi == HI) continue;
            const int id = q * 2 + HI;
            const char* src = smem + piece(w ^ q, hi, id - (id > hi ? 1 : 0)) + lane * 16;
#pragma unroll
            for (int r = 0; r < 2; ++r) {
                const f32x4 v = *(const f32x4*)(src + r * 1024);
#pragma unroll
                for (int k = 0; k < 4; ++k) o[4 * r + k] += v[k];
            }
        }
    const int tok = lane & 31, h = lane >> 5;
    const int r = tt * 32 + tok;
#pragma unroll
    for (int qd = 0; qd < 2; ++qd) {
        const unsigned a = hv[2 * qd], b = hv[2 * qd + 1];
        const u32x2 v = {pack2(o[4 * qd] + bf_lo(a), o[4 * qd + 1] + bf_hi(a)), pack2(o[4 * qd + 2] + bf_lo(b), o[4 * qd + 3] + bf_hi(b))};
        *(u32x2*)(smem + K5_T + r * 128 + (((ft * 4 + 2 * HI + qd) ^ ((r >> 1) & 7)) << 4) + h * 8) = v;
    }
    WAIT_L0();
    BAR();
    const int rr = w8 * 8 + (lane >> 3), cp = lane & 7;
    const u32x4 v = *(const u32x4*)(smem + K5_T + rr * 128 + cp * 16);
    *(u32x4*)((char*)out + (size_t)(tb * 64 + rr) * 1024 + fp * 128 + ((cp ^ ((rr >> 1) & 7)) << 4)) = v;
    WAIT_V(0);
}

__global__ __launch_bounds__(512, 2) void k4_mlp(char* __restrict__ ws, uint16_t* __restrict__ out) {
    extern __shared__ __attribute__((aligned(16))) char smem[];
    const int tid = threadIdx.x, lane = tid & 63, wv = __builtin_amdgcn_readfirstlane(tid >> 6), w = wv & 3;
    const int xcd = blockIdx.x & 7, idx = blockIdx.x >> 3;
    const int rb = K4_RB(xcd, idx), j = K4_J(xcd, idx);
    asm volatile(".Lmlp_begin:");
    const int tile = rb * 4 + w;
    const int tb = 4 * xcd + (idx >> 3), fp = idx & 7;
    if (wv >= 4) {
        const char* asrc = ws + WS_WGU + (size_t)j * (32 * 7168) + w * 7168 + lane * 16;
        auto issue = [&](int c) {
#pragma unroll
            for (int n = 0; n < 7; ++n) glds16(asrc + c * 28672 + n * 1024, smem, (c % K4_RING) * K4_SLOT + w * 7168 + n * 1024);
        };
        const char* wdt = ws + WS_WDT + (size_t)fp * (112 * 2048) + lane * 16;
        bf16x8 fr[14][2];
        auto wd_regs = [&](int s0, int s1) {
#pragma unroll
            for (int s = s0; s < s1; ++s)
#pragma unroll
                for (int q = 0; q < 2; ++q) fr[s][q] = *(const bf16x8*)(wdt + (size_t)((w + 4) * 14 + s) * 2048 + (q ^ (w >> 1)) * 1024);
        };
        auto wd_stage = [&]() {
#pragma unroll
            for (int n = 0; n < 28; ++n) glds16(wdt + (size_t)(w * 14) * 2048 + n * 1024, smem, wd_slot(w) + n * 1024);
        };
#pragma unroll
        for (int c = 0; c < K4_RING; ++c) issue(c);
        static_for<0, 8>([&](auto cc) {
            constexpr int c = decltype(cc)::value;
            if (c >= 5 && w <= c - 5) wait_vm<wd_pending(c) + 28>();
            else wait_vm<wd_pending(c)>();
            BAR();
            if (c >= 1 && c - 1 + K4_RING < 8) issue(c - 1 + K4_RING);
            SB();
            wd_regs(wd_first(c), wd_first(c + 1));
            SB();
            if (c >= 4 && w == c - 4) wd_stage();
        });
        BAR();
        wd_regs(wd_first(8), 14);
        wait_vm<2 * (14 - wd_first(8))>();
        BAR();
        if (w == 0) await(counter(ws, 1, tb >> 1), 16);
        WAIT_V0();
        BAR();
        down_phase<1>(ws, out, smem, tb, fp, w, lane, fr);
        return;
    }
    const int tok = lane & 31, h = lane >> 5;
    const char* bsrc = ws + WS_HF + (size_t)tile * 32768 + lane * 16;
    bf16x8 bq[K4_BWIN];
#pragma unroll
    for (int s = 0; s < K4_BWIN; ++s) bq[s] = *(const bf16x8*)(bsrc + s * 1024);
    SB();
    f32x2 st[8];
#pragma unroll
    for (int n = 0; n < 8; ++n) st[n] = ((const f32x2*)(ws + WS_ST2))[(h * 8 + n) * S_ + tile * 32 + tok];
    {
        const int t = tid < 224 ? tid : 0;
        const int gu = t >= 112, col = j * 112 + (gu ? t - 112 : t);
        float csq[4];
#pragma unroll
        for (int n = 0; n < 4; ++n) csq[n] = ((const float*)(ws + WS_CSP))[(n * 2 + gu) * F_ + col];
        if (tid < 224) ((float*)(smem + K4_CS))[tid] = (csq[0] + csq[1]) + (csq[2] + csq[3]);
    }
    float sum = 0.f, sq = 0.f;
#pragma unroll
    for (int n = 0; n < 8; ++n) {
        sum += st[n][0];
        sq += st[n][1];
    }
    sum = swap32_add(sum);
    sq = swap32_add(sq);
    const float mean = sum * (1.0f / D_);
    const float rstd = __builtin_amdgcn_rsqf(fmaxf(sq * (1.0f / D_) - mean * mean, 0.0f) + EPS);
    f32x16 acc[7];
#pragma unroll
    for (int t = 0; t < 7; ++t) acc[t] = f32x16{0};
    bf16x8 af[2][7];
    auto rdA = [&](int s, int t) {
        af[s & 1][t] = lds_read16(smem, ((s >> 2) % K4_RING) * K4_SLOT + ((s & 3) * 7 + t) * 1024 + lane * 16);
    };
    WAIT_L0();
    BAR();
#pragma unroll
    for (int t = 0; t < 7; ++t) rdA(0, t);
    SB();
#pragma unroll
    for (int s = 0; s < 32; ++s) {
        const int b = s & 1;
        const bool edge = (s & 3) == 3;
#pragma unroll
        for (int t = 0; t < 7; ++t) {
            if (edge && s < 31 && t == 4) {
                WAIT_L0();
                BAR();
                SB();
            }
            acc[t] = mfma32(af[b][t], bq[s % K4_BWIN], acc[t]);
            if (s < 31) {
                if (!edge) {
                    rdA(s + 1, t);
                } else if (t == 4) {
                    rdA(s + 1, 0);
                    rdA(s + 1, 1);
                } else if (t == 5) {
                    rdA(s + 1, 2);
                    rdA(s + 1, 3);
                    rdA(s + 1, 4);
                } else if (t == 6) {
                    rdA(s + 1, 5);
                    rdA(s + 1, 6);
                }
            }
            SB();
        }
        if (s + K4_BWIN < 32) bq[s % K4_BWIN] = *(const bf16x8*)(bsrc + (s + K4_BWIN) * 1024);
        SB();
    }
    const float* cs = (const float*)(smem + K4_CS);
    char* dst = ws + WS_MF + (size_t)(tile * 112 + j * 7) * 1024 + lane * 16;
#pragma unroll
    for (int t = 0; t < 7; ++t) {
        float v[8];
#pragma unroll
        for (int qd = 0; qd < 2; ++qd) {
            const f32x4 cg = *(const f32x4*)(cs + t * 16 + 8 * qd + 4 * h);
            const f32x4 cu = *(const f32x4*)(cs + 112 + t * 16 + 8 * qd + 4 * h);
#pragma unroll
            for (int r = 0; r < 4; ++r) {
                const float gt = (acc[t][4 * qd + r] - mean * cg[r]) * rstd;
                const float up = (acc[t][8 + 4 * qd + r] - mean * cu[r]) * rstd;
                v[4 * qd + r] = fmaxf(gt, 0.0f) * up;
            }
        }
        st16(dst + t * 1024, u32x4{pack2(v[0], v[1]), pack2(v[2], v[3]), pack2(v[4], v[5]), pack2(v[6], v[7])});
    }
    BAR();
    WAIT_V(0);
    BAR();
    if (tid == 0) arrive(counter(ws, 1, rb));
    BAR();
    const bf16x8 none[14][2] = {};
    down_phase<0>(ws, out, smem, tb, fp, w, lane, none);
    asm volatile(".subsection 1\n.Lcode_end:\n.subsection 0");
}

extern "C" void kernel_launch(void* const* d_in, const int* in_sizes, int n_in, void* d_out, int out_size, void* d_ws,
                              size_t ws_size, hipStream_t stream) {
    const uint16_t* x = (const uint16_t*)d_in[0];
    const uint16_t* wqkv = (const uint16_t*)d_in[1];
    const uint16_t* wo = (const uint16_t*)d_in[2];
    const uint16_t* wg = (const uint16_t*)d_in[4];
    const uint16_t* wu = (const uint16_t*)d_in[5];
    const uint16_t* wd = (const uint16_t*)d_in[6];
    char* ws = (char*)d_ws;
    (void)hipFuncSetAttribute((const void*)k1_qkv, hipFuncAttributeMaxDynamicSharedMemorySize, K1_LDS);
    (void)hipFuncSetAttribute((const void*)k2_attn, hipFuncAttributeMaxDynamicSharedMemorySize, K2_LDS);
    (void)hipFuncSetAttribute((const void*)k4_mlp, hipFuncAttributeMaxDynamicSharedMemorySize, K4_LDS);
    k1_qkv<<<256, 512, K1_LDS, stream>>>(x, wqkv, wo, ws);
    k2_attn<<<256, 512, K2_LDS, stream>>>(x, wg, wu, wd, ws);
    k4_mlp<<<256, 512, K4_LDS, stream>>>(ws, (uint16_t*)d_out);
}
```
